```python
import torch

def _softmax(x, axis=-1):
    return torch.softmax(x, dim=axis)

def _gelu(x):
    return torch.nn.functional.gelu(x, approximate='tanh')

def _silu(x):
    return torch.nn.functional.silu(x)

def _sigmoid(x):
    return torch.sigmoid(x)

def _layernorm(x, axis=-1, eps=1e-05):
    m = x.mean(dim=axis, keepdim=True)
    v = x.var(dim=axis, keepdim=True, unbiased=False)
    return (x - m) / torch.sqrt(v + eps)

def _scatter_add(tgt, idx, src, axis=0):
    return tgt.clone().index_add_(axis, idx, src)

def _segment_sum(src, seg, n):
    out = torch.zeros((n,) + src.shape[1:], dtype=src.dtype)
    return out.index_add_(0, seg, src)

def _pad(x, pw):
    flat = tuple((v for pair in reversed(pw) for v in pair))
    return torch.nn.functional.pad(x, flat)
kernel_name = 'gated_mix__spectral_gfnet__memory_aug_recurrent_mem_tr__prompt'

def setup_inputs():

    _w = lambda a: (lambda t: t.to(torch.bfloat16) if t.is_floating_point() else t)(torch.as_tensor(a))

    def _A_setup():
        B, S, D = (1, 2048, 640)
        Df = D // 2 + 1
        return {'x': _w((lambda *s: torch.randn(*s, dtype=torch.bfloat16) * 0.05)(B, S, D)), 'w_r': _w((lambda *s: torch.randn(*s, dtype=torch.bfloat16) * 3.2)(S, Df) * 0.02), 'w_i': _w((lambda *s: torch.randn(*s, dtype=torch.bfloat16) * 3.2)(S, Df) * 0.02)}
    _A_in = _A_setup()

    def _B_setup():
        B, S, Dm = (1, 2048, 640)
        H, Dh = (32, 128)
        M, seg = (8, 32)
        seg = min(seg, S)
        S = S // seg * seg
        return {'x': _w((lambda *s: torch.randn(*s, dtype=torch.bfloat16) * 0.05)(B, S, Dm)), 'mem0': _w((lambda *s: torch.randn(*s, dtype=torch.bfloat16) * 0.4)(B, M, Dm)), 'Wq': _w((lambda *s: torch.randn(*s, dtype=torch.bfloat16) * 6.4)(Dm, H, Dh) * Dm ** (-0.5)), 'Wk': _w((lambda *s: torch.randn(*s, dtype=torch.bfloat16) * 6.4)(Dm, H, Dh) * Dm ** (-0.5)), 'Wv': _w((lambda *s: torch.randn(*s, dtype=torch.bfloat16) * 0.4)(Dm, H, Dh) * Dm ** (-0.5)), 'Wo': _w((lambda *s: torch.randn(*s, dtype=torch.bfloat16) * 0.4)(H, Dh, Dm) * (H * Dh) ** (-0.5)), 'seg_len': seg}
    _B_in = _B_setup()
    return {'A_x': _A_in['x'], 'A_w_r': _A_in['w_r'], 'A_w_i': _A_in['w_i'], 'B_x': _B_in['x'], 'B_mem0': _B_in['mem0'], 'B_Wq': _B_in['Wq'], 'B_Wk': _B_in['Wk'], 'B_Wv': _B_in['Wv'], 'B_Wo': _B_in['Wo'], 'B_seg_len': _B_in['seg_len'], 'gate_w': _w((lambda *s: torch.randn(*s, dtype=torch.bfloat16) * 0.05)(640) * 16.0)}

def reference(A_x, A_w_r, A_w_i, B_x, B_mem0, B_Wq, B_Wk, B_Wv, B_Wo, B_seg_len, gate_w):

    def _A_ref(x, w_r, w_i):
        B, S, D = x.shape
        X = (lambda x, axes=(-2, -1): torch.fft.rfft2(x.float(), dim=axes))(x, axes=(-2, -1))
        W = w_r + 1j * w_i
        X = X * W
        y = (lambda x, s, axes=(-2, -1): torch.fft.irfft2(x, s=s, dim=axes).to(torch.bfloat16))(X, (S, D), axes=(-2, -1))
        return y

    def _B_ref(x, mem0, Wq, Wk, Wv, Wo, seg_len):
        B, S, Dm = x.shape
        H, Dh = (Wq.shape[1], Wq.shape[2])
        M = mem0.shape[1]
        scale = Dh ** (-0.5)
        n_seg = S // seg_len
        L = M + seg_len
        idx = torch.arange(L)
        mask = (idx[:, None] < M) | (idx[:, None] >= idx[None, :])
        mem = mem0
        outs = []
        for s in range(n_seg):
            xs = x[:, s * seg_len:(s + 1) * seg_len, :].clone()
            h = torch.concatenate([mem, xs], axis=1)
            q = torch.einsum('btd,dhe->bhte', h, Wq)
            k = torch.einsum('btd,dhe->bhte', h, Wk)
            v = torch.einsum('btd,dhe->bhte', h, Wv)
            scores = torch.einsum('bhqe,bhke->bhqk', q, k) * scale
            scores = torch.where(mask[None, None], scores, -1000000000.0)
            p = _softmax(scores, axis=-1)
            o = torch.einsum('bhqk,bhke->bhqe', p, v)
            o = torch.einsum('bhte,hed->btd', o, Wo)
            mem = o[:, :M, :].clone()
            outs.append(o[:, M:, :])
        return torch.concatenate(outs, axis=1)
    x = A_x
    g = _sigmoid((x * gate_w).sum(-1, keepdims=True))
    a = _A_ref(x, A_w_r, A_w_i)
    b = _B_ref(x, B_mem0, B_Wq, B_Wk, B_Wv, B_Wo, B_seg_len)
    return g * a + (1 - g) * b
```

```cpp
#include <hip/hip_runtime.h>
#include <hip/hip_bf16.h>
#include <stdint.h>

#define LAS __attribute__((address_space(3)))
typedef __attribute__((ext_vector_type(8))) __bf16 bf16x8;
typedef __attribute__((ext_vector_type(16))) __bf16 bf16x16;
typedef __attribute__((ext_vector_type(4))) short s16x4;
typedef __attribute__((ext_vector_type(4))) float f32x4;
typedef __attribute__((ext_vector_type(4))) unsigned u32x4;
typedef __attribute__((ext_vector_type(2))) unsigned u32x2;
typedef __attribute__((ext_vector_type(8))) unsigned u32x8;
typedef unsigned short u16;

namespace gm {
constexpr int S_ = 2048, D_ = 640, H_ = 32, E_ = 128, NSEG = 64;
constexpr int NA = 32, NB = 80, NREC = NA + NB, NP = 32, NV = 32, NO = 64;
constexpr int R_P = NREC, R_V = R_P + NP, R_O = R_V + NV, R_CAL = R_O + NO;
constexpr int NT = 512;
constexpr size_t MB = 1024 * 1024;
constexpr size_t OFF_HO = 0;
constexpr size_t OFF_CTRL = 4 * MB;
constexpr size_t OFF_XF = 5 * MB;
constexpr size_t OFF_WB = 8 * MB;
constexpr size_t OFF_KXF = 36 * MB;
constexpr size_t OFF_QXF = 52 * MB;
constexpr size_t OFF_UF = 68 * MB;
constexpr size_t OFF_ZF = 148 * MB;
constexpr size_t OFF_KMF = 168 * MB;
constexpr size_t OFF_PXF = 172 * MB;
constexpr size_t OFF_X1T = 178 * MB;
constexpr size_t OFF_WT = 184 * MB;
constexpr size_t OFF_A = 187 * MB;
constexpr size_t OFF_G = 190 * MB;
constexpr size_t OFF_DBG = 240 * MB;
constexpr int SLOT_P = 0, SLOT_MEM = 20480, SLOT_SIGA = 30720, SLOT_SIGB = 30848;
constexpr int C_HALF = 0, C_POL = 1, C_CNT0 = 64, C_CNT1 = 128, C_ABORT = 192;
constexpr int C_PP = 1024, C_VP = C_PP + 32 * 32, C_AP = C_VP + 32 * 32, C_BP = C_AP + 32 * 32, C_ATT = C_BP + 80 * 32, C_OB = C_ATT + 64 * 32, C_END = C_OB + 4 * 32;
constexpr int CTRL_WORDS = 16384;
constexpr unsigned SPIN_CAP = 1u << 18;
}
using namespace gm;

__device__ __forceinline__ unsigned xcc_id() { return (unsigned)__builtin_amdgcn_s_getreg(20 | (3 << 11)) & 0xfu; }
__device__ __forceinline__ unsigned lmap(unsigned L) { return ((L >> 13) << 14) + (L & 8191u); }
__device__ __forceinline__ unsigned pk_bf16(float a, float b) {
    typedef __attribute__((ext_vector_type(2))) __bf16 bf2; typedef __attribute__((ext_vector_type(2))) float f2;
    f2 v = {a, b}; bf2 r = __builtin_convertvector(v, bf2); return __builtin_bit_cast(unsigned, r);
}
__device__ __forceinline__ unsigned f2u(float a) { return __builtin_bit_cast(unsigned, a); }
__device__ __forceinline__ float u2f(unsigned a) { return __builtin_bit_cast(float, a); }
__device__ __forceinline__ float rbf(float a) { return __builtin_bit_cast(float, pk_bf16(a, 0.f) << 16); }
__device__ __forceinline__ unsigned nosent(unsigned w) { return w < 0xFFFFFFFEu ? w : 0xFFFFFFFEu; }
#define BARRIER() do { asm volatile("s_waitcnt lgkmcnt(0)\n\ts_barrier" ::: "memory"); } while (0)
__device__ __forceinline__ bf16x8 lo8(bf16x16 w) { return __builtin_shufflevector(w, w, 0, 1, 2, 3, 4, 5, 6, 7); }
__device__ __forceinline__ bf16x8 hi8(bf16x16 w) { return __builtin_shufflevector(w, w, 8, 9, 10, 11, 12, 13, 14, 15); }
__host__ __device__ __forceinline__ int k2d(int k) { int G = k >> 4, L = k & 15, q = L >> 2, p = L & 3; return 16 * G + (p < 2 ? 2 * q + p : 8 + 2 * q + (p - 2)); }
__host__ __device__ __forceinline__ int frag_k(int gb, int jb) { return jb < 8 ? 8 * gb + jb : 32 + 8 * gb + (jb - 8); }

__device__ __forceinline__ void glds16(const void* gsrc, unsigned lds_dst) {
    unsigned keep;
    asm volatile("s_mov_b32 %0, m0\n\ts_mov_b32 m0, %2\n\ts_nop 0\n\tglobal_load_lds_dwordx4 %1, off\n\ts_mov_b32 m0, %0"
                 : "=&s"(keep) : "v"(gsrc), "s"(lds_dst) : "memory");
}

__global__ void __launch_bounds__(640, 1)
k_prologue(const u16* __restrict__ x, const u16* __restrict__ Wv, const u16* __restrict__ Wo, char* __restrict__ ws) {
    __shared__ __attribute__((aligned(16))) char lds[128 * 176 + 64];
    const int b = blockIdx.x, tid = threadIdx.x, lane = tid & 63;
    const int wv = __builtin_amdgcn_readfirstlane(tid >> 6);
    if (b == 256) {
        unsigned* ctrl = (unsigned*)(ws + OFF_CTRL);
        for (int i = tid; i < CTRL_WORDS; i += 640) ctrl[i] = 0u;
        return;
    }
    {
        u32x4 sv = {~0u, ~0u, ~0u, ~0u};
        u32x4* p = (u32x4*)(ws + OFF_HO + (size_t)b * 16384);
        for (int i = tid; i < 1024; i += 640) if (!((b & 3) == 3 && ((i * 16) & 8191) >= 7168)) p[i] = sv;
    }
    {
        const int id = b * 640 + tid;
        const int l = id & 63, tt = (id >> 6) & 1, hf = (id >> 7) & 1, rest = id >> 8;
        const int seg = rest / 10, kb = rest - seg * 10;
        const int ga = l >> 4, tok = 32 * seg + 16 * tt + (l & 15);
        const int dbase = 64 * kb + 16 * (2 * hf + (ga >> 1)) + 4 * (ga & 1);
        const u32x2 c0 = *(const u32x2*)(x + (size_t)tok * D_ + dbase);
        const u32x2 c1 = *(const u32x2*)(x + (size_t)tok * D_ + dbase + 8);
        u32x4 o = {c0.x, c1.x, c0.y, c1.y};
        *(u32x4*)(ws + OFF_XF + (size_t)id * 16) = o;
    }
    const int h = b >> 3, cg = b & 7;
    for (int i = tid; i < 128 * 10; i += 640) {
        const int e = i / 10, c = i - e * 10;
        *(u32x4*)(lds + e * 176 + c * 16) = *(const u32x4*)(Wo + ((size_t)h * E_ + e) * D_ + cg * 80 + c * 8);
    }
    const int kb = wv;
    bf16x8 af[4][4];
    {
        const int row = lane & 15, g = lane >> 4;
#pragma unroll
        for (int u = 0; u < 4; ++u) {
            const int d = 64 * kb + k2d(frag_k(row >> 2, 4 * u + (row & 3)));
#pragma unroll
            for (int t = 0; t < 4; ++t)
                af[u][t] = *(const bf16x8*)(Wv + ((size_t)d * H_ + h) * E_ + 32 * t + 8 * g);
        }
    }
    __syncthreads();
    const int g = lane >> 4, n = lane & 15, q4 = (lane & 15) >> 2, p4 = lane & 3;
#pragma unroll
    for (int ct = 0; ct < 5; ++ct) {
        f32x4 acc[4];
#pragma unroll
        for (int u = 0; u < 4; ++u) acc[u] = f32x4{0.f, 0.f, 0.f, 0.f};
#pragma unroll
        for (int t = 0; t < 4; ++t) {
            LAS s16x4* a0 = (LAS s16x4*)(lds + (32 * t + 8 * g + q4) * 176 + (16 * ct + 4 * p4) * 2);
            LAS s16x4* a1 = (LAS s16x4*)(lds + (32 * t + 8 * g + 4 + q4) * 176 + (16 * ct + 4 * p4) * 2);
            s16x4 b0 = __builtin_amdgcn_ds_read_tr16_b64_v4i16(a0);
            s16x4 b1 = __builtin_amdgcn_ds_read_tr16_b64_v4i16(a1);
            typedef __attribute__((ext_vector_type(8))) short s16x8;
            s16x8 bb = __builtin_shufflevector(b0, b1, 0, 1, 2, 3, 4, 5, 6, 7);
            bf16x8 bfr = __builtin_bit_cast(bf16x8, bb);
#pragma unroll
            for (int u = 0; u < 4; ++u) acc[u] = __builtin_amdgcn_mfma_f32_16x16x32_bf16(af[u][t], bfr, acc[u], 0, 0, 0);
        }
        const int col = 80 * cg + 16 * ct + n, cs = col >> 3, c8 = col & 7;
        const int w = h >> 2, tl = (h >> 1) & 1, hl = h & 1;
        u32x4 o0 = {pk_bf16(acc[0][0], acc[0][1]), pk_bf16(acc[0][2], acc[0][3]), pk_bf16(acc[1][0], acc[1][1]), pk_bf16(acc[1][2], acc[1][3])};
        u32x4 o1 = {pk_bf16(acc[2][0], acc[2][1]), pk_bf16(acc[2][2], acc[2][3]), pk_bf16(acc[3][0], acc[3][1]), pk_bf16(acc[3][2], acc[3][3])};
        char* dst = ws + OFF_WB + (size_t)cs * 327680 + ((((size_t)w * 2 + tl) * 10 + kb) * 64 + (g * 16 + hl * 8 + c8)) * 32;
        *(u32x4*)dst = o0; *(u32x4*)(dst + 16) = o1;
    }
}

typedef __attribute__((ext_vector_type(2))) float c32;
__device__ __forceinline__ c32 cmul(c32 a, c32 b) { return c32{a.x * b.x - a.y * b.y, a.x * b.y + a.y * b.x}; }
__device__ __forceinline__ c32 cmulc(c32 a, c32 b) { return c32{a.x * b.x + a.y * b.y, a.y * b.x - a.x * b.y}; }
template <int SG> __device__ __forceinline__ c32 rot90(c32 a) { return SG < 0 ? c32{a.y, -a.x} : c32{-a.y, a.x}; }
template <int SG> __device__ __forceinline__ void dft4(c32& a0, c32& a1, c32& a2, c32& a3) {
    c32 t0 = a0 + a2, t1 = a0 - a2, t2 = a1 + a3, t3 = rot90<SG>(a1 - a3);
    a0 = t0 + t2; a1 = t1 + t3; a2 = t0 - t2; a3 = t1 - t3;
}
template <int SG> __device__ __forceinline__ void dft8(c32 (&v)[8]) {
    const float r = 0.70710678118654752f;
    c32 e0 = v[0], e1 = v[2], e2 = v[4], e3 = v[6], o0 = v[1], o1 = v[3], o2 = v[5], o3 = v[7];
    dft4<SG>(e0, e1, e2, e3); dft4<SG>(o0, o1, o2, o3);
    c32 p1 = (SG < 0) ? c32{(o1.x + o1.y) * r, (o1.y - o1.x) * r} : c32{(o1.x - o1.y) * r, (o1.y + o1.x) * r};
    c32 p2 = rot90<SG>(o2);
    c32 p3 = (SG < 0) ? c32{(o3.y - o3.x) * r, -(o3.x + o3.y) * r} : c32{-(o3.x + o3.y) * r, (o3.x - o3.y) * r};
    v[0] = e0 + o0; v[4] = e0 - o0; v[1] = e1 + p1; v[5] = e1 - p1; v[2] = e2 + p2; v[6] = e2 - p2; v[3] = e3 + p3; v[7] = e3 - p3;
}
__device__ __forceinline__ void dft5f(c32& a0, c32& a1, c32& a2, c32& a3, c32& a4) {
    const float c1 = 0.30901699437494742f, c2 = -0.80901699437494742f, s1 = 0.95105651629515357f, s2 = 0.58778525229247313f;
    c32 t1 = a1 + a4, t2 = a2 + a3, t3 = a1 - a4, t4 = a2 - a3;
    c32 m1 = a0 + c1 * t1 + c2 * t2, m2 = a0 + c2 * t1 + c1 * t2;
    c32 n1 = s1 * t3 + s2 * t4, n2 = s2 * t3 - s1 * t4;
    a0 = a0 + t1 + t2;
    a1 = c32{m1.x + n1.y, m1.y - n1.x}; a4 = c32{m1.x - n1.y, m1.y + n1.x};
    a2 = c32{m2.x + n2.y, m2.y - n2.x}; a3 = c32{m2.x - n2.y, m2.y + n2.x};
}
__device__ __forceinline__ void dft10f(c32 (&v)[10]) {
    c32 a[5], b[5];
#pragma unroll
    for (int n2 = 0; n2 < 5; ++n2) { c32 x0 = v[(2 * n2) % 10], x1 = v[(5 + 2 * n2) % 10]; a[n2] = x0 + x1; b[n2] = x0 - x1; }
    dft5f(a[0], a[1], a[2], a[3], a[4]); dft5f(b[0], b[1], b[2], b[3], b[4]);
#pragma unroll
    for (int k2 = 0; k2 < 5; ++k2) { v[(6 * k2) % 10] = a[k2]; v[(5 + 6 * k2) % 10] = b[k2]; }
}
#define LDSWAIT() asm volatile("s_waitcnt lgkmcnt(0)" ::: "memory")
__device__ __forceinline__ void fft640(c32 (&v)[10], c32* buf, const c32* tw, int lane) {
    dft10f(v);
#pragma unroll
    for (int k1 = 0; k1 < 10; ++k1) { c32 t = k1 ? cmul(v[k1], tw[lane * k1]) : v[k1]; buf[k1 * 72 + (lane >> 3) * 9 + (lane & 7)] = t; }
    LDSWAIT();
    c32 w[2][8];
#pragma unroll
    for (int rd = 0; rd < 2; ++rd) {
        const int id = lane + 64 * rd;
        if (id < 80) {
            const int k1 = id >> 3, b = id & 7;
#pragma unroll
            for (int a = 0; a < 8; ++a) w[rd][a] = buf[k1 * 72 + a * 9 + b];
            dft8<-1>(w[rd]);
#pragma unroll
            for (int c = 1; c < 8; ++c) w[rd][c] = cmul(w[rd][c], tw[10 * b * c]);
        }
    }
    LDSWAIT();
#pragma unroll
    for (int rd = 0; rd < 2; ++rd) {
        const int id = lane + 64 * rd;
        if (id < 80) {
            const int k1 = id >> 3, b = id & 7;
#pragma unroll
            for (int c = 0; c < 8; ++c) buf[k1 * 72 + c * 9 + b] = w[rd][c];
        }
    }
    LDSWAIT();
#pragma unroll
    for (int rd = 0; rd < 2; ++rd) {
        const int id = lane + 64 * rd;
        if (id < 80) {
            const int k1 = id >> 3, c = id & 7;
#pragma unroll
            for (int b = 0; b < 8; ++b) w[rd][b] = buf[k1 * 72 + c * 9 + b];
            dft8<-1>(w[rd]);
        }
    }
    LDSWAIT();
#pragma unroll
    for (int rd = 0; rd < 2; ++rd) {
        const int id = lane + 64 * rd;
        if (id < 80) {
            const int k1 = id >> 3, c = id & 7;
#pragma unroll
            for (int d = 0; d < 8; ++d) buf[k1 + 10 * (c + 8 * d)] = w[rd][d];
        }
    }
    LDSWAIT();
}

struct MainArgs {
    const u16* x; const u16* mem0; const u16* Wq; const u16* Wk; const u16* Wv; const u16* Wo; const u16* wr; const u16* wi; const u16* gw; char* ws; u16* out;
};
#define DBC 20
#define DAC 22
#define DBM 30
constexpr int L_XB = 0;
constexpr int L_MF = 81920;
constexpr int L_QF = 92160;
constexpr int L_KM = 96256;
constexpr int L_T = 100352;
constexpr int L_RED = 92160;
constexpr int L_MISC = 116736;
constexpr int L_PROF = 120 * 1024;
constexpr int LDS_BYTES = 132 * 1024;

__device__ __forceinline__ unsigned rt32() { return (unsigned)__builtin_amdgcn_s_memrealtime(); }
__device__ __forceinline__ void wait_rt(unsigned target) {
    if ((int)(target - rt32()) > 2000) return;
    while ((int)(rt32() - target) < 0) {}
}
__device__ __forceinline__ unsigned umax3(unsigned a, unsigned b, unsigned c) { return max(max(a, b), c); }
__device__ __forceinline__ bool spin_abort(unsigned& spins, unsigned* ctrl) {
    ++spins;
    if (spins > SPIN_CAP) { __hip_atomic_store(ctrl + C_ABORT, 1u, __ATOMIC_RELAXED, __HIP_MEMORY_SCOPE_AGENT); return true; }
    if ((spins & 1023u) == 0u) return __builtin_amdgcn_readfirstlane(__hip_atomic_load(ctrl + C_ABORT, __ATOMIC_RELAXED, __HIP_MEMORY_SCOPE_AGENT)) != 0;
    return false;
}
__device__ __forceinline__ unsigned read_sig(const char* slot, unsigned off, unsigned* ctrl) {
    unsigned v, spins = 0;
    for (;;) {
        asm volatile("v_mov_b32 %0, 0\n\tglobal_load_dword %0, %0, %1 sc1\n\ts_waitcnt vmcnt(0)" : "=&v"(v) : "s"(slot + off) : "memory");
        v = __builtin_amdgcn_readfirstlane(v);
        if (v != ~0u) break;
        if (spin_abort(spins, ctrl)) break;
    }
    return v;
}
__device__ __forceinline__ unsigned wait_counters(unsigned* ctrl, int base, int cnt, unsigned need, int lane) {
    unsigned spins = 0, m;
    const unsigned off = (unsigned)(base + (lane < cnt ? lane : 0) * 32) * 4u;
    for (;;) {
        unsigned v;
        asm volatile("global_load_dword %0, %1, %2 sc1\n\ts_waitcnt vmcnt(0)" : "=&v"(v) : "v"(off), "s"(ctrl) : "memory");
#pragma unroll
        for (int o = 32; o >= 1; o >>= 1) v = min(v, (unsigned)__shfl_xor((int)v, o));
        m = __builtin_amdgcn_readfirstlane(v);
        if (m >= need) break;
        __builtin_amdgcn_s_sleep(4);
        if (spin_abort(spins, ctrl)) break;
    }
    return m;
}
__device__ __forceinline__ void set_counter(unsigned* ctrl, int idx, unsigned v) { __hip_atomic_store(ctrl + idx, v, __ATOMIC_RELAXED, __HIP_MEMORY_SCOPE_AGENT); }
__device__ __forceinline__ void st16_sc1(const void* base, unsigned off, u32x4 v) { asm volatile("global_store_dwordx4 %0, %1, %2 sc1\n\ts_nop 1" :: "v"(off), "v"(v), "s"(base) : "memory"); }
__device__ __forceinline__ void st8_sc1(const void* base, unsigned off, u32x2 v) { asm volatile("global_store_dwordx2 %0, %1, %2 sc1\n\ts_nop 1" :: "v"(off), "v"(v), "s"(base) : "memory"); }
__device__ __forceinline__ void st4_sc1(const void* base, unsigned off, unsigned v) { asm volatile("global_store_dword %0, %1, %2 sc1" :: "v"(off), "v"(v), "s"(base) : "memory"); }
__device__ __forceinline__ void load_mem0(const u16* mem0, char* lds, int tid) {
    for (int c = tid; c < 640; c += NT) {
        const int kb = c >> 6, l = c & 63;
        *(u32x4*)(lds + L_MF + c * 16) = *(const u32x4*)(mem0 + (l & 7) * D_ + 64 * kb + 16 * (l >> 4) + 8 * ((l >> 3) & 1));
    }
}
__device__ __forceinline__ void prefetch_x(const char* xf, int seg, int buf, int wv, int lane) {
    const char* src = xf + (size_t)seg * 40960 + wv * 5120 + lane * 16;
    const unsigned dst = L_XB + buf * 40960 + wv * 5120;
#pragma unroll
    for (int i = 0; i < 5; ++i) glds16(src + i * 1024, dst + i * 1024);
}
#define ST2(k) do {} while (0)
#define MFMA32(a, b, c) __builtin_amdgcn_mfma_f32_16x16x32_bf16((a), (b), (c), 0, 0, 0)
#define SMFMAC(a, b, c) __builtin_amdgcn_smfmac_f32_16x16x64_bf16((a), (b), (c), sidx, 0, 0)
typedef __attribute__((ext_vector_type(16))) short s16x16;
typedef __attribute__((ext_vector_type(8))) short s16x8;
__device__ __forceinline__ bf16x16 cat4(s16x4 a, s16x4 b, s16x4 c, s16x4 d) {
    s16x8 x = __builtin_shufflevector(a, b, 0, 1, 2, 3, 4, 5, 6, 7), y = __builtin_shufflevector(c, d, 0, 1, 2, 3, 4, 5, 6, 7);
    return __builtin_bit_cast(bf16x16, (s16x16)__builtin_shufflevector(x, y, 0, 1, 2, 3, 4, 5, 6, 7, 8, 9, 10, 11, 12, 13, 14, 15));
}
__device__ __forceinline__ void load_w2(const u16* Wa, const u16* Wb, int h, char* lds, int tid, int wv, bf16x16 (&wa)[10], bf16x16 (&wb)[10]) {
    const int lane = tid & 63, g = lane >> 4, n = lane & 15;
    u32x4 st[10][4];
    const unsigned wo_ = (unsigned)(((tid >> 4) * H_ + h) * E_ + (tid & 15) * 8) * 2u;
#pragma unroll
    for (int kb = 0; kb < 10; ++kb)
#pragma unroll
        for (int i = 0; i < 4; ++i) {
            const char* W = (const char*)((i & 2) ? Wb : Wa) + (size_t)(64 * kb + 32 * (i & 1)) * (H_ * E_ * 2);
            st[kb][i] = *(const u32x4*)(W + wo_);
        }
    const int q4 = n >> 2, p4 = n & 3;
#pragma unroll
    for (int kb = 0; kb < 10; ++kb) {
        char* sb = lds + (kb & 1) * 33792;
#pragma unroll
        for (int i = 0; i < 4; ++i)
            *(u32x4*)(sb + ((tid >> 4) + 32 * (i & 1)) * 528 + (i >> 1) * 256 + (tid & 15) * 16) = st[kb][i];
        BARRIER();
        s16x4 r[2][4];
#pragma unroll
        for (int tl = 0; tl < 2; ++tl)
#pragma unroll
            for (int u = 0; u < 4; ++u) {
                const int d = k2d(frag_k(g, 4 * u + q4));
                r[tl][u] = __builtin_amdgcn_ds_read_tr16_b64_v4i16((LAS s16x4*)(sb + d * 528 + tl * 256 + (16 * wv + 4 * p4) * 2));
            }
        wa[kb] = cat4(r[0][0], r[0][1], r[0][2], r[0][3]);
        wb[kb] = cat4(r[1][0], r[1][1], r[1][2], r[1][3]);
        asm volatile("" : "+v"(wa[kb]), "+v"(wb[kb]));
    }
    BARRIER();
}
__device__ __forceinline__ void tw_store(char* dst  , f32x4 a0, f32x4 a1) {
    const unsigned p0 = pk_bf16(a0[0], a0[1]), p1 = pk_bf16(a0[2], a0[3]), p2 = pk_bf16(a1[0], a1[1]), p3 = pk_bf16(a1[2], a1[3]);
    *(u16*)(dst + 0) = (u16)p0; *(u16*)(dst + 16) = (u16)(p0 >> 16); *(u16*)(dst + 32) = (u16)p1; *(u16*)(dst + 48) = (u16)(p1 >> 16);
    *(u16*)(dst + 4096) = (u16)p2; *(u16*)(dst + 4096 + 16) = (u16)(p2 >> 16); *(u16*)(dst + 4096 + 32) = (u16)p3; *(u16*)(dst + 4096 + 48) = (u16)(p3 >> 16);
}

__global__ void __launch_bounds__(NT, 2) k_main(MainArgs a) {
    extern __shared__ __attribute__((aligned(16))) char lds[];
    const int tid = threadIdx.x, lane = tid & 63;
    const int wv = __builtin_amdgcn_readfirstlane(tid >> 6);
    unsigned* ctrl = (unsigned*)(a.ws + OFF_CTRL);
    int role;
    {
        unsigned* sm = (unsigned*)(lds + L_MISC);
        if (tid == 0) {
            const unsigned nearh = (xcc_id() >> 2) == 0;
            const unsigned t = __hip_atomic_fetch_add(ctrl + (nearh ? C_CNT0 : C_CNT1), 1u, __ATOMIC_RELAXED, __HIP_MEMORY_SCOPE_AGENT);
            int r;
            if (nearh && t < (unsigned)NREC) r = (int)t;
            else {
                unsigned n0, n1, spins = 0;
                for (;;) {
                    n0 = __hip_atomic_load(ctrl + C_CNT0, __ATOMIC_RELAXED, __HIP_MEMORY_SCOPE_AGENT);
                    n1 = __hip_atomic_load(ctrl + C_CNT1, __ATOMIC_RELAXED, __HIP_MEMORY_SCOPE_AGENT);
                    if (n0 + n1 >= gridDim.x) break;
                    __builtin_amdgcn_s_sleep(2);
                    if (++spins > SPIN_CAP) { __hip_atomic_store(ctrl + C_ABORT, 1u, __ATOMIC_RELAXED, __HIP_MEMORY_SCOPE_AGENT); break; }
                }
                if (n0 >= (unsigned)NREC) r = NREC + (nearh ? (int)(n1 + t - NREC) : (int)t);
                else r = (int)(n0 + t);
            }
            sm[0] = (unsigned)r;
        }
        __syncthreads();
        role = __builtin_amdgcn_readfirstlane((int)sm[0]);
        __syncthreads();
    }
    if (role == R_CAL) {
        if (tid == 0) {
            unsigned best[4] = {~0u, ~0u, ~0u, ~0u};
            for (int rep = 0; rep < 4; ++rep)
#pragma unroll
                for (int t = 0; t < 4; ++t) {
                    unsigned* ad = (unsigned*)(a.ws + OFF_HO + (size_t)(t >> 1) * 2 * MB + 3 * 16384 + (t & 1) * 8192 + 7168);
                    unsigned z = 0, r;
                    unsigned long long c0 = __builtin_amdgcn_s_memtime();
                    asm volatile("s_waitcnt lgkmcnt(0)\n\tglobal_atomic_add %0, %1, %2, off sc0\n\ts_waitcnt vmcnt(0)" : "=v"(r) : "v"(ad), "v"(z) : "memory");
                    unsigned long long c1 = __builtin_amdgcn_s_memtime();
                    unsigned d = (unsigned)(c1 - c0); best[t] = d < best[t] ? d : best[t];
                }
            __hip_atomic_store(ctrl + C_POL, 0x100u | (best[1] < best[0] ? 1u : 0u) | (best[3] < best[2] ? 2u : 0u), __ATOMIC_RELAXED, __HIP_MEMORY_SCOPE_AGENT);
        }
        return;
    }
    auto get_pol = [&]() -> unsigned {
        unsigned v, spins = 0;
        for (;;) {
            v = __builtin_amdgcn_readfirstlane(__hip_atomic_load(ctrl + C_POL, __ATOMIC_RELAXED, __HIP_MEMORY_SCOPE_AGENT));
            if (v & 0x100u) break;
            __builtin_amdgcn_s_sleep(2);
            if (spin_abort(spins, ctrl)) break;
        }
        return v;
    };
    const char* xf = a.ws + OFF_XF;
    char* ho = a.ws + OFF_HO;
    const int g = lane >> 4, n = lane & 15;
    const unsigned mo0 = lmap(SLOT_MEM + tid * 16), mo1 = lmap(SLOT_MEM + 8192 + tid * 4);
    constexpr unsigned so_a = ((SLOT_SIGA >> 13) << 14) + (SLOT_SIGA & 8191), so_b = ((SLOT_SIGB >> 13) << 14) + (SLOT_SIGB & 8191);
    const int sidx = (lane & 8) ? 0xEEEE : 0x4444;
    const unsigned tw_off = (wv >> 1) * 1024 + (2 * (wv & 1) + (n >> 3)) * 256 + (n & 7) * 2;

    if (role < NA) {
        const int h = role;
        bf16x16 wq[10], wk[10];
        load_w2(a.Wq, a.Wk, h, lds, tid, wv, wq, wk);
        load_mem0(a.mem0, lds, tid);
        const unsigned pol = get_pol();
        auto slotp = [&](int s) -> char* { return ho + (size_t)s * 65536 + (((pol >> (s >> 5)) & 1) ? 8192 : 0); };
        unsigned target = 0;
        auto front = [&](int s, unsigned& t_sig, unsigned ap_est) {
            if (s > 0) {
                const char* pslot = slotp(s - 1);
                ST2(0);
                if (s > 1) wait_rt(target);
                ST2(1);
                u32x4 v; unsigned d; unsigned spins = 0;
                for (;;) {
                    asm volatile("global_load_dwordx4 %0, %2, %4 sc1\n\tglobal_load_dword %1, %3, %4 sc1\n\ts_waitcnt vmcnt(0)"
                                 : "=&v"(v), "=&v"(d) : "v"(mo0), "v"(mo1), "s"(pslot) : "memory");
                    if (__all(umax3(umax3(v.x, v.y, v.z), v.w, d) != ~0u)) break;
                    if (spin_abort(spins, ctrl)) break;
                }
                *(u32x4*)(lds + L_MF + tid * 16) = v;
                *(unsigned*)(lds + L_MF + 8192 + tid * 4) = d;
            }
            ST2(2);
            BARRIER();
            ST2(3);
            f32x4 aq = {0.f, 0.f, 0.f, 0.f}, ak = aq;
            bf16x8 mf[10];
#pragma unroll
            for (int kb = 0; kb < 3; ++kb) mf[kb] = *(const bf16x8*)(lds + L_MF + kb * 1024 + lane * 16);
            if (role == 0 && wv == 1) {
                t_sig = rt32();
                if (lane == 0) st4_sc1(slotp(s), so_a, nosent(t_sig + ap_est));
            }
            __builtin_amdgcn_sched_barrier(0);
#pragma unroll
            for (int kb = 0; kb < 10; ++kb) {
                aq = SMFMAC(mf[kb], wq[kb], aq); ak = SMFMAC(mf[kb], wk[kb], ak);
                if (kb + 3 < 10) mf[kb + 3] = *(const bf16x8*)(lds + L_MF + (kb + 3) * 1024 + lane * 16);
                __builtin_amdgcn_sched_barrier(0);
            }
            float cv[4];
#pragma unroll
            for (int r = 0; r < 4; ++r) {
                auto sw = __builtin_amdgcn_permlane32_swap(f2u(aq[r]), f2u(ak[r]), false, false);
                cv[r] = u2f(sw[0]) + u2f(sw[1]);
            }
            char* qd = lds + L_QF + (lane >> 5) * 4096 + tw_off + (g & 1) * 64;
            const unsigned p0 = pk_bf16(cv[0], cv[1]), p1 = pk_bf16(cv[2], cv[3]);
            *(u16*)(qd + 0) = (u16)p0; *(u16*)(qd + 16) = (u16)(p0 >> 16); *(u16*)(qd + 32) = (u16)p1; *(u16*)(qd + 48) = (u16)(p1 >> 16);
        };
        auto next_target = [&](int s) {
            if (s > 0 && s < NSEG - 1) {
                unsigned sa, gb, spins = 0;
                for (;;) {
                    asm volatile("v_mov_b32 %0, 0\n\tglobal_load_dword %1, %0, %3 sc1\n\tglobal_load_dword %0, %0, %2 sc1\n\ts_waitcnt vmcnt(0)"
                                 : "=&v"(sa), "=&v"(gb) : "s"(slotp(s) + so_a), "s"(slotp(s - 1) + so_b) : "memory");
                    sa = __builtin_amdgcn_readfirstlane(sa); gb = __builtin_amdgcn_readfirstlane(gb);
                    if (sa != ~0u && gb != ~0u) break;
                    if (spin_abort(spins, ctrl)) break;
                }
                target = sa + DBC + gb + DAC;
            }
        };
        if (wv == 0) {
            const int wg = h >> 2;
            const unsigned po_x = lmap(SLOT_P + wg * 2560 + ((h >> 1) & 1) * 1024 + (g * 16 + (h & 1) * 8 + (n & 7)) * 16 + (n >> 3) * 8);
            const unsigned po_m = lmap(SLOT_P + wg * 2560 + 2048 + (h & 3) * 128 + (g & 1) * 64 + (n & 7) * 8 + (n >> 3) * 4);
            const unsigned qoff = L_QF + (g * 16 + (n & 7)) * 16;
            const bool hi = n >= 8;
            unsigned pp = 0, dummy = 0;
            u32x4 kx[8];
            auto load_kx = [&](int s) {
                if (pp <= (unsigned)s) pp = wait_counters(ctrl, C_PP + h * 32, 1, s + 1, lane);
                const char* src = a.ws + OFF_KXF + ((size_t)s * H_ + h) * 8192;
                const unsigned o = lane * 16;
                asm volatile("global_load_dwordx4 %0, %8, %9 sc1\n\tglobal_load_dwordx4 %1, %8, %9 offset:1024 sc1\n\tglobal_load_dwordx4 %2, %8, %9 offset:2048 sc1\n\tglobal_load_dwordx4 %3, %8, %9 offset:3072 sc1\n\t"
                             "global_load_dwordx4 %4, %8, %10 sc1\n\tglobal_load_dwordx4 %5, %8, %10 offset:1024 sc1\n\tglobal_load_dwordx4 %6, %8, %10 offset:2048 sc1\n\tglobal_load_dwordx4 %7, %8, %10 offset:3072 sc1"
                             : "=&v"(kx[0]), "=&v"(kx[1]), "=&v"(kx[2]), "=&v"(kx[3]), "=&v"(kx[4]), "=&v"(kx[5]), "=&v"(kx[6]), "=&v"(kx[7])
                             : "v"(o), "s"(src), "s"(src + 4096) : "memory");
            };
            load_kx(0);
            for (int s = 0; s < NSEG; ++s) {
                front(s, dummy, 0);
                asm volatile("s_waitcnt vmcnt(0)" : "+v"(kx[0]), "+v"(kx[1]), "+v"(kx[2]), "+v"(kx[3]), "+v"(kx[4]), "+v"(kx[5]), "+v"(kx[6]), "+v"(kx[7]) :: "memory");
                ST2(4);
                BARRIER();
                ST2(5);
                if (s < NSEG - 1) {
                    char* slot = slotp(s);
                    f32x4 s0 = {0.f, 0.f, 0.f, 0.f}, s1 = s0, s2 = s0;
                    bf16x8 qf[4], km[4];
#pragma unroll
                    for (int t = 0; t < 4; ++t) { qf[t] = *(const bf16x8*)(lds + qoff + t * 1024); km[t] = *(const bf16x8*)(lds + L_KM + t * 1024 + lane * 16); }
#pragma unroll
                    for (int t = 0; t < 4; ++t) {
                        s1 = MFMA32(__builtin_bit_cast(bf16x8, kx[t]), qf[t], s1); s2 = MFMA32(__builtin_bit_cast(bf16x8, kx[4 + t]), qf[t], s2); s0 = MFMA32(km[t], qf[t], s0);
                    }
                    __builtin_amdgcn_sched_group_barrier(0x100, 8, 0);
                    __builtin_amdgcn_sched_group_barrier(0x8, 12, 0);
                    const float cexp = 0.08838834764831845f * 1.4426950408889634f;
                    float v[6];
#pragma unroll
                    for (int r = 0; r < 4; ++r) v[r] = hi ? s2[r] : s1[r];
                    v[4] = hi ? s0[2] : s0[0]; v[5] = hi ? s0[3] : s0[1];
                    if (g >= 2) { v[4] = -INFINITY; v[5] = -INFINITY; }
                    float m = fmaxf(fmaxf(fmaxf(v[0], v[1]), v[2]), fmaxf(fmaxf(v[3], v[4]), v[5]));
                    m = fmaxf(m, u2f(__builtin_amdgcn_update_dpp(0, (int)f2u(m), 0x128, 0xf, 0xf, false)));
                    {
                        auto sw = __builtin_amdgcn_permlane16_swap(f2u(m), f2u(m), false, false);
                        m = fmaxf(u2f(sw[0]), u2f(sw[1]));
                        auto sw2 = __builtin_amdgcn_permlane32_swap(f2u(m), f2u(m), false, false);
                        m = fmaxf(u2f(sw2[0]), u2f(sw2[1]));
                    }
                    const float mc = -m * cexp;
#pragma unroll
                    for (int r = 0; r < 6; ++r) v[r] = __builtin_amdgcn_exp2f(__builtin_fmaf(v[r], cexp, mc));
                    float sum = ((v[0] + v[1]) + (v[2] + v[3])) + (v[4] + v[5]);
                    sum += u2f(__builtin_amdgcn_update_dpp(0, (int)f2u(sum), 0x128, 0xf, 0xf, false));
                    {
                        auto sw = __builtin_amdgcn_permlane16_swap(f2u(sum), f2u(sum), false, false);
                        sum = u2f(sw[0]) + u2f(sw[1]);
                        auto sw2 = __builtin_amdgcn_permlane32_swap(f2u(sum), f2u(sum), false, false);
                        sum = u2f(sw2[0]) + u2f(sw2[1]);
                    }
                    const float inv = __builtin_amdgcn_rcpf(sum);
                    u32x2 px = {nosent(pk_bf16(v[0] * inv, v[1] * inv)), nosent(pk_bf16(v[2] * inv, v[3] * inv))};
                    const unsigned pm = nosent(pk_bf16(v[4] * inv, v[5] * inv));
                    st8_sc1(slot, po_x, px);
                    if (g < 2) st4_sc1(slot, po_m, pm);
                }
                ST2(6);
                asm volatile("s_barrier" ::: "memory");
                if (s + 1 < NSEG - 1) load_kx(s + 1);
                next_target(s);
            }
        } else {
            unsigned ap_est = 70, t_sig = 0;
            for (int s = 0; s < NSEG; ++s) {
                front(s, t_sig, ap_est);
                BARRIER();
                u32x4 kmv;
                const int c = tid - 128;
                if (wv == 2 || wv == 3) kmv = *(const u32x4*)(lds + L_KM + (c >> 5) * 1024 + ((c >> 3) & 3) * 256 + (c & 7) * 16);
                asm volatile("s_waitcnt lgkmcnt(0)\n\ts_barrier" ::: "memory");
                if (role == 0 && wv == 1) ap_est = rt32() - t_sig;
                if (wv == 2 || wv == 3) st16_sc1(a.ws + OFF_KMF + ((size_t)s * H_ + h) * 2048, c * 16, kmv);
                if (wv == 4 && lane == 0 && s > 0) set_counter(ctrl, C_AP + h * 32, s);
                next_target(s);
            }
            asm volatile("s_waitcnt vmcnt(0)" ::: "memory");
        }
        __syncthreads();
        if (tid == 0) set_counter(ctrl, C_AP + h * 32, NSEG);
    } else if (role < NREC) {
        const int cs = role - NA;
        bf16x16 w0[10], w1[10];
        {
            const char* src = a.ws + OFF_WB + (size_t)cs * 327680 + (size_t)wv * 40960 + lane * 32;
#pragma unroll
            for (int kb = 0; kb < 10; ++kb) { w0[kb] = *(const bf16x16*)(src + kb * 2048); w1[kb] = *(const bf16x16*)(src + 20480 + kb * 2048); }
        }
        load_mem0(a.mem0, lds, tid);
#pragma unroll
        for (int kb = 0; kb < 10; ++kb) asm volatile("" : "+v"(w0[kb]), "+v"(w1[kb]));
        const unsigned pol = get_pol();
        auto slotp = [&](int s) -> char* { return ho + (size_t)s * 65536 + (((pol >> (s >> 5)) & 1) ? 8192 : 0); };
        BARRIER();
        const unsigned po0 = lmap(SLOT_P + wv * 2560 + lane * 16), po1 = lmap(SLOT_P + wv * 2560 + 1024 + lane * 16);
        const unsigned po2 = lmap(SLOT_P + wv * 2560 + 2048 + (2 * (g >> 1) + (n >> 3)) * 128 + (g & 1) * 64 + (n & 7) * 8);
        const unsigned mo_pub = lmap(SLOT_MEM + cs * 128 + (lane & 15) * 8);
        auto zcompute = [&](int s) -> u32x2 {
            f32x4 z0 = {0.f, 0.f, 0.f, 0.f}, z1 = z0;
            bf16x8 mf[10];
#pragma unroll
            for (int kb = 0; kb < 3; ++kb) mf[kb] = *(const bf16x8*)(lds + L_MF + kb * 1024 + lane * 16);
            __builtin_amdgcn_sched_barrier(0);
#pragma unroll
            for (int kb = 0; kb < 10; ++kb) {
                z0 = SMFMAC(mf[kb], w0[kb], z0); z1 = SMFMAC(mf[kb], w1[kb], z1);
                if (kb + 3 < 10) mf[kb + 3] = *(const bf16x8*)(lds + L_MF + (kb + 3) * 1024 + lane * 16);
                __builtin_amdgcn_sched_barrier(0);
            }
            float cv[4];
#pragma unroll
            for (int r = 0; r < 4; ++r) {
                auto sw = __builtin_amdgcn_permlane32_swap(f2u(z0[r]), f2u(z1[r]), false, false);
                cv[r] = u2f(sw[0]) + u2f(sw[1]);
            }
            u32x2 z = {pk_bf16(cv[0], cv[1]), pk_bf16(cv[2], cv[3])};
            st8_sc1(a.ws + OFF_ZF + ((size_t)s * NB + cs) * 4096, tid * 8, z);
            return z;
        };
        unsigned vp = 0;
        u32x4 uf0, uf1;
        auto load_u = [&](int s) {
            if (vp <= (unsigned)s) vp = wait_counters(ctrl, C_VP + 4 * wv * 32, 4, s + 1, lane);
            const char* src = a.ws + OFF_UF + ((size_t)s * NB + cs) * 16384 + wv * 2048;
            const unsigned o = lane * 16;
            asm volatile("global_load_dwordx4 %0, %2, %3 sc1\n\tglobal_load_dwordx4 %1, %2, %3 offset:1024 sc1" : "=&v"(uf0), "=&v"(uf1) : "v"(o), "s"(src) : "memory");
        };
        u32x2 zf = zcompute(0);
        load_u(0);
        unsigned gbmin = 1000, sigv = ~0u;
        for (int s = 0; s < NSEG - 1; ++s) {
            char* slot = slotp(s);
            asm volatile("s_waitcnt vmcnt(0)" : "+v"(sigv), "+v"(uf0), "+v"(uf1) :: "memory");
            sigv = __builtin_amdgcn_readfirstlane(sigv);
            if (sigv == ~0u) sigv = read_sig(slot, so_a, ctrl);
            const unsigned target = sigv + DBC;
            ST2(0);
            wait_rt(target);
            ST2(1);
            u32x4 p0, p1; u32x2 p2; unsigned spins = 0;
            for (;;) {
                asm volatile("global_load_dwordx4 %0, %3, %6 sc1\n\tglobal_load_dwordx4 %1, %4, %6 sc1\n\tglobal_load_dwordx2 %2, %5, %6 sc1\n\ts_waitcnt vmcnt(0)"
                             : "=&v"(p0), "=&v"(p1), "=&v"(p2) : "v"(po0), "v"(po1), "v"(po2), "s"(slot) : "memory");
                if (__all(umax3(umax3(umax3(p0.x, p0.y, p0.z), p0.w, p1.x), umax3(p1.y, p1.z, p1.w), max(p2.x, p2.y)) != ~0u)) break;
                if (spin_abort(spins, ctrl)) break;
            }
            ST2(2);
            f32x4 acc = {0.f, 0.f, 0.f, 0.f}, acc2 = acc;
            acc2 = __builtin_amdgcn_mfma_f32_16x16x16bf16_1k(__builtin_bit_cast(s16x4, zf), __builtin_bit_cast(s16x4, p2), acc2, 0, 0, 0);
            acc = MFMA32(__builtin_bit_cast(bf16x8, uf0), __builtin_bit_cast(bf16x8, p0), acc);
            acc = MFMA32(__builtin_bit_cast(bf16x8, uf1), __builtin_bit_cast(bf16x8, p1), acc);
            acc += acc2;
            if ((n >> 3) == (g >> 1)) *(f32x4*)(lds + L_RED + (wv * 2 + (n >> 3)) * 256 + (n & 7) * 32 + (g & 1) * 16) = acc;
            BARRIER();
            ST2(3);
            if (wv == 0) {
                const char* rp = lds + L_RED + g * 1024 + n * 16;
                f32x4 t = (*(const f32x4*)(rp) + *(const f32x4*)(rp + 256)) + (*(const f32x4*)(rp + 512) + *(const f32x4*)(rp + 768));
#pragma unroll
                for (int r = 0; r < 4; ++r) {
                    auto sw = __builtin_amdgcn_permlane16_swap(f2u(t[r]), f2u(t[r]), false, false);
                    float y = u2f(sw[0]) + u2f(sw[1]);
                    auto sw2 = __builtin_amdgcn_permlane32_swap(f2u(y), f2u(y), false, false);
                    t[r] = u2f(sw2[0]) + u2f(sw2[1]);
                }
                u32x2 o = {nosent(pk_bf16(t[0], t[1])), nosent(pk_bf16(t[2], t[3]))};
                if (lane < 16) st8_sc1(slot, mo_pub, o);
                if (cs == 0) {
                    const unsigned gb = rt32() - target;
                    gbmin = gb < gbmin ? gb : gbmin;
                    if (lane == 0) st4_sc1(slot, so_b, gbmin);
                }
            }
            ST2(4);
            asm volatile("s_barrier" ::: "memory");
            const unsigned tpub = rt32();
            if (wv == 1 && lane == 0) set_counter(ctrl, C_BP + cs * 32, s + 1);
            if (s + 1 < NSEG - 1) load_u(s + 1);
            wait_rt(tpub + DBM);
            ST2(5);
            {
                u32x4 v; unsigned d; unsigned sp2 = 0;
                for (;;) {
                    asm volatile("global_load_dwordx4 %0, %2, %4 sc1\n\tglobal_load_dword %1, %3, %4 sc1\n\ts_waitcnt vmcnt(0)"
                                 : "=&v"(v), "=&v"(d) : "v"(mo0), "v"(mo1), "s"(slot) : "memory");
                    if (__all(umax3(umax3(v.x, v.y, v.z), v.w, d) != ~0u)) break;
                    if (spin_abort(sp2, ctrl)) break;
                }
                *(u32x4*)(lds + L_MF + tid * 16) = v;
                *(unsigned*)(lds + L_MF + 8192 + tid * 4) = d;
            }
            ST2(6);
            BARRIER();
            ST2(7);
            if (s + 1 < NSEG - 1) asm volatile("v_mov_b32 %0, 0\n\tglobal_load_dword %0, %0, %1 sc1" : "=&v"(sigv) : "s"(slotp(s + 1) + so_a) : "memory");
            zf = zcompute(s + 1);
        }
        asm volatile("s_waitcnt vmcnt(0)" ::: "memory");
        __syncthreads();
        if (tid == 0) set_counter(ctrl, C_BP + cs * 32, NSEG);
    } else if (role < R_V) {
        const int h = role - R_P;
        bf16x16 wq[10], wk[10];
        load_w2(a.Wq, a.Wk, h, lds, tid, wv, wq, wk);
        prefetch_x(xf, 0, 0, wv, lane);
        for (int s = 0; s < NSEG; ++s) {
            asm volatile("s_waitcnt vmcnt(0)" ::: "memory");
            BARRIER();
            if (s > 0 && tid == 0) set_counter(ctrl, C_PP + h * 32, s);
            if (s + 1 < NSEG) prefetch_x(xf, s + 1, (s + 1) & 1, wv, lane);
            f32x4 q0 = {0.f, 0.f, 0.f, 0.f}, q1 = q0, k0 = q0, k1 = q0;
            const char* xb = lds + L_XB + (s & 1) * 40960 + lane * 16;
#pragma unroll
            for (int kb = 0; kb < 10; ++kb) {
                bf16x8 x00 = *(const bf16x8*)(xb + (kb * 4 + 0) * 1024), x01 = *(const bf16x8*)(xb + (kb * 4 + 1) * 1024);
                bf16x8 x10 = *(const bf16x8*)(xb + (kb * 4 + 2) * 1024), x11 = *(const bf16x8*)(xb + (kb * 4 + 3) * 1024);
                q0 = MFMA32(x00, lo8(wq[kb]), q0); q1 = MFMA32(x01, lo8(wq[kb]), q1); k0 = MFMA32(x00, lo8(wk[kb]), k0); k1 = MFMA32(x01, lo8(wk[kb]), k1);
                q0 = MFMA32(x10, hi8(wq[kb]), q0); q1 = MFMA32(x11, hi8(wq[kb]), q1); k0 = MFMA32(x10, hi8(wk[kb]), k0); k1 = MFMA32(x11, hi8(wk[kb]), k1);
            }
            tw_store(lds + L_T + tw_off + g * 64, q0, q1);
            tw_store(lds + L_T + 8192 + tw_off + g * 64, k0, k1);
            BARRIER();
            const u32x4 vq = *(const u32x4*)(lds + L_T + tid * 16), vk = *(const u32x4*)(lds + L_T + 8192 + tid * 16);
            st16_sc1(a.ws + OFF_KXF + ((size_t)s * H_ + h) * 8192, tid * 16, vk);
            st16_sc1(a.ws + OFF_QXF + ((size_t)s * H_ + h) * 8192, tid * 16, vq);
        }
        asm volatile("s_waitcnt vmcnt(0)" ::: "memory");
        __syncthreads();
        if (tid == 0) set_counter(ctrl, C_PP + h * 32, NSEG);
    } else if (role < R_O) {
        const int h = role - R_V;
        bf16x16 wvv[10];
        bf16x8 wo[5][4];
        {
            u32x4 st[10][2], so[4][5];
            const unsigned wo_ = (unsigned)(((tid >> 4) * H_ + h) * E_ + (tid & 15) * 8) * 2u;
#pragma unroll
            for (int kb = 0; kb < 10; ++kb)
#pragma unroll
                for (int i = 0; i < 2; ++i) st[kb][i] = *(const u32x4*)((const char*)a.Wv + (size_t)(64 * kb + 32 * i) * (H_ * E_ * 2) + wo_);
#pragma unroll
            for (int t = 0; t < 4; ++t)
#pragma unroll
                for (int i = 0; i < 5; ++i) so[t][i] = *(const u32x4*)((const char*)a.Wo + ((size_t)h * E_ + 32 * t) * (D_ * 2) + (size_t)(i * NT + tid) * 16);
            const int q4 = n >> 2, p4 = n & 3;
#pragma unroll
            for (int kb = 0; kb < 10; ++kb) {
                char* sb = lds + (kb & 1) * 17408;
#pragma unroll
                for (int i = 0; i < 2; ++i) *(u32x4*)(sb + ((tid >> 4) + 32 * i) * 272 + (tid & 15) * 16) = st[kb][i];
                BARRIER();
                s16x4 r[4];
#pragma unroll
                for (int u = 0; u < 4; ++u) r[u] = __builtin_amdgcn_ds_read_tr16_b64_v4i16((LAS s16x4*)(sb + k2d(frag_k(g, 4 * u + q4)) * 272 + (16 * wv + 4 * p4) * 2));
                wvv[kb] = cat4(r[0], r[1], r[2], r[3]);
                asm volatile("" : "+v"(wvv[kb]));
            }
            BARRIER();
#pragma unroll
            for (int t = 0; t < 4; ++t) {
                char* sb = lds + (t & 1) * 41472;
#pragma unroll
                for (int i = 0; i < 5; ++i) { const int c = i * NT + tid, e = c / 80, cc = c - e * 80; *(u32x4*)(sb + e * 1296 + cc * 16) = so[t][i]; }
                BARRIER();
#pragma unroll
                for (int ct = 0; ct < 5; ++ct) {
                    s16x4 b0 = __builtin_amdgcn_ds_read_tr16_b64_v4i16((LAS s16x4*)(sb + (8 * g + q4) * 1296 + (16 * (5 * wv + ct) + 4 * p4) * 2));
                    s16x4 b1 = __builtin_amdgcn_ds_read_tr16_b64_v4i16((LAS s16x4*)(sb + (8 * g + 4 + q4) * 1296 + (16 * (5 * wv + ct) + 4 * p4) * 2));
                    wo[ct][t] = __builtin_bit_cast(bf16x8, (s16x8)__builtin_shufflevector(b0, b1, 0, 1, 2, 3, 4, 5, 6, 7));
                    asm volatile("" : "+v"(wo[ct][t]));
                }
            }
            BARRIER();
        }
        prefetch_x(xf, 0, 0, wv, lane);
        const unsigned uo = (unsigned)((10 * wv + (n >> 3)) * 16384 + (h >> 2) * 2048 + ((h >> 1) & 1) * 1024 + (g * 16 + (h & 1) * 8 + (n & 7)) * 16);
        for (int s = 0; s < NSEG; ++s) {
            asm volatile("s_waitcnt vmcnt(0)" ::: "memory");
            BARRIER();
            if (s > 0 && tid == 0) set_counter(ctrl, C_VP + h * 32, s);
            if (s + 1 < NSEG) prefetch_x(xf, s + 1, (s + 1) & 1, wv, lane);
            f32x4 v0 = {0.f, 0.f, 0.f, 0.f}, v1 = v0;
            const char* xb = lds + L_XB + (s & 1) * 40960 + lane * 16;
#pragma unroll
            for (int kb = 0; kb < 10; ++kb) {
                bf16x8 x00 = *(const bf16x8*)(xb + (kb * 4 + 0) * 1024), x01 = *(const bf16x8*)(xb + (kb * 4 + 1) * 1024);
                bf16x8 x10 = *(const bf16x8*)(xb + (kb * 4 + 2) * 1024), x11 = *(const bf16x8*)(xb + (kb * 4 + 3) * 1024);
                v0 = MFMA32(x00, lo8(wvv[kb]), v0); v1 = MFMA32(x01, lo8(wvv[kb]), v1);
                v0 = MFMA32(x10, hi8(wvv[kb]), v0); v1 = MFMA32(x11, hi8(wvv[kb]), v1);
            }
            tw_store(lds + L_T + tw_off + g * 64, v0, v1);
            BARRIER();
            bf16x8 vf[2][4];
#pragma unroll
            for (int tt = 0; tt < 2; ++tt)
#pragma unroll
                for (int t = 0; t < 4; ++t) vf[tt][t] = *(const bf16x8*)(lds + L_T + (tt * 4 + t) * 1024 + lane * 16);
            const char* ub = a.ws + OFF_UF + (size_t)s * NB * 16384;
#pragma unroll
            for (int ct = 0; ct < 5; ++ct) {
                f32x4 u0 = {0.f, 0.f, 0.f, 0.f}, u1 = u0;
#pragma unroll
                for (int t = 0; t < 4; ++t) { u0 = MFMA32(vf[0][t], wo[ct][t], u0); u1 = MFMA32(vf[1][t], wo[ct][t], u1); }
                u32x4 o = {pk_bf16(u0[0], u0[1]), pk_bf16(u0[2], u0[3]), pk_bf16(u1[0], u1[1]), pk_bf16(u1[2], u1[3])};
                st16_sc1(ub, uo + ct * 32768, o);
            }
        }
        asm volatile("s_waitcnt vmcnt(0)" ::: "memory");
        __syncthreads();
        if (tid == 0) set_counter(ctrl, C_VP + h * 32, NSEG);
    } else if (role < R_CAL) {
        const int o = role - R_O;
        const auto rs = __builtin_amdgcn_make_buffer_rsrc(a.ws, 0, (unsigned)(250 * MB), 0x00020000);
        auto obar = [&](int idx) {
            asm volatile("s_waitcnt vmcnt(0)" ::: "memory");
            __syncthreads();
            if (wv == 0) {
                if (lane == 0) __hip_atomic_fetch_add(ctrl + C_OB + idx * 32, 1u, __ATOMIC_RELAXED, __HIP_MEMORY_SCOPE_AGENT);
                wait_counters(ctrl, C_OB + idx * 32, 1, NO, lane);
            }
            __syncthreads();
        };
        auto bf2f = [](unsigned short u) -> float { return u2f((unsigned)u << 16); };
        c32* tw = (c32*)(lds + 96 * 1024);
        c32* wbuf = (c32*)(lds) + wv * 720;
        for (int i = tid; i < 640; i += NT) { float sn, cs_; sincospif(-(float)i / 320.f, &sn, &cs_); tw[i] = c32{cs_, sn}; }
        float gwv[10];
#pragma unroll
        for (int j = 0; j < 10; ++j) gwv[j] = bf2f(a.gw[64 * j + lane]);
        __syncthreads();
        for (int task = o; task < 160; task += NO) {
            if (task < 128) {
                const int r0 = task * 16 + wv * 2;
                c32 v[10]; float ga = 0.f, gb_ = 0.f;
#pragma unroll
                for (int j = 0; j < 10; ++j) {
                    v[j] = c32{bf2f(a.x[(size_t)r0 * D_ + 64 * j + lane]), bf2f(a.x[(size_t)(r0 + 1) * D_ + 64 * j + lane])};
                    ga += rbf(v[j].x * gwv[j]); gb_ += rbf(v[j].y * gwv[j]);
                }
#pragma unroll
                for (int of = 32; of >= 1; of >>= 1) { ga += __shfl_xor(ga, of); gb_ += __shfl_xor(gb_, of); }
                if (lane < 2) {
                    const float z = rbf(lane ? gb_ : ga);
                    const float gv = rbf(1.f / (1.f + __expf(-z)));
                    __builtin_amdgcn_raw_buffer_store_b32(f2u(gv), rs, (unsigned)(OFF_G + (r0 + lane) * 4), 0, 16);
                }
                fft640(v, wbuf, tw, lane);
                __syncthreads();
                for (int i = tid; i < 321 * 16; i += NT) {
                    const int k = i >> 4, t = i & 15;
                    const c32* zb = (const c32*)lds + (t >> 1) * 720;
                    const c32 z1 = zb[k], z2 = zb[k ? 640 - k : 0];
                    c32 r = (t & 1) ? c32{0.5f * (z1.y + z2.y), 0.5f * (z2.x - z1.x)} : c32{0.5f * (z1.x + z2.x), 0.5f * (z1.y - z2.y)};
                    __builtin_amdgcn_raw_buffer_store_b64(__builtin_bit_cast(u32x2, r), rs, (unsigned)(OFF_X1T + ((size_t)k * S_ + task * 16 + t) * 8), 0, 16);
                }
                __syncthreads();
            } else {
                const int p0 = (task - 128) * 64;
                unsigned* tile = (unsigned*)lds;
                for (int r = wv; r < 64; r += 8) {
                    const int pos = p0 + r, m = (pos >> 8) + 8 * ((pos >> 5) & 7) + 64 * ((pos >> 2) & 7) + 512 * (pos & 3);
                    for (int k = lane; k < 321; k += 64) tile[r * 323 + k] = (unsigned)a.wr[(size_t)m * 321 + k] | ((unsigned)a.wi[(size_t)m * 321 + k] << 16);
                }
                __syncthreads();
                for (int i = tid; i < 321 * 64; i += NT) {
                    const int k = i >> 6, r = i & 63;
                    __builtin_amdgcn_raw_buffer_store_b32(tile[r * 323 + k], rs, (unsigned)(OFF_WT + ((size_t)k * S_ + p0 + r) * 4), 0, 16);
                }
                __syncthreads();
            }
        }
        for (int i = tid; i < 2048; i += NT) { float sn, cs_; sincospif(-(float)i / 1024.f, &sn, &cs_); tw[i] = c32{cs_, sn}; }
        obar(0);
        {
            const int j = tid & 255, hf = tid >> 8;
            c32* buf = (c32*)lds + hf * 2304;
            auto pd = [](int p) { return p + ((p >> 5) << 2); };
            for (int b = o; b < 161; b += NO) {
                const int col = min(2 * b + hf, 320);
                const unsigned cb = (unsigned)(OFF_X1T + (size_t)col * S_ * 8);
                c32 v[8];
#pragma unroll
                for (int q = 0; q < 8; ++q) v[q] = __builtin_bit_cast(c32, __builtin_amdgcn_raw_buffer_load_b64(rs, cb + (q * 256 + j) * 8, 0, 16));
                const u32x4 wa = __builtin_amdgcn_raw_buffer_load_b128(rs, (unsigned)(OFF_WT + ((size_t)col * S_ + 8 * j) * 4), 0, 16);
                const u32x4 wb = __builtin_amdgcn_raw_buffer_load_b128(rs, (unsigned)(OFF_WT + ((size_t)col * S_ + 8 * j + 4) * 4), 0, 16);
                dft8<-1>(v);
#pragma unroll
                for (int q = 0; q < 8; ++q) buf[pd(q * 256 + j)] = q ? cmul(v[q], tw[j * q]) : v[q];
                __syncthreads();
                const int b2 = (j >> 5) * 256 + (j & 31), lo = j & 31;
#pragma unroll
                for (int q = 0; q < 8; ++q) v[q] = buf[pd(b2 + q * 32)];
                dft8<-1>(v);
#pragma unroll
                for (int q = 0; q < 8; ++q) buf[pd(b2 + q * 32)] = q ? cmul(v[q], tw[8 * lo * q]) : v[q];
                __syncthreads();
                const int b3 = (j >> 2) * 32 + (j & 3), n0 = j & 3;
#pragma unroll
                for (int q = 0; q < 8; ++q) v[q] = buf[pd(b3 + q * 4)];
                dft8<-1>(v);
#pragma unroll
                for (int q = 0; q < 8; ++q) buf[pd(b3 + q * 4)] = q ? cmul(v[q], tw[64 * n0 * q]) : v[q];
                __syncthreads();
#pragma unroll
                for (int q = 0; q < 8; ++q) v[q] = buf[pd(8 * j + q)];
                dft4<-1>(v[0], v[1], v[2], v[3]); dft4<-1>(v[4], v[5], v[6], v[7]);
                const unsigned wv8[8] = {wa.x, wa.y, wa.z, wa.w, wb.x, wb.y, wb.z, wb.w};
#pragma unroll
                for (int q = 0; q < 8; ++q) v[q] = cmul(v[q], c32{u2f(wv8[q] << 16), u2f(wv8[q] & 0xFFFF0000u)});
                dft4<1>(v[0], v[1], v[2], v[3]); dft4<1>(v[4], v[5], v[6], v[7]);
#pragma unroll
                for (int q = 0; q < 8; ++q) buf[pd(8 * j + q)] = v[q];
                __syncthreads();
#pragma unroll
                for (int q = 0; q < 8; ++q) { c32 t = buf[pd(b3 + q * 4)]; v[q] = q ? cmulc(t, tw[64 * n0 * q]) : t; }
                dft8<1>(v);
#pragma unroll
                for (int q = 0; q < 8; ++q) buf[pd(b3 + q * 4)] = v[q];
                __syncthreads();
#pragma unroll
                for (int q = 0; q < 8; ++q) { c32 t = buf[pd(b2 + q * 32)]; v[q] = q ? cmulc(t, tw[8 * lo * q]) : t; }
                dft8<1>(v);
#pragma unroll
                for (int q = 0; q < 8; ++q) buf[pd(b2 + q * 32)] = v[q];
                __syncthreads();
#pragma unroll
                for (int q = 0; q < 8; ++q) { c32 t = buf[pd(q * 256 + j)]; v[q] = q ? cmulc(t, tw[j * q]) : t; }
                dft8<1>(v);
                if (2 * b + hf <= 320) {
#pragma unroll
                    for (int q = 0; q < 8; ++q) __builtin_amdgcn_raw_buffer_store_b64(__builtin_bit_cast(u32x2, v[q]), rs, cb + (q * 256 + j) * 8, 0, 16);
                }
                __syncthreads();
            }
        }
        for (int i = tid; i < 640; i += NT) { float sn, cs_; sincospif(-(float)i / 320.f, &sn, &cs_); tw[i] = c32{cs_, sn}; }
        obar(1);
        for (int task = o; task < 128; task += NO) {
            for (int i = tid; i < 321 * 8; i += NT) {
                const int k = i >> 3, p = i & 7;
                u32x4 y = __builtin_amdgcn_raw_buffer_load_b128(rs, (unsigned)(OFF_X1T + ((size_t)k * S_ + task * 16 + 2 * p) * 8), 0, 16);
                float ar = u2f(y.x), ai = u2f(y.y), br = u2f(y.z), bi = u2f(y.w);
                if (k == 0 || k == 320) { ai = 0.f; bi = 0.f; }
                c32* zb = (c32*)lds + p * 720;
                zb[k] = c32{ar - bi, -(ai + br)};
                if (k > 0 && k < 320) zb[640 - k] = c32{ar + bi, -(br - ai)};
            }
            __syncthreads();
            c32 v[10];
#pragma unroll
            for (int j = 0; j < 10; ++j) v[j] = wbuf[64 * j + lane];
            LDSWAIT();
            fft640(v, wbuf, tw, lane);
            const float sc = 1.f / (2048.f * 640.f);
            const int r0 = task * 16 + wv * 2;
            for (int i = lane; i < 320; i += 64) {
                const c32 z0 = wbuf[2 * i], z1 = wbuf[2 * i + 1];
                __builtin_amdgcn_raw_buffer_store_b32(pk_bf16(z0.x * sc, z1.x * sc), rs, (unsigned)(OFF_A + ((size_t)r0 * D_ + 2 * i) * 2), 0, 16);
                __builtin_amdgcn_raw_buffer_store_b32(pk_bf16(-z0.y * sc, -z1.y * sc), rs, (unsigned)(OFF_A + ((size_t)(r0 + 1) * D_ + 2 * i) * 2), 0, 16);
            }
            __syncthreads();
        }
        obar(2);
        if (o < 60) {
            const int grp = o / 10, c = o - grp * 10;
            const int cs = 8 * c + wv;
            unsigned pp = 0, ap = 0, vp = 0, bp = 0;
            for (int s = grp; s < NSEG; s += 6) {
                if (vp <= (unsigned)s) vp = wait_counters(ctrl, C_VP, 32, s + 1, lane);
                u32x4 uf[16];
#pragma unroll
                for (int i = 0; i < 16; ++i) uf[i] = __builtin_amdgcn_raw_buffer_load_b128(rs, (unsigned)(OFF_UF + ((size_t)s * NB + cs) * 16384) + i * 1024 + lane * 16, 0, 16);
                if (c < 8) {
                    const int h = 4 * c + (wv >> 1), qt = wv & 1;
                    if (pp <= (unsigned)s) pp = wait_counters(ctrl, C_PP + h * 32, 1, s + 1, lane);
                    const unsigned qb = (unsigned)(OFF_QXF + ((size_t)s * H_ + h) * 8192) + qt * 4096 + lane * 16, kb_ = (unsigned)(OFF_KXF + ((size_t)s * H_ + h) * 8192) + lane * 16;
                    u32x4 qf[4], kx[8], km[4];
#pragma unroll
                    for (int t = 0; t < 4; ++t) qf[t] = __builtin_amdgcn_raw_buffer_load_b128(rs, qb + t * 1024, 0, 16);
#pragma unroll
                    for (int t = 0; t < 8; ++t) kx[t] = __builtin_amdgcn_raw_buffer_load_b128(rs, kb_ + t * 1024, 0, 16);
                    if (ap <= (unsigned)s) ap = wait_counters(ctrl, C_AP + h * 32, 1, s + 1, lane);
#pragma unroll
                    for (int t = 0; t < 4; ++t) km[t] = __builtin_amdgcn_raw_buffer_load_b128(rs, (unsigned)(OFF_KMF + ((size_t)s * H_ + h) * 2048) + t * 512 + g * 128 + (n & 7) * 16, 0, 16);
                    f32x4 s0 = {0.f, 0.f, 0.f, 0.f}, s1 = s0, s2 = s0;
#pragma unroll
                    for (int t = 0; t < 4; ++t) {
                        s1 = MFMA32(__builtin_bit_cast(bf16x8, kx[t]), __builtin_bit_cast(bf16x8, qf[t]), s1);
                        s2 = MFMA32(__builtin_bit_cast(bf16x8, kx[4 + t]), __builtin_bit_cast(bf16x8, qf[t]), s2);
                        s0 = MFMA32(__builtin_bit_cast(bf16x8, km[t]), __builtin_bit_cast(bf16x8, qf[t]), s0);
                    }
                    const float scale = 0.08838834764831845f;
                    const int qi = 16 * qt + n;
                    float v[12];
#pragma unroll
                    for (int r = 0; r < 4; ++r) {
                        v[r] = (g < 2) ? rbf(rbf(s0[r]) * scale) : -INFINITY;
                        v[4 + r] = (4 * g + r <= qi) ? rbf(rbf(s1[r]) * scale) : -999292928.f;
                        v[8 + r] = (16 + 4 * g + r <= qi) ? rbf(rbf(s2[r]) * scale) : -999292928.f;
                    }
                    float m = v[0];
#pragma unroll
                    for (int r = 1; r < 12; ++r) m = fmaxf(m, v[r]);
                    m = fmaxf(m, __shfl_xor(m, 16)); m = fmaxf(m, __shfl_xor(m, 32));
                    float sum = 0.f;
#pragma unroll
                    for (int r = 0; r < 12; ++r) { v[r] = __expf(v[r] - m); sum += v[r]; }
                    sum += __shfl_xor(sum, 16); sum += __shfl_xor(sum, 32);
                    const float inv = 1.f / sum;
                    u32x4 px = {pk_bf16(v[4] * inv, v[5] * inv), pk_bf16(v[6] * inv, v[7] * inv), pk_bf16(v[8] * inv, v[9] * inv), pk_bf16(v[10] * inv, v[11] * inv)};
                    u32x2 pm = {pk_bf16(v[0] * inv, v[1] * inv), pk_bf16(v[2] * inv, v[3] * inv)};
                    const unsigned pb = (unsigned)(OFF_PXF + (size_t)s * 81920) + (2 * qt + (n >> 3)) * 20480 + c * 2560;
                    __builtin_amdgcn_raw_buffer_store_b128(px, rs, pb + ((h >> 1) & 1) * 1024 + (g * 16 + (h & 1) * 8 + (n & 7)) * 16, 0, 16);
                    if (g < 2) __builtin_amdgcn_raw_buffer_store_b64(pm, rs, pb + 2048 + (h & 3) * 128 + g * 64 + (n & 7) * 8, 0, 16);
                    asm volatile("s_waitcnt vmcnt(0)" ::: "memory");
                    __syncthreads();
                    if (tid == 0) __hip_atomic_fetch_add(ctrl + C_ATT + s * 32, 1u, __ATOMIC_RELAXED, __HIP_MEMORY_SCOPE_AGENT);
                }
                if (bp <= (unsigned)s) bp = wait_counters(ctrl, C_BP + cs * 32, 1, s + 1, lane);
                u32x2 zf[8];
#pragma unroll
                for (int i = 0; i < 8; ++i) zf[i] = __builtin_amdgcn_raw_buffer_load_b64(rs, (unsigned)(OFF_ZF + ((size_t)s * NB + cs) * 4096) + i * 512 + lane * 8, 0, 16);
                wait_counters(ctrl, C_ATT + s * 32, 1, 8, lane);
                {
                    u32x4 t[10];
#pragma unroll
                    for (int i = 0; i < 10; ++i) t[i] = __builtin_amdgcn_raw_buffer_load_b128(rs, (unsigned)(OFF_PXF + (size_t)s * 81920) + (i * NT + tid) * 16, 0, 16);
#pragma unroll
                    for (int i = 0; i < 10; ++i) *(u32x4*)(lds + (i * NT + tid) * 16) = t[i];
                }
                __syncthreads();
                float* tile = (float*)(lds + 81920);
                const unsigned pmo = 2048 + (2 * (g >> 1) + (n >> 3)) * 128 + (g & 1) * 64 + (n & 7) * 8;
#pragma unroll
                for (int qo = 0; qo < 4; ++qo) {
                    f32x4 acc = {0.f, 0.f, 0.f, 0.f}, acc2 = acc;
#pragma unroll
                    for (int wg = 0; wg < 8; ++wg) {
                        const char* pbp = lds + qo * 20480 + wg * 2560;
                        const bf16x8 p0 = *(const bf16x8*)(pbp + lane * 16), p1 = *(const bf16x8*)(pbp + 1024 + lane * 16);
                        const s16x4 p2 = *(const s16x4*)(pbp + pmo);
                        acc = MFMA32(__builtin_bit_cast(bf16x8, uf[2 * wg]), p0, acc);
                        acc = MFMA32(__builtin_bit_cast(bf16x8, uf[2 * wg + 1]), p1, acc);
                        acc2 = __builtin_amdgcn_mfma_f32_16x16x16bf16_1k(__builtin_bit_cast(s16x4, zf[wg]), p2, acc2, 0, 0, 0);
                    }
                    acc += acc2;
                    f32x4 tot;
#pragma unroll
                    for (int r = 0; r < 4; ++r) tot[r] = acc[r] + __shfl(acc[r], (lane + 40) & 63);
                    if (n < 8 && g < 2) *(f32x4*)(tile + (8 * qo + n) * 68 + wv * 8 + 4 * g) = tot;
                }
                __syncthreads();
                {
                    const int t = tid >> 4, c4 = (tid & 15) * 4, row = 32 * s + t;
                    const f32x4 bv = *(const f32x4*)(tile + t * 68 + c4);
                    const u32x2 av = __builtin_amdgcn_raw_buffer_load_b64(rs, (unsigned)(OFF_A + ((size_t)row * D_ + 64 * c + c4) * 2), 0, 16);
                    const float gv = u2f(__builtin_amdgcn_raw_buffer_load_b32(rs, (unsigned)(OFF_G + row * 4), 0, 16));
                    const float og = rbf(1.f - gv);
                    const float a0 = u2f(av.x << 16), a1 = u2f(av.x & 0xFFFF0000u), a2 = u2f(av.y << 16), a3 = u2f(av.y & 0xFFFF0000u);
                    u32x2 ov = {pk_bf16(rbf(gv * a0) + rbf(og * rbf(bv[0])), rbf(gv * a1) + rbf(og * rbf(bv[1]))),
                                pk_bf16(rbf(gv * a2) + rbf(og * rbf(bv[2])), rbf(gv * a3) + rbf(og * rbf(bv[3])))};
                    *(u32x2*)(a.out + (size_t)row * D_ + 64 * c + c4) = ov;
                }
                __syncthreads();
            }
        }
    }
}

extern "C" void kernel_launch(void* const* d_in, const int* in_sizes, int n_in, void* d_out, int out_size, void* d_ws, size_t ws_size, hipStream_t stream) {
    char* ws = (char*)(((uintptr_t)d_ws + 2 * MB - 1) & ~(uintptr_t)(2 * MB - 1));
    const u16* x = (const u16*)d_in[0];
    k_prologue<<<257, 640, 0, stream>>>(x, (const u16*)d_in[7], (const u16*)d_in[8], ws);
    MainArgs a;
    a.x = x; a.mem0 = (const u16*)d_in[4]; a.Wq = (const u16*)d_in[5]; a.Wk = (const u16*)d_in[6]; a.Wv = (const u16*)d_in[7]; a.Wo = (const u16*)d_in[8]; a.wr = (const u16*)d_in[1]; a.wi = (const u16*)d_in[2]; a.gw = (const u16*)d_in[10]; a.ws = ws; a.out = (u16*)d_out;
    (void)hipFuncSetAttribute((const void*)k_main, hipFuncAttributeMaxDynamicSharedMemorySize, LDS_BYTES);
    k_main<<<256, NT, LDS_BYTES, stream>>>(a);
}
```

```cpp
#include <hip/hip_runtime.h>
#include <hip/hip_bf16.h>
#include <stdint.h>

#define LAS __attribute__((address_space(3)))
typedef __attribute__((ext_vector_type(8))) __bf16 bf16x8;
typedef __attribute__((ext_vector_type(16))) __bf16 bf16x16;
typedef __attribute__((ext_vector_type(4))) short s16x4;
typedef __attribute__((ext_vector_type(8))) short s16x8;
typedef __attribute__((ext_vector_type(16))) short s16x16;
typedef __attribute__((ext_vector_type(4))) float f32x4;
typedef __attribute__((ext_vector_type(4))) unsigned u32x4;
typedef __attribute__((ext_vector_type(2))) unsigned u32x2;
typedef unsigned short u16;
typedef __amdgpu_buffer_rsrc_t rsrc_t;

namespace gm {
constexpr int S_ = 2048, D_ = 640, H_ = 32, E_ = 128, NSEG = 64;
constexpr int NA = 32, NM = 32, NB = 40, NREC = NA + NM + NB, NP = 32, NV = 32, NO = 64;
constexpr int R_M = NA, R_B = NA + NM, R_P = NREC, R_V = R_P + NP, R_O = R_V + NV, R_S = R_O + NO;
constexpr int NS = 256 - R_S;
constexpr int NT = 512;
constexpr size_t MB = 1024 * 1024;
constexpr size_t OFF_HO = 0;
constexpr size_t OFF_CTRL = 4 * MB;
constexpr size_t OFF_XF = 5 * MB;
constexpr size_t OFF_KXF = 8 * MB;
constexpr size_t OFF_QXF = 24 * MB;
constexpr size_t OFF_UF = 40 * MB;
constexpr size_t OFF_ZF = 120 * MB;
constexpr size_t OFF_KMF = 140 * MB;
constexpr size_t OFF_PXF = 144 * MB;
constexpr size_t OFF_X1T = 150 * MB;
constexpr size_t OFF_WT = 156 * MB;
constexpr size_t OFF_A = 159 * MB;
constexpr size_t OFF_G = 162 * MB;
constexpr size_t OFF_DBG = 200 * MB;
constexpr int SLOT_P = 0, SLOT_MEM = 20480, SLOT_SIGA = 30720, SLOT_SIGB = 30848;
constexpr int C_POL = 0, C_CNT0 = 64, C_CNT1 = 128, C_ABORT = 192;
constexpr int C_PP = 256, C_VP = C_PP + 32 * 32, C_AP = C_VP + 32 * 32, C_ZP = C_AP + 32 * 32, C_ATT = C_ZP + 40 * 32, C_OB = C_ATT + 64 * 32,
              C_XF = C_OB + 4 * 32, C_FILL = C_XF + 32, C_END = C_FILL + 32;
constexpr int CTRL_WORDS = 6912;
static_assert(C_END <= CTRL_WORDS, "ctrl");
constexpr unsigned SPIN_CAP = 1u << 20;
}
using namespace gm;

__device__ __forceinline__ unsigned xcc_id() { return (unsigned)__builtin_amdgcn_s_getreg(20 | (3 << 11)) & 0xfu; }
__host__ __device__ constexpr unsigned lmap(unsigned L) { return ((L >> 13) << 14) + (L & 8191u); }
__device__ __forceinline__ unsigned pk_bf16(float a, float b) {
    typedef __attribute__((ext_vector_type(2))) __bf16 bf2; typedef __attribute__((ext_vector_type(2))) float f2;
    f2 v = {a, b}; bf2 r = __builtin_convertvector(v, bf2); return __builtin_bit_cast(unsigned, r);
}
__device__ __forceinline__ unsigned f2u(float a) { return __builtin_bit_cast(unsigned, a); }
__device__ __forceinline__ float u2f(unsigned a) { return __builtin_bit_cast(float, a); }
__device__ __forceinline__ float rbf(float a) { return __builtin_bit_cast(float, pk_bf16(a, 0.f) << 16); }
__device__ __forceinline__ unsigned nosent(unsigned w) { return w < 0xFFFFFFFEu ? w : 0xFFFFFFFEu; }
#define BARRIER() do { asm volatile("s_waitcnt lgkmcnt(0)\n\ts_barrier" ::: "memory"); } while (0)
__device__ __forceinline__ bf16x8 lo8(bf16x16 w) { return __builtin_shufflevector(w, w, 0, 1, 2, 3, 4, 5, 6, 7); }
__device__ __forceinline__ bf16x8 hi8(bf16x16 w) { return __builtin_shufflevector(w, w, 8, 9, 10, 11, 12, 13, 14, 15); }
__host__ __device__ __forceinline__ int k2d(int k) { int G = k >> 4, L = k & 15, q = L >> 2, p = L & 3; return 16 * G + (p < 2 ? 2 * q + p : 8 + 2 * q + (p - 2)); }
__host__ __device__ __forceinline__ int frag_k(int gb, int jb) { return jb < 8 ? 8 * gb + jb : 32 + 8 * gb + (jb - 8); }
__device__ __forceinline__ void glds16(const void* gsrc, unsigned lds_dst) {
    unsigned keep;
    asm volatile("s_mov_b32 %0, m0\n\ts_mov_b32 m0, %2\n\ts_nop 0\n\tglobal_load_lds_dwordx4 %1, off sc1\n\ts_mov_b32 m0, %0"
                 : "=&s"(keep) : "v"(gsrc), "s"(lds_dst) : "memory");
}
typedef __attribute__((ext_vector_type(2))) float c32;
__device__ __forceinline__ c32 cmul(c32 a, c32 b) { return c32{a.x * b.x - a.y * b.y, a.x * b.y + a.y * b.x}; }
__device__ __forceinline__ c32 cmulc(c32 a, c32 b) { return c32{a.x * b.x + a.y * b.y, a.y * b.x - a.x * b.y}; }
template <int SG> __device__ __forceinline__ c32 rot90(c32 a) { return SG < 0 ? c32{a.y, -a.x} : c32{-a.y, a.x}; }
template <int SG> __device__ __forceinline__ void dft4(c32& a0, c32& a1, c32& a2, c32& a3) {
    c32 t0 = a0 + a2, t1 = a0 - a2, t2 = a1 + a3, t3 = rot90<SG>(a1 - a3);
    a0 = t0 + t2; a1 = t1 + t3; a2 = t0 - t2; a3 = t1 - t3;
}
template <int SG> __device__ __forceinline__ void dft8(c32 (&v)[8]) {
    const float r = 0.70710678118654752f;
    c32 e0 = v[0], e1 = v[2], e2 = v[4], e3 = v[6], o0 = v[1], o1 = v[3], o2 = v[5], o3 = v[7];
    dft4<SG>(e0, e1, e2, e3); dft4<SG>(o0, o1, o2, o3);
    c32 p1 = (SG < 0) ? c32{(o1.x + o1.y) * r, (o1.y - o1.x) * r} : c32{(o1.x - o1.y) * r, (o1.y + o1.x) * r};
    c32 p2 = rot90<SG>(o2);
    c32 p3 = (SG < 0) ? c32{(o3.y - o3.x) * r, -(o3.x + o3.y) * r} : c32{-(o3.x + o3.y) * r, (o3.x - o3.y) * r};
    v[0] = e0 + o0; v[4] = e0 - o0; v[1] = e1 + p1; v[5] = e1 - p1; v[2] = e2 + p2; v[6] = e2 - p2; v[3] = e3 + p3; v[7] = e3 - p3;
}
__device__ __forceinline__ void dft5f(c32& a0, c32& a1, c32& a2, c32& a3, c32& a4) {
    const float c1 = 0.30901699437494742f, c2 = -0.80901699437494742f, s1 = 0.95105651629515357f, s2 = 0.58778525229247313f;
    c32 t1 = a1 + a4, t2 = a2 + a3, t3 = a1 - a4, t4 = a2 - a3;
    c32 m1 = a0 + c1 * t1 + c2 * t2, m2 = a0 + c2 * t1 + c1 * t2;
    c32 n1 = s1 * t3 + s2 * t4, n2 = s2 * t3 - s1 * t4;
    a0 = a0 + t1 + t2;
    a1 = c32{m1.x + n1.y, m1.y - n1.x}; a4 = c32{m1.x - n1.y, m1.y + n1.x};
    a2 = c32{m2.x + n2.y, m2.y - n2.x}; a3 = c32{m2.x - n2.y, m2.y + n2.x};
}
__device__ __forceinline__ void dft10f(c32 (&v)[10]) {
    c32 a[5], b[5];
#pragma unroll
    for (int n2 = 0; n2 < 5; ++n2) { c32 x0 = v[(2 * n2) % 10], x1 = v[(5 + 2 * n2) % 10]; a[n2] = x0 + x1; b[n2] = x0 - x1; }
    dft5f(a[0], a[1], a[2], a[3], a[4]); dft5f(b[0], b[1], b[2], b[3], b[4]);
#pragma unroll
    for (int k2 = 0; k2 < 5; ++k2) { v[(6 * k2) % 10] = a[k2]; v[(5 + 6 * k2) % 10] = b[k2]; }
}
#define LDSWAIT() asm volatile("s_waitcnt lgkmcnt(0)" ::: "memory")
__device__ __forceinline__ void fft640(c32 (&v)[10], c32* buf, const c32* tw, int lane) {
    dft10f(v);
#pragma unroll
    for (int k1 = 0; k1 < 10; ++k1) { c32 t = k1 ? cmul(v[k1], tw[lane * k1]) : v[k1]; buf[k1 * 72 + (lane >> 3) * 9 + (lane & 7)] = t; }
    LDSWAIT();
    c32 w[2][8];
#pragma unroll
    for (int rd = 0; rd < 2; ++rd) {
        const int id = lane + 64 * rd;
        if (id < 80) {
            const int k1 = id >> 3, b = id & 7;
#pragma unroll
            for (int a = 0; a < 8; ++a) w[rd][a] = buf[k1 * 72 + a * 9 + b];
            dft8<-1>(w[rd]);
#pragma unroll
            for (int c = 1; c < 8; ++c) w[rd][c] = cmul(w[rd][c], tw[10 * b * c]);
        }
    }
    LDSWAIT();
#pragma unroll
    for (int rd = 0; rd < 2; ++rd) {
        const int id = lane + 64 * rd;
        if (id < 80) {
            const int k1 = id >> 3, b = id & 7;
#pragma unroll
            for (int c = 0; c < 8; ++c) buf[k1 * 72 + c * 9 + b] = w[rd][c];
        }
    }
    LDSWAIT();
#pragma unroll
    for (int rd = 0; rd < 2; ++rd) {
        const int id = lane + 64 * rd;
        if (id < 80) {
            const int k1 = id >> 3, c = id & 7;
#pragma unroll
            for (int b = 0; b < 8; ++b) w[rd][b] = buf[k1 * 72 + c * 9 + b];
            dft8<-1>(w[rd]);
        }
    }
    LDSWAIT();
#pragma unroll
    for (int rd = 0; rd < 2; ++rd) {
        const int id = lane + 64 * rd;
        if (id < 80) {
            const int k1 = id >> 3, c = id & 7;
#pragma unroll
            for (int d = 0; d < 8; ++d) buf[k1 + 10 * (c + 8 * d)] = w[rd][d];
        }
    }
    LDSWAIT();
}

struct MainArgs {
    const u16* x; const u16* mem0; const u16* Wq; const u16* Wk; const u16* Wv; const u16* Wo; const u16* wr; const u16* wi; const u16* gw; char* ws; u16* out;
};
#define DBC 20
#define DAC 22
#define ZLEAD 15
constexpr int L_XB = 0;
constexpr int L_MF = 81920;
constexpr int L_QF = 92160;
constexpr int L_KM = 96256;
constexpr int L_KX = 100352;
constexpr int L_T = 122880;
constexpr int L_RED = 0;
constexpr int L_MISC = 139264;
constexpr int L_PROF = 140 * 1024;
constexpr int LDS_BYTES = 152 * 1024;

__device__ __forceinline__ int head_of(int idx) { return (idx & 3) * 8 + (idx >> 2); }
__device__ __forceinline__ unsigned rt32() { return (unsigned)__builtin_amdgcn_s_memrealtime(); }
__device__ __forceinline__ void wait_rt(unsigned target) {
    if ((int)(target - rt32()) > 2000) return;
    while ((int)(rt32() - target) < 0) {}
}
__device__ __forceinline__ float vmax(float a, float b) { float r; asm("v_max_f32 %0, %1, %2" : "=v"(r) : "v"(a), "v"(b)); return r; }
__device__ __forceinline__ float vmax3(float a, float b, float c) { float r; asm("v_max3_f32 %0, %1, %2, %3" : "=v"(r) : "v"(a), "v"(b), "v"(c)); return r; }
__device__ __forceinline__ unsigned umax3(unsigned a, unsigned b, unsigned c) { return max(max(a, b), c); }
__device__ __forceinline__ bool spin_abort(unsigned& spins, unsigned* ctrl) {
    ++spins;
    if (spins > SPIN_CAP) { __hip_atomic_store(ctrl + C_ABORT, 1u, __ATOMIC_RELAXED, __HIP_MEMORY_SCOPE_AGENT); return true; }
    if ((spins & 1023u) == 0u) return __builtin_amdgcn_readfirstlane(__hip_atomic_load(ctrl + C_ABORT, __ATOMIC_RELAXED, __HIP_MEMORY_SCOPE_AGENT)) != 0;
    return false;
}
__device__ __forceinline__ unsigned read_sig(const char* p, unsigned* ctrl) {
    unsigned v, spins = 0;
    for (;;) {
        asm volatile("v_mov_b32 %0, 0\n\tglobal_load_dword %0, %0, %1 sc1\n\ts_waitcnt vmcnt(0)" : "=&v"(v) : "s"(p) : "memory");
        v = __builtin_amdgcn_readfirstlane(v);
        if (v != ~0u) break;
        if (spin_abort(spins, ctrl)) break;
    }
    return v;
}
__device__ __forceinline__ unsigned wait_counters(unsigned* ctrl, int base, int cnt, unsigned need, int lane) {
    unsigned spins = 0, m;
    const unsigned off = (unsigned)(base + (lane < cnt ? lane : 0) * 32) * 4u;
    for (;;) {
        unsigned v;
        asm volatile("global_load_dword %0, %1, %2 sc1\n\ts_waitcnt vmcnt(0)" : "=&v"(v) : "v"(off), "s"(ctrl) : "memory");
        if (cnt > 1) {
#pragma unroll
            for (int o = 16; o >= 1; o >>= 1) v = min(v, (unsigned)__shfl_xor((int)v, o));
        }
        m = __builtin_amdgcn_readfirstlane(v);
        if (m >= need) break;
        __builtin_amdgcn_s_sleep(4);
        if (spin_abort(spins, ctrl)) break;
    }
    return m;
}
__device__ __forceinline__ void set_counter(unsigned* ctrl, int idx, unsigned v) { __hip_atomic_store(ctrl + idx, v, __ATOMIC_RELAXED, __HIP_MEMORY_SCOPE_AGENT); }
__device__ __forceinline__ void st16_sc1(const void* base, unsigned off, u32x4 v) { asm volatile("global_store_dwordx4 %0, %1, %2 sc1\n\ts_nop 1" :: "v"(off), "v"(v), "s"(base) : "memory"); }
__device__ __forceinline__ void st8_sc1(const void* base, unsigned off, u32x2 v) { asm volatile("global_store_dwordx2 %0, %1, %2 sc1\n\ts_nop 1" :: "v"(off), "v"(v), "s"(base) : "memory"); }
__device__ __forceinline__ void st4_sc1(const void* base, unsigned off, unsigned v) { asm volatile("global_store_dword %0, %1, %2 sc1" :: "v"(off), "v"(v), "s"(base) : "memory"); }
__device__ __forceinline__ void load_mem0(const u16* mem0, char* lds, int tid) {
    for (int c = tid; c < 640; c += NT) {
        const int kb = c >> 6, l = c & 63;
        *(u32x4*)(lds + L_MF + c * 16) = *(const u32x4*)(mem0 + (l & 7) * D_ + 64 * kb + 16 * (l >> 4) + 8 * ((l >> 3) & 1));
    }
}
__device__ __forceinline__ void prefetch_x(const char* xf, int seg, int buf, int wv, int lane) {
    const char* src = xf + (size_t)seg * 40960 + wv * 5120 + lane * 16;
    const unsigned dst = L_XB + buf * 40960 + wv * 5120;
#pragma unroll
    for (int i = 0; i < 5; ++i) glds16(src + i * 1024, dst + i * 1024);
}
#define ST2(k) do {} while (0)
#define ST2F() do {} while (0)
#define MFMA32(a, b, c) __builtin_amdgcn_mfma_f32_16x16x32_bf16((a), (b), (c), 0, 0, 0)
#define MFMA16(a, b, c) __builtin_amdgcn_mfma_f32_16x16x16bf16_1k((a), (b), (c), 0, 0, 0)
#define SMFMAC(a, b, c) __builtin_amdgcn_smfmac_f32_16x16x64_bf16((a), (b), (c), sidx, 0, 0)
#define BC8(x) __builtin_bit_cast(bf16x8, (x))
#define BC4(x) __builtin_bit_cast(s16x4, (x))
__device__ __forceinline__ bf16x16 cat4(s16x4 a, s16x4 b, s16x4 c, s16x4 d) {
    s16x8 x = __builtin_shufflevector(a, b, 0, 1, 2, 3, 4, 5, 6, 7), y = __builtin_shufflevector(c, d, 0, 1, 2, 3, 4, 5, 6, 7);
    return __builtin_bit_cast(bf16x16, (s16x16)__builtin_shufflevector(x, y, 0, 1, 2, 3, 4, 5, 6, 7, 8, 9, 10, 11, 12, 13, 14, 15));
}
#define TRREAD(p) __builtin_amdgcn_ds_read_tr16_b64_v4i16((LAS s16x4*)(p))
__device__ __forceinline__ void load_w2(const u16* Wa, const u16* Wb, int h, char* lds, int tid, int wv, bf16x16 (&wa)[10], bf16x16 (&wb)[10]) {
    const int lane = tid & 63, g = lane >> 4, n = lane & 15;
    u32x4 st[10][4];
    const unsigned wo_ = (unsigned)(((tid >> 4) * H_ + h) * E_ + (tid & 15) * 8) * 2u;
#pragma unroll
    for (int kb = 0; kb < 10; ++kb)
#pragma unroll
        for (int i = 0; i < 4; ++i) {
            const char* W = (const char*)((i & 2) ? Wb : Wa) + (size_t)(64 * kb + 32 * (i & 1)) * (H_ * E_ * 2);
            st[kb][i] = *(const u32x4*)(W + wo_);
        }
    const int q4 = n >> 2, p4 = n & 3;
#pragma unroll
    for (int kb = 0; kb < 10; ++kb) {
        char* sb = lds + (kb & 1) * 33792;
#pragma unroll
        for (int i = 0; i < 4; ++i)
            *(u32x4*)(sb + ((tid >> 4) + 32 * (i & 1)) * 528 + (i >> 1) * 256 + (tid & 15) * 16) = st[kb][i];
        BARRIER();
        s16x4 r[2][4];
#pragma unroll
        for (int tl = 0; tl < 2; ++tl)
#pragma unroll
            for (int u = 0; u < 4; ++u) r[tl][u] = TRREAD(sb + k2d(frag_k(g, 4 * u + q4)) * 528 + tl * 256 + (16 * wv + 4 * p4) * 2);
        wa[kb] = cat4(r[0][0], r[0][1], r[0][2], r[0][3]);
        wb[kb] = cat4(r[1][0], r[1][1], r[1][2], r[1][3]);
        asm volatile("" : "+v"(wa[kb]), "+v"(wb[kb]));
    }
    BARRIER();
}
__device__ __forceinline__ void load_wvo(const u16* Wv, const u16* Wo, int h, char* lds, int tid, int wv, bf16x16 (&wvv)[10], bf16x16 (&wo)[5][2]) {
    const int lane = tid & 63, g = lane >> 4, n = lane & 15;
    u32x4 st[10][2], so[2][10];
    const unsigned wo_ = (unsigned)(((tid >> 4) * H_ + h) * E_ + (tid & 15) * 8) * 2u;
#pragma unroll
    for (int kb = 0; kb < 10; ++kb)
#pragma unroll
        for (int i = 0; i < 2; ++i) st[kb][i] = *(const u32x4*)((const char*)Wv + (size_t)(64 * kb + 32 * i) * (H_ * E_ * 2) + wo_);
#pragma unroll
    for (int t = 0; t < 2; ++t)
#pragma unroll
        for (int i = 0; i < 10; ++i) so[t][i] = *(const u32x4*)((const char*)Wo + ((size_t)h * E_ + 64 * t) * (D_ * 2) + (size_t)i * (NT * 16) + tid * 16);
    const int q4 = n >> 2, p4 = n & 3;
#pragma unroll
    for (int kb = 0; kb < 10; ++kb) {
        char* sb = lds + (kb & 1) * 17408;
#pragma unroll
        for (int i = 0; i < 2; ++i) *(u32x4*)(sb + ((tid >> 4) + 32 * i) * 272 + (tid & 15) * 16) = st[kb][i];
        BARRIER();
        s16x4 r[4];
#pragma unroll
        for (int u = 0; u < 4; ++u) r[u] = TRREAD(sb + k2d(frag_k(g, 4 * u + q4)) * 272 + (16 * wv + 4 * p4) * 2);
        wvv[kb] = cat4(r[0], r[1], r[2], r[3]);
        asm volatile("" : "+v"(wvv[kb]));
    }
#pragma unroll
    for (int t = 0; t < 2; ++t) {
        BARRIER();
#pragma unroll
        for (int i = 0; i < 10; ++i) { const int c = i * NT + tid, e = c / 80, cc = c - e * 80; *(u32x4*)(lds + e * 1296 + cc * 16) = so[t][i]; }
        BARRIER();
#pragma unroll
        for (int ct = 0; ct < 5; ++ct) {
            s16x4 r[4];
#pragma unroll
            for (int u = 0; u < 4; ++u) r[u] = TRREAD(lds + k2d(frag_k(g, 4 * u + q4)) * 1296 + (16 * (5 * wv + ct) + 4 * p4) * 2);
            wo[ct][t] = cat4(r[0], r[1], r[2], r[3]);
            asm volatile("" : "+v"(wo[ct][t]));
        }
    }
    BARRIER();
}
__device__ __forceinline__ void tw_store(char* dst, f32x4 a0, f32x4 a1) {
    const unsigned p0 = pk_bf16(a0[0], a0[1]), p1 = pk_bf16(a0[2], a0[3]), p2 = pk_bf16(a1[0], a1[1]), p3 = pk_bf16(a1[2], a1[3]);
    *(u16*)(dst + 0) = (u16)p0; *(u16*)(dst + 16) = (u16)(p0 >> 16); *(u16*)(dst + 32) = (u16)p1; *(u16*)(dst + 48) = (u16)(p1 >> 16);
    *(u16*)(dst + 4096) = (u16)p2; *(u16*)(dst + 4096 + 16) = (u16)(p2 >> 16); *(u16*)(dst + 4096 + 32) = (u16)p3; *(u16*)(dst + 4096 + 48) = (u16)(p3 >> 16);
}
__device__ __forceinline__ void arrive(unsigned* ctrl, int idx, int tid) {
    asm volatile("s_waitcnt vmcnt(0)" ::: "memory");
    __syncthreads();
    if (tid == 0) __hip_atomic_fetch_add(ctrl + idx, 1u, __ATOMIC_RELAXED, __HIP_MEMORY_SCOPE_AGENT);
}
__device__ __forceinline__ u32x2 half_add(f32x4 a, f32x4 b) {
    float cv[4];
#pragma unroll
    for (int r = 0; r < 4; ++r) {
        auto sw = __builtin_amdgcn_permlane32_swap(f2u(a[r]), f2u(b[r]), false, false);
        cv[r] = u2f(sw[0]) + u2f(sw[1]);
    }
    return u32x2{pk_bf16(cv[0], cv[1]), pk_bf16(cv[2], cv[3])};
}

__global__ void __launch_bounds__(NT, 2) k_main(MainArgs a) {
    extern __shared__ __attribute__((aligned(16))) char lds[];
    const int tid = threadIdx.x, lane = tid & 63;
    const int wv = __builtin_amdgcn_readfirstlane(tid >> 6);
    unsigned* ctrl = (unsigned*)(a.ws + OFF_CTRL);
#define SS(k) do {} while (0)
    int role;
    {
        unsigned* sm = (unsigned*)(lds + L_MISC);
        if (tid == 0) {
            const unsigned nearh = (xcc_id() >> 2) == 0;
            const unsigned t = __hip_atomic_fetch_add(ctrl + (nearh ? C_CNT0 : C_CNT1), 1u, __ATOMIC_RELAXED, __HIP_MEMORY_SCOPE_AGENT);
            const bool own = t < 128u;
            const int idx = own ? (int)t : 255 - (int)t;
            const bool near_list = (nearh != 0) == own;
            const int r = near_list ? (idx < NREC ? idx : idx - NREC + R_S) : NREC + idx;
            sm[0] = (unsigned)r;
        }
        __syncthreads();
        role = __builtin_amdgcn_readfirstlane((int)sm[0]);
        __syncthreads();
    }
    SS(1);
    const rsrc_t rs = __builtin_amdgcn_make_buffer_rsrc(a.ws, 0, (unsigned)(250 * MB), 0x00020000);
    if (role >= R_S) {
        const int sp = role - R_S;
        if (sp == 0) {
            if (tid == 0) {
                unsigned best[4] = {~0u, ~0u, ~0u, ~0u};
                for (int rep = 0; rep < 4; ++rep)
#pragma unroll
                    for (int t = 0; t < 4; ++t) {
                        unsigned* ad = (unsigned*)(a.ws + OFF_HO + (size_t)(t >> 1) * 2 * MB + 50 * 16384 + (t & 1) * 8192);
                        unsigned z = 0, r;
                        unsigned long long c0 = __builtin_amdgcn_s_memtime();
                        asm volatile("s_waitcnt lgkmcnt(0)\n\tglobal_atomic_add %0, %1, %2, off sc0\n\ts_waitcnt vmcnt(0)" : "=v"(r) : "v"(ad), "v"(z) : "memory");
                        unsigned long long c1 = __builtin_amdgcn_s_memtime();
                        unsigned d = (unsigned)(c1 - c0); best[t] = d < best[t] ? d : best[t];
                    }
                __hip_atomic_store(ctrl + C_POL, 0x100u | (best[1] < best[0] ? 1u : 0u) | (best[3] < best[2] ? 2u : 0u), __ATOMIC_RELAXED, __HIP_MEMORY_SCOPE_AGENT);
            }
            __syncthreads();
        }
        for (int task = NS - 1 - sp; task < 32; task += NS) {
            const int p0 = task * 64;
            unsigned* tile = (unsigned*)lds;
            unsigned short xr[8][6], xi[8][6];
#pragma unroll
            for (int rr = 0; rr < 8; ++rr) {
                const int pos = p0 + wv + 8 * rr, m = (pos >> 8) + 8 * ((pos >> 5) & 7) + 64 * ((pos >> 2) & 7) + 512 * (pos & 3);
#pragma unroll
                for (int kk = 0; kk < 6; ++kk) { const int k = min(lane + 64 * kk, 320); xr[rr][kk] = a.wr[(size_t)m * 321 + k]; xi[rr][kk] = a.wi[(size_t)m * 321 + k]; }
            }
#pragma unroll
            for (int rr = 0; rr < 8; ++rr)
#pragma unroll
                for (int kk = 0; kk < 6; ++kk) { const int k = lane + 64 * kk; if (k < 321) tile[(wv + 8 * rr) * 323 + k] = (unsigned)xr[rr][kk] | ((unsigned)xi[rr][kk] << 16); }
            __syncthreads();
            for (int i = tid; i < 321 * 16; i += NT) {
                const int k = i >> 4, r = (i & 15) * 4;
                u32x4 v = {tile[r * 323 + k], tile[(r + 1) * 323 + k], tile[(r + 2) * 323 + k], tile[(r + 3) * 323 + k]};
                __builtin_amdgcn_raw_buffer_store_b128(v, rs, (unsigned)(OFF_WT + ((size_t)k * S_ + p0 + r) * 4), 0, 16);
            }
            __syncthreads();
        }
        arrive(ctrl, C_OB, tid);
        return;
    }
    auto get_pol = [&]() -> unsigned {
        if (wv == 0) {
            unsigned v, spins = 0;
            for (;;) {
                v = __builtin_amdgcn_readfirstlane(__hip_atomic_load(ctrl + C_POL, __ATOMIC_RELAXED, __HIP_MEMORY_SCOPE_AGENT));
                if (v & 0x100u) break;
                __builtin_amdgcn_s_sleep(2);
                if (spin_abort(spins, ctrl)) break;
            }
            wait_counters(ctrl, C_FILL, 1, NB, lane);
            if (lane == 0) *(volatile unsigned*)(lds + L_MISC + 32) = v;
        }
        __syncthreads();
        return __builtin_amdgcn_readfirstlane(*(volatile unsigned*)(lds + L_MISC + 32));
    };
    const char* xf = a.ws + OFF_XF;
    char* ho = a.ws + OFF_HO;
    const int g = lane >> 4, n = lane & 15;
    constexpr unsigned so_a = lmap(SLOT_SIGA), so_b = lmap(SLOT_SIGB);
    const int sidx = (lane & 8) ? 0xEEEE : 0x4444;
    const unsigned tw_off = (wv >> 1) * 1024 + (2 * (wv & 1) + (n >> 3)) * 256 + (n & 7) * 2;

    if (role < R_B) {
        const int h = head_of(role & 31);
        const unsigned mo0 = lmap(SLOT_MEM + tid * 16), mo1 = lmap(SLOT_MEM + 8192 + (tid & 127) * 16);
        unsigned pol = 0, target = 0;
        auto slotp = [&](int s) -> char* { return ho + (size_t)(s & 3) * MB + (((pol >> ((s >> 1) & 1)) & 1) ? 8192 : 0); };
        auto gather = [&](int s) {
            if (s > 0) {
                const char* pslot = slotp(s - 1);
                ST2(0);
                if (s > 1) wait_rt(target);
                ST2(1);
                unsigned spins = 0;
                if (wv < 2) {
                    u32x4 v, v2;
                    for (;;) {
                        asm volatile("global_load_dwordx4 %0, %2, %4 sc1\n\tglobal_load_dwordx4 %1, %3, %4 sc1\n\ts_waitcnt vmcnt(0)"
                                     : "=&v"(v), "=&v"(v2) : "v"(mo0), "v"(mo1), "s"(pslot) : "memory");
                        if (__all(max(umax3(umax3(v.x, v.y, v.z), v.w, v2.x), umax3(v2.y, v2.z, v2.w)) != ~0u)) break;
                        if (spin_abort(spins, ctrl)) break;
                    }
                    *(u32x4*)(lds + L_MF + tid * 16) = v;
                    *(u32x4*)(lds + L_MF + 8192 + tid * 16) = v2;
                } else {
                    u32x4 v;
                    for (;;) {
                        asm volatile("global_load_dwordx4 %0, %1, %2 sc1\n\ts_waitcnt vmcnt(0)" : "=&v"(v) : "v"(mo0), "s"(pslot) : "memory");
                        if (__all(max(umax3(v.x, v.y, v.z), v.w) != ~0u)) break;
                        if (spin_abort(spins, ctrl)) break;
                    }
                    *(u32x4*)(lds + L_MF + tid * 16) = v;
                }
            }
            ST2(2);
            BARRIER();
            ST2(3);
        };
        auto next_target = [&](int s) {
            if (s > 0 && s < NSEG - 1) {
                unsigned sa, gb, spins = 0;
                for (;;) {
                    asm volatile("v_mov_b32 %0, 0\n\tglobal_load_dword %1, %0, %3 sc1\n\tglobal_load_dword %0, %0, %2 sc1\n\ts_waitcnt vmcnt(0)"
                                 : "=&v"(sa), "=&v"(gb) : "s"(slotp(s) + so_a), "s"(slotp(s - 1) + so_b) : "memory");
                    sa = __builtin_amdgcn_readfirstlane(sa); gb = __builtin_amdgcn_readfirstlane(gb);
                    if (sa != ~0u && gb != ~0u) break;
                    if (spin_abort(spins, ctrl)) break;
                }
                target = sa + DBC + gb + DAC;
            }
        };
        if (role < NA) {
            bf16x16 wq[10], wk[10];
            load_w2(a.Wq, a.Wk, h, lds, tid, wv, wq, wk);
            SS(2);
            load_mem0(a.mem0, lds, tid);
            pol = get_pol();
            SS(3);
            auto front = [&](int s, unsigned& t_sig, unsigned ap_est, auto&& hook) {
                gather(s);
                f32x4 aq = {0.f, 0.f, 0.f, 0.f}, ak = aq;
                bf16x8 mf[10];
#pragma unroll
                for (int kb = 0; kb < 3; ++kb) mf[kb] = *(const bf16x8*)(lds + L_MF + kb * 1024 + lane * 16);
                if (role == 0 && wv == 1) {
                    t_sig = rt32();
                    if (lane == 0) st4_sc1(slotp(s) + so_a, 0u, nosent(t_sig + ap_est));
                }
                __builtin_amdgcn_sched_barrier(0);
#pragma unroll
                for (int kb = 0; kb < 10; ++kb) {
                    aq = SMFMAC(mf[kb], wq[kb], aq); ak = SMFMAC(mf[kb], wk[kb], ak);
                    if (kb + 3 < 10) mf[kb + 3] = *(const bf16x8*)(lds + L_MF + (kb + 3) * 1024 + lane * 16);
                    __builtin_amdgcn_sched_barrier(0);
                }
                hook();
                const u32x2 p = half_add(aq, ak);
                char* qd = lds + L_QF + (lane >> 5) * 4096 + tw_off + (g & 1) * 64;
                *(u16*)(qd + 0) = (u16)p.x; *(u16*)(qd + 16) = (u16)(p.x >> 16); *(u16*)(qd + 32) = (u16)p.y; *(u16*)(qd + 48) = (u16)(p.y >> 16);
            };
            if (wv == 0) {
                const int wg = h >> 2;
                const unsigned po_x = lmap(SLOT_P + wg * 2560 + ((h >> 1) & 1) * 1024 + (g * 16 + (h & 1) * 8 + (n & 7)) * 16 + (n >> 3) * 8);
                const unsigned po_m = lmap(SLOT_P + wg * 2560 + 2048 + (h & 3) * 128 + (n & 7) * 16 + (g & 1) * 8 + (n >> 3) * 4);
                const unsigned qoff = L_QF + (g * 16 + (n & 7)) * 16;
                const bool hi = n >= 8;
                unsigned dummy = 0;
                BARRIER();
                SS(4);
                for (int s = 0; s < NSEG; ++s) {
                    bf16x8 kx[8];
                    front(s, dummy, 0, [&]() {
#pragma unroll
                        for (int t = 0; t < 8; ++t) kx[t] = *(const bf16x8*)(lds + L_KX + t * 1024 + lane * 16);
                    });
                    ST2(4);
                    BARRIER();
                    ST2(5);
                    if (s < NSEG - 1) {
                        char* slot = slotp(s);
                        f32x4 s0 = {0.f, 0.f, 0.f, 0.f}, s1 = s0, s2 = s0;
                        bf16x8 qf[4], km[4];
#pragma unroll
                        for (int t = 0; t < 4; ++t) { qf[t] = *(const bf16x8*)(lds + qoff + t * 1024); km[t] = *(const bf16x8*)(lds + L_KM + t * 1024 + lane * 16); }
#pragma unroll
                        for (int t = 0; t < 4; ++t) { s1 = MFMA32(kx[t], qf[t], s1); s2 = MFMA32(kx[4 + t], qf[t], s2); s0 = MFMA32(km[t], qf[t], s0); }
                        __builtin_amdgcn_sched_group_barrier(0x100, 8, 0);
                        __builtin_amdgcn_sched_group_barrier(0x8, 12, 0);
                        const float cexp = 0.08838834764831845f * 1.4426950408889634f;
                        float v[6];
#pragma unroll
                        for (int r = 0; r < 4; ++r) v[r] = hi ? s2[r] : s1[r];
                        v[4] = hi ? s0[2] : s0[0]; v[5] = hi ? s0[3] : s0[1];
                        if (g >= 2) { v[4] = -INFINITY; v[5] = -INFINITY; }
                        float m, tmp_;
                        asm("v_max3_f32 %0, %2, %3, %4\n\tv_max3_f32 %1, %5, %6, %7\n\tv_max_f32 %0, %0, %1\n\ts_nop 1\n\t"
                            "v_max_f32_dpp %0, %0, %0 row_ror:8 row_mask:0xf bank_mask:0xf\n\tv_mov_b32 %1, %0\n\ts_nop 1\n\t"
                            "v_permlane16_swap_b32 %0, %1\n\tv_max_f32 %0, %0, %1\n\tv_mov_b32 %1, %0\n\ts_nop 1\n\t"
                            "v_permlane32_swap_b32 %0, %1\n\tv_max_f32 %0, %0, %1"
                            : "=&v"(m), "=&v"(tmp_) : "v"(v[0]), "v"(v[1]), "v"(v[2]), "v"(v[3]), "v"(v[4]), "v"(v[5]));
                        const float mc = -m * cexp;
#pragma unroll
                        for (int r = 0; r < 6; ++r) v[r] = __builtin_amdgcn_exp2f(__builtin_fmaf(v[r], cexp, mc));
                        float sum = ((v[0] + v[1]) + (v[2] + v[3])) + (v[4] + v[5]);
                        sum += u2f(__builtin_amdgcn_update_dpp(0, (int)f2u(sum), 0x128, 0xf, 0xf, false));
                        {
                            auto sw = __builtin_amdgcn_permlane16_swap(f2u(sum), f2u(sum), false, false);
                            sum = u2f(sw[0]) + u2f(sw[1]);
                            auto sw2 = __builtin_amdgcn_permlane32_swap(f2u(sum), f2u(sum), false, false);
                            sum = u2f(sw2[0]) + u2f(sw2[1]);
                        }
                        const float inv = __builtin_amdgcn_rcpf(sum);
                        u32x2 px = {nosent(pk_bf16(v[0] * inv, v[1] * inv)), nosent(pk_bf16(v[2] * inv, v[3] * inv))};
                        const unsigned pm = nosent(pk_bf16(v[4] * inv, v[5] * inv));
                        st8_sc1(slot, po_x, px);
                        if (g < 2) st4_sc1(slot, po_m, pm);
                        ST2(7);
                        if (role == 0) { const unsigned t = rt32(); if (lane == 0) *(volatile unsigned*)(lds + L_MISC + 48) = t; LDSWAIT(); }
                    }
                    ST2(6);
                    asm volatile("s_barrier" ::: "memory");
                    ST2F();
                    if (s >= 2) {
                        st8_sc1(slotp(s + 2), po_x, u32x2{~0u, ~0u});
                        if (g < 2) st4_sc1(slotp(s + 2), po_m, ~0u);
                    }
                    next_target(s);
                }
            } else if (wv == 2 || wv == 3) {
                const int qt = wv - 2;
                unsigned dummy = 0;
                BARRIER();
                const unsigned pbx = (2 * qt + (n >> 3)) * 20480 + (h >> 2) * 2560 + ((h >> 1) & 1) * 1024 + (g * 16 + (h & 1) * 8 + (n & 7)) * 16;
                const unsigned pbm = (2 * qt + (n >> 3)) * 20480 + (h >> 2) * 2560 + 2048 + (h & 3) * 128 + (n & 7) * 16 + (g & 1) * 8;
                for (int s = 0; s < NSEG; ++s) {
                    front(s, dummy, 0, []() {});
                    BARRIER();
                    f32x4 s0 = {0.f, 0.f, 0.f, 0.f}, s1 = s0, s2 = s0;
#pragma unroll
                    for (int t = 0; t < 4; ++t) {
                        const bf16x8 km = *(const bf16x8*)(lds + L_KM + t * 1024 + lane * 16), qf = *(const bf16x8*)(lds + L_KX + 8192 + (qt * 4 + t) * 1024 + lane * 16);
                        const bf16x8 k1 = *(const bf16x8*)(lds + L_KX + t * 1024 + lane * 16), k2 = *(const bf16x8*)(lds + L_KX + (4 + t) * 1024 + lane * 16);
                        s1 = MFMA32(k1, qf, s1); s2 = MFMA32(k2, qf, s2); s0 = MFMA32(km, qf, s0);
                    }
                    const float scale = 0.08838834764831845f;
                    const int qi = 16 * qt + n;
                    float v[12];
#pragma unroll
                    for (int r = 0; r < 4; ++r) {
                        v[r] = (g < 2) ? rbf(rbf(s0[r]) * scale) : -INFINITY;
                        v[4 + r] = (4 * g + r <= qi) ? rbf(rbf(s1[r]) * scale) : -999292928.f;
                        v[8 + r] = (16 + 4 * g + r <= qi) ? rbf(rbf(s2[r]) * scale) : -999292928.f;
                    }
                    float m = v[0];
#pragma unroll
                    for (int r = 1; r < 12; ++r) m = fmaxf(m, v[r]);
                    {
                        auto sw = __builtin_amdgcn_permlane16_swap(f2u(m), f2u(m), false, false);
                        m = fmaxf(u2f(sw[0]), u2f(sw[1]));
                        auto sw2 = __builtin_amdgcn_permlane32_swap(f2u(m), f2u(m), false, false);
                        m = fmaxf(u2f(sw2[0]), u2f(sw2[1]));
                    }
                    float sum = 0.f;
#pragma unroll
                    for (int r = 0; r < 12; ++r) { v[r] = __expf(v[r] - m); sum += v[r]; }
                    {
                        auto sw = __builtin_amdgcn_permlane16_swap(f2u(sum), f2u(sum), false, false);
                        sum = u2f(sw[0]) + u2f(sw[1]);
                        auto sw2 = __builtin_amdgcn_permlane32_swap(f2u(sum), f2u(sum), false, false);
                        sum = u2f(sw2[0]) + u2f(sw2[1]);
                    }
                    const float inv = 1.f / sum;
                    u32x4 px = {pk_bf16(v[4] * inv, v[5] * inv), pk_bf16(v[6] * inv, v[7] * inv), pk_bf16(v[8] * inv, v[9] * inv), pk_bf16(v[10] * inv, v[11] * inv)};
                    u32x2 pm = {pk_bf16(v[0] * inv, v[1] * inv), pk_bf16(v[2] * inv, v[3] * inv)};
                    asm volatile("s_barrier" ::: "memory");
                    const char* pxb = a.ws + OFF_PXF + (size_t)s * 81920;
                    st16_sc1(pxb, pbx, px);
                    if (g < 2) st8_sc1(pxb, pbm, pm);
                    next_target(s);
                }
                asm volatile("s_waitcnt vmcnt(0)" ::: "memory");
            } else {
                unsigned ap_est = 70, t_sig = 0, pp = 0;
                auto load_qk = [&](int s) {
                    if (wv >= 4) {
                        if (pp <= (unsigned)s) pp = wait_counters(ctrl, C_PP + h * 32, 1, s + 1, lane);
                        const char* src = a.ws + (wv < 6 ? OFF_KXF : OFF_QXF) + ((size_t)s * H_ + h) * 8192 + (wv & 1) * 4096 + lane * 16;
#pragma unroll
                        for (int i = 0; i < 4; ++i) glds16(src + i * 1024, L_KX + (wv - 4) * 4096 + i * 1024);
                    }
                };
                load_qk(0);
                asm volatile("s_waitcnt vmcnt(0)" ::: "memory");
                BARRIER();
                for (int s = 0; s < NSEG; ++s) {
                    front(s, t_sig, ap_est, []() {});
                    BARRIER();
                    asm volatile("s_barrier" ::: "memory");
                    if (s + 1 < NSEG) load_qk(s + 1);
                    if (role == 0 && wv == 1) {
                        ap_est = __builtin_amdgcn_readfirstlane(*(volatile unsigned*)(lds + L_MISC + 48)) - t_sig;
                        if (s >= 2 && lane == 0) st4_sc1(slotp(s + 2) + so_a, 0u, ~0u);
                    }
                    if (wv == 4 && lane == 0 && s > 0) set_counter(ctrl, C_AP + h * 32, s);
                    next_target(s);
                }
            }
            __syncthreads();
            if (tid == 0) set_counter(ctrl, C_AP + h * 32, NSEG);
        } else {
            bf16x16 wvv[10], wo[5][2];
            load_wvo(a.Wv, a.Wo, h, lds, tid, wv, wvv, wo);
            SS(2);
            load_mem0(a.mem0, lds, tid);
            pol = get_pol();
            SS(3);
            const unsigned vm_off = L_QF + (wv >> 2) * 1024 + ((wv & 3) * 16 + (n >> 3) * 8 + 4 * (g & 1)) * 16 + (n & 7) * 2;
            const unsigned zo4 = (4 + 5 * wv + (g >> 1) + 2 * (g & 1)) * 16384 + h * 256 + n * 16;
            const unsigned zo1 = (4 + 5 * wv + 4) * 16384 + h * 256 + n * 16 + (g & 1) * 8;
            for (int s = 0; s < NSEG; ++s) {
                gather(s);
                f32x4 av = {0.f, 0.f, 0.f, 0.f};
                bf16x8 mf[10];
#pragma unroll
                for (int kb = 0; kb < 4; ++kb) mf[kb] = *(const bf16x8*)(lds + L_MF + kb * 1024 + lane * 16);
                __builtin_amdgcn_sched_barrier(0);
#pragma unroll
                for (int kb = 0; kb < 10; ++kb) {
                    av = SMFMAC(mf[kb], wvv[kb], av);
                    if (kb + 4 < 10) mf[kb + 4] = *(const bf16x8*)(lds + L_MF + (kb + 4) * 1024 + lane * 16);
                    __builtin_amdgcn_sched_barrier(0);
                }
                const u32x2 p = half_add(av, av);
                char* vd = lds + vm_off;
                *(u16*)(vd + 0) = (u16)p.x; *(u16*)(vd + 16) = (u16)(p.x >> 16); *(u16*)(vd + 32) = (u16)p.y; *(u16*)(vd + 48) = (u16)(p.y >> 16);
                ST2(4);
                BARRIER();
                ST2(5);
                const bf16x8 vf0 = *(const bf16x8*)(lds + L_QF + lane * 16), vf1 = *(const bf16x8*)(lds + L_QF + 1024 + lane * 16);
                f32x4 z[5];
#pragma unroll
                for (int ct = 0; ct < 5; ++ct) { z[ct] = f32x4{0.f, 0.f, 0.f, 0.f}; z[ct] = SMFMAC(vf0, wo[ct][0], z[ct]); }
#pragma unroll
                for (int ct = 0; ct < 5; ++ct) z[ct] = SMFMAC(vf1, wo[ct][1], z[ct]);
                {
                    char* slot = slotp(s);
                    const u32x2 z01 = half_add(z[0], z[1]), z23 = half_add(z[2], z[3]);
                    auto sx = __builtin_amdgcn_permlane16_swap(z01.x, z23.x, false, false);
                    auto sy = __builtin_amdgcn_permlane16_swap(z01.y, z23.y, false, false);
                    st16_sc1(slot, zo4, u32x4{nosent(sx[0]), nosent(sy[0]), nosent(sx[1]), nosent(sy[1])});
                    const u32x2 z4 = half_add(z[4], z[4]);
                    if (lane < 32) st8_sc1(slot, zo1, u32x2{nosent(z4.x), nosent(z4.y)});
                }
                ST2(6);
                ST2F();
                next_target(s);
            }
        }
    } else if (role < NREC) {
        const int ct = role - R_B;
        {
            const u32x4 sv = {~0u, ~0u, ~0u, ~0u};
            for (int q = ct; q < 176; q += NB) {
                const unsigned base = (unsigned)OFF_HO + (q / 44) * (unsigned)MB + (q % 44) * 16384 + tid * 16;
                __builtin_amdgcn_raw_buffer_store_b128(sv, rs, base, 0, 16);
                __builtin_amdgcn_raw_buffer_store_b128(sv, rs, base, 8192, 16);
            }
            arrive(ctrl, C_FILL, tid);
        }
        SS(2);
        const unsigned pol = get_pol();
        SS(3);
        auto slotp = [&](int s) -> char* { return ho + (size_t)(s & 3) * MB + (((pol >> ((s >> 1) & 1)) & 1) ? 8192 : 0); };
        const unsigned po0 = lmap(SLOT_P + wv * 2560 + lane * 16), po1 = lmap(SLOT_P + wv * 2560 + 1024 + lane * 16);
        const unsigned po2 = lmap(SLOT_P + wv * 2560 + 2048 + g * 128 + (n & 7) * 16);
        const unsigned zo = (4 + ct) * 16384 + wv * 1024 + lane * 16;
        const unsigned mo_pub = lmap(SLOT_MEM + (2 * ct + (wv & 1)) * 128 + (lane & 31) * 4);
        const bool hi = n >= 8;
        unsigned vp = 0;
        u32x4 uf[4];
        auto load_u = [&](int s) {
            if (vp <= (unsigned)s) vp = wait_counters(ctrl, C_VP + 4 * wv * 32, 4, s + 1, lane);
            const char* src = a.ws + OFF_UF + ((size_t)s * NB + ct) * 32768 + wv * 4096;
            const unsigned o = lane * 16;
            asm volatile("global_load_dwordx4 %0, %4, %5 sc1\n\tglobal_load_dwordx4 %1, %4, %5 offset:1024 sc1\n\tglobal_load_dwordx4 %2, %4, %5 offset:2048 sc1\n\tglobal_load_dwordx4 %3, %4, %5 offset:3072 sc1"
                         : "=&v"(uf[0]), "=&v"(uf[1]), "=&v"(uf[2]), "=&v"(uf[3]) : "v"(o), "s"(src) : "memory");
        };
        load_u(0);
        SS(4);
        unsigned gbmin = 1000;
        for (int s = 0; s < NSEG - 1; ++s) {
            char* slot = slotp(s);
            const unsigned target = read_sig(slot + so_a, ctrl) + DBC;
            asm volatile("" : "+v"(uf[0]), "+v"(uf[1]), "+v"(uf[2]), "+v"(uf[3]));
            u32x4 p0, p1, p2, zz; unsigned spins = 0, spz = 0;
            ST2(0);
            wait_rt(target);
            ST2(1);
            for (;;) {
                asm volatile("global_load_dwordx4 %3, %7, %8 sc1\n\tglobal_load_dwordx4 %0, %4, %8 sc1\n\tglobal_load_dwordx4 %1, %5, %8 sc1\n\tglobal_load_dwordx4 %2, %6, %8 sc1\n\ts_waitcnt vmcnt(0)"
                             : "=&v"(p0), "=&v"(p1), "=&v"(p2), "=&v"(zz) : "v"(po0), "v"(po1), "v"(po2), "v"(zo), "s"(slot) : "memory");
                const unsigned m0 = umax3(umax3(p0.x, p0.y, p0.z), p0.w, p1.x), m1 = umax3(p1.y, p1.z, p1.w), m2 = umax3(umax3(zz.x, zz.y, zz.z), zz.w, p2.x), m3 = umax3(p2.y, p2.z, p2.w);
                if (__all(max(umax3(m0, m1, m2), m3) != ~0u)) break;
                if (spin_abort(spins, ctrl)) break;
            }
            ST2(2);
            const f32x4 zero = {0.f, 0.f, 0.f, 0.f};
            f32x4 xa = MFMA32(BC8(zz), BC8(p2), zero), xb = MFMA32(BC8(uf[1]), BC8(p0), zero);
            xa = MFMA32(BC8(uf[0]), BC8(p0), xa); xb = MFMA32(BC8(uf[3]), BC8(p1), xb);
            xa = MFMA32(BC8(uf[2]), BC8(p1), xa);
            f32x4 acc;
#pragma unroll
            for (int r = 0; r < 4; ++r) acc[r] = hi ? xb[r] : xa[r];
            *(f32x4*)(lds + L_RED + (wv * 16 + n) * 64 + g * 16) = acc;
            BARRIER();
            ST2(3);
            if (wv < 2) {
                const char* rp = lds + L_RED + (lane >> 5) * 4096 + ((lane >> 2) & 7) * 64 + wv * 32 + (lane & 3) * 8;
                c32 t = ((*(const c32*)(rp) + *(const c32*)(rp + 512)) + (*(const c32*)(rp + 1024) + *(const c32*)(rp + 1536))) +
                        ((*(const c32*)(rp + 2048) + *(const c32*)(rp + 2560)) + (*(const c32*)(rp + 3072) + *(const c32*)(rp + 3584)));
                auto sw = __builtin_amdgcn_permlane32_swap(f2u(t.x), f2u(t.y), false, false);
                const float y = u2f(sw[0]) + u2f(sw[1]);
                auto sw2 = __builtin_amdgcn_permlane32_swap(f2u(y), f2u(y), false, false);
                const unsigned o = nosent(pk_bf16(u2f(sw2[0]), u2f(sw2[1])));
                if (lane < 32) st4_sc1(slot, mo_pub, o);
                ST2(7);
                if (ct == 0 && wv == 0) {
                    const unsigned gb = rt32() - target;
                    gbmin = gb < gbmin ? gb : gbmin;
                    if (lane == 0) { st4_sc1(slot + so_b, 0u, gbmin); if (s >= 2) st4_sc1(slotp(s + 2) + so_b, 0u, ~0u); }
                }
                if (s >= 2 && lane < 32) st4_sc1(slotp(s + 2), mo_pub, ~0u);
            }
            ST2(4);
            asm volatile("s_barrier" ::: "memory");
            ST2F();
            if (wv == 2 && lane == 0 && s > 0) set_counter(ctrl, C_ZP + ct * 32, s);
            st16_sc1(a.ws + OFF_ZF + ((size_t)s * NB + ct) * 8192, tid * 16, zz);
            st16_sc1(slot, zo, u32x4{~0u, ~0u, ~0u, ~0u});
            if (s + 1 < NSEG - 1) load_u(s + 1);
        }
        {
            char* slot = slotp(NSEG - 1);
            u32x4 zz; unsigned spins = 0;
            for (;;) {
                asm volatile("global_load_dwordx4 %0, %1, %2 sc1\n\ts_waitcnt vmcnt(0)" : "=&v"(zz) : "v"(zo), "s"(slot) : "memory");
                if (__all(max(umax3(zz.x, zz.y, zz.z), zz.w) != ~0u)) break;
                __builtin_amdgcn_s_sleep(2);
                if (spin_abort(spins, ctrl)) break;
            }
            st16_sc1(a.ws + OFF_ZF + ((size_t)(NSEG - 1) * NB + ct) * 8192, tid * 16, zz);
            asm volatile("s_waitcnt vmcnt(0)" ::: "memory");
            __syncthreads();
            if (tid == 0) set_counter(ctrl, C_ZP + ct * 32, NSEG);
        }
    } else if (role < R_V) {
        const int h = head_of(role - R_P);
        bf16x16 wq[10], wk[10];
        load_w2(a.Wq, a.Wk, h, lds, tid, wv, wq, wk);
        SS(2);
        if (wv == 0) wait_counters(ctrl, C_XF, 1, NO, lane);
        __syncthreads();
        prefetch_x(xf, 0, 0, wv, lane);
        prefetch_x(xf, 1, 1, wv, lane);
        int b3 = 0;
        for (int s = 0; s < NSEG; ++s) {
            ST2(0);
            if (s < 3 || s >= NSEG - 2) asm volatile("s_waitcnt vmcnt(0)" ::: "memory"); else asm volatile("s_waitcnt vmcnt(7)" ::: "memory");
            ST2(1);
            BARRIER();
            ST2(2);
            if (s > 0 && tid == 0) set_counter(ctrl, C_PP + h * 32, s < 3 ? s : s - 1);
            if (s < 4) SS(3 + s);
            const char* psrc = xf + (size_t)min(s + 2, NSEG - 1) * 40960 + wv * 5120 + lane * 16;
            const unsigned pdst = L_XB + (b3 == 0 ? 2 : b3 - 1) * 40960 + wv * 5120;
            ST2(3);
            f32x4 q0 = {0.f, 0.f, 0.f, 0.f}, q1 = q0, k0 = q0, k1 = q0;
            const char* xb = lds + L_XB + b3 * 40960 + lane * 16;
            b3 = b3 == 2 ? 0 : b3 + 1;
            bf16x8 xr[10][4];
#pragma unroll
            for (int i = 0; i < 4; ++i) xr[0][i] = *(const bf16x8*)(xb + i * 1024);
#pragma unroll
            for (int kb = 0; kb < 10; ++kb) {
                if (kb + 1 < 10) {
#pragma unroll
                    for (int i = 0; i < 4; ++i) xr[kb + 1][i] = *(const bf16x8*)(xb + ((kb + 1) * 4 + i) * 1024);
                }
                __builtin_amdgcn_sched_barrier(0);
                q0 = MFMA32(xr[kb][0], lo8(wq[kb]), q0); q1 = MFMA32(xr[kb][1], lo8(wq[kb]), q1); k0 = MFMA32(xr[kb][0], lo8(wk[kb]), k0); k1 = MFMA32(xr[kb][1], lo8(wk[kb]), k1);
                q0 = MFMA32(xr[kb][2], hi8(wq[kb]), q0); q1 = MFMA32(xr[kb][3], hi8(wq[kb]), q1); k0 = MFMA32(xr[kb][2], hi8(wk[kb]), k0); k1 = MFMA32(xr[kb][3], hi8(wk[kb]), k1);
                __builtin_amdgcn_sched_barrier(0);
                if ((kb & 1) == 0) glds16(psrc + (kb >> 1) * 1024, pdst + (kb >> 1) * 1024);
            }
            tw_store(lds + L_T + tw_off + g * 64, q0, q1);
            tw_store(lds + L_T + 8192 + tw_off + g * 64, k0, k1);
            ST2(4);
            BARRIER();
            ST2(5);
            const u32x4 vq = *(const u32x4*)(lds + L_T + tid * 16), vk = *(const u32x4*)(lds + L_T + 8192 + tid * 16);
            st16_sc1(a.ws + OFF_KXF + ((size_t)s * H_ + h) * 8192, tid * 16, vk);
            st16_sc1(a.ws + OFF_QXF + ((size_t)s * H_ + h) * 8192, tid * 16, vq);
            ST2(6);
            ST2F();
        }
        asm volatile("s_waitcnt vmcnt(0)" ::: "memory");
        __syncthreads();
        if (tid == 0) set_counter(ctrl, C_PP + h * 32, NSEG);
    } else if (role < R_O) {
        const int h = head_of(role - R_V);
        bf16x16 wvv[10], wo[5][2];
        load_wvo(a.Wv, a.Wo, h, lds, tid, wv, wvv, wo);
        SS(2);
        if (wv == 0) wait_counters(ctrl, C_XF, 1, NO, lane);
        __syncthreads();
        prefetch_x(xf, 0, 0, wv, lane);
        prefetch_x(xf, 1, 1, wv, lane);
        int b3 = 0;
        const int Lq = (n < 8) ? 4 * (n >> 1) + (n & 1) : 4 * ((n - 8) >> 1) + 2 + (n & 1);
        const unsigned twv_off = (wv >> 1) * 1024 + (2 * (wv & 1) + (Lq >> 3)) * 256 + (Lq & 7) * 2;
        const unsigned uo = (unsigned)(5 * wv * 32768 + h * 1024 + lane * 16);
        for (int s = 0; s < NSEG; ++s) {
            ST2(0);
            if (s < 3 || s >= NSEG - 2) asm volatile("s_waitcnt vmcnt(0)" ::: "memory"); else asm volatile("s_waitcnt vmcnt(10)" ::: "memory");
            ST2(1);
            BARRIER();
            ST2(2);
            if (s > 0 && tid == 0) set_counter(ctrl, C_VP + h * 32, s < 3 ? s : s - 1);
            if (s < 4) SS(3 + s);
            const char* psrc = xf + (size_t)min(s + 2, NSEG - 1) * 40960 + wv * 5120 + lane * 16;
            const unsigned pdst = L_XB + (b3 == 0 ? 2 : b3 - 1) * 40960 + wv * 5120;
            ST2(3);
            f32x4 v0 = {0.f, 0.f, 0.f, 0.f}, v1 = v0;
            const char* xb = lds + L_XB + b3 * 40960 + lane * 16;
            b3 = b3 == 2 ? 0 : b3 + 1;
            bf16x8 xr[10][4];
#pragma unroll
            for (int kb = 0; kb < 2; ++kb)
#pragma unroll
                for (int i = 0; i < 4; ++i) xr[kb][i] = *(const bf16x8*)(xb + (kb * 4 + i) * 1024);
#pragma unroll
            for (int kb = 0; kb < 10; ++kb) {
                if (kb + 2 < 10) {
#pragma unroll
                    for (int i = 0; i < 4; ++i) xr[kb + 2][i] = *(const bf16x8*)(xb + ((kb + 2) * 4 + i) * 1024);
                }
                __builtin_amdgcn_sched_barrier(0);
                v0 = MFMA32(xr[kb][0], lo8(wvv[kb]), v0); v1 = MFMA32(xr[kb][1], lo8(wvv[kb]), v1);
                v0 = MFMA32(xr[kb][2], hi8(wvv[kb]), v0); v1 = MFMA32(xr[kb][3], hi8(wvv[kb]), v1);
                __builtin_amdgcn_sched_barrier(0);
                if ((kb & 1) == 0) glds16(psrc + (kb >> 1) * 1024, pdst + (kb >> 1) * 1024);
            }
            tw_store(lds + L_T + twv_off + g * 64, v0, v1);
            ST2(4);
            BARRIER();
            ST2(5);
            bf16x8 vf[2][4];
#pragma unroll
            for (int tt = 0; tt < 2; ++tt)
#pragma unroll
                for (int t = 0; t < 4; ++t) vf[tt][t] = *(const bf16x8*)(lds + L_T + (tt * 4 + t) * 1024 + lane * 16);
            const char* ub = a.ws + OFF_UF + (size_t)s * NB * 32768;
#pragma unroll
            for (int ct = 0; ct < 5; ++ct) {
                f32x4 u0 = {0.f, 0.f, 0.f, 0.f}, u1 = u0;
#pragma unroll
                for (int t = 0; t < 2; ++t) {
                    u0 = MFMA32(vf[0][2 * t], lo8(wo[ct][t]), u0); u1 = MFMA32(vf[1][2 * t], lo8(wo[ct][t]), u1);
                    u0 = MFMA32(vf[0][2 * t + 1], hi8(wo[ct][t]), u0); u1 = MFMA32(vf[1][2 * t + 1], hi8(wo[ct][t]), u1);
                }
                u32x4 o = {pk_bf16(u0[0], u0[1]), pk_bf16(u0[2], u0[3]), pk_bf16(u1[0], u1[1]), pk_bf16(u1[2], u1[3])};
                st16_sc1(ub + ct * 32768, uo, o);
            }
            ST2(6);
            ST2F();
        }
        asm volatile("s_waitcnt vmcnt(0)" ::: "memory");
        __syncthreads();
        if (tid == 0) set_counter(ctrl, C_VP + h * 32, NSEG);
    } else {
        const int o = role - R_O;
        {
#pragma unroll
            for (int i = 0; i < 5; ++i) {
                const int id = i * NT + tid, l = id & 63, tt = (id >> 6) & 1, hf = (id >> 7) & 1, kb = id >> 8;
                const int ga = l >> 4, tok = 32 * o + 16 * tt + (l & 15), dbase = 64 * kb + 16 * (2 * hf + (ga >> 1)) + 4 * (ga & 1);
                const u32x2 c0 = *(const u32x2*)(a.x + (size_t)tok * D_ + dbase), c1 = *(const u32x2*)(a.x + (size_t)tok * D_ + dbase + 8);
                u32x4 ov = {c0.x, c1.x, c0.y, c1.y};
                __builtin_amdgcn_raw_buffer_store_b128(ov, rs, (unsigned)OFF_XF + o * 40960 + id * 16, 0, 16);
            }
            arrive(ctrl, C_XF, tid);
        }
        SS(2);
        auto obar = [&](int idx) {
            asm volatile("s_waitcnt vmcnt(0)" ::: "memory");
            __syncthreads();
            if (wv == 0) {
                if (lane == 0) __hip_atomic_fetch_add(ctrl + C_OB + idx * 32, 1u, __ATOMIC_RELAXED, __HIP_MEMORY_SCOPE_AGENT);
                wait_counters(ctrl, C_OB + idx * 32, 1, idx == 0 ? NO + NS : NO, lane);
            }
            __syncthreads();
        };
        auto bf2f = [](unsigned short u) -> float { return u2f((unsigned)u << 16); };
        c32* tw = (c32*)(lds + 96 * 1024);
        c32* wbuf = (c32*)(lds) + wv * 720;
        for (int i = tid; i < 640; i += NT) { float sn, cs_; sincospif(-(float)i / 320.f, &sn, &cs_); tw[i] = c32{cs_, sn}; }
        float gwv[10];
#pragma unroll
        for (int j = 0; j < 10; ++j) gwv[j] = bf2f(a.gw[64 * j + lane]);
        __syncthreads();
        for (int task = o; task < 128; task += NO) {
            {
                const int r0 = task * 16 + wv * 2;
                c32 v[10]; float ga = 0.f, gb_ = 0.f;
#pragma unroll
                for (int j = 0; j < 10; ++j) {
                    v[j] = c32{bf2f(a.x[(size_t)r0 * D_ + 64 * j + lane]), bf2f(a.x[(size_t)(r0 + 1) * D_ + 64 * j + lane])};
                    ga += rbf(v[j].x * gwv[j]); gb_ += rbf(v[j].y * gwv[j]);
                }
#pragma unroll
                for (int of = 32; of >= 1; of >>= 1) { ga += __shfl_xor(ga, of); gb_ += __shfl_xor(gb_, of); }
                if (lane < 2) {
                    const float z = rbf(lane ? gb_ : ga);
                    const float gv = rbf(1.f / (1.f + __expf(-z)));
                    __builtin_amdgcn_raw_buffer_store_b32(f2u(gv), rs, (unsigned)(OFF_G + (r0 + lane) * 4), 0, 16);
                }
                fft640(v, wbuf, tw, lane);
                __syncthreads();
                for (int i = tid; i < 321 * 16; i += NT) {
                    const int k = i >> 4, t = i & 15;
                    const c32* zb = (const c32*)lds + (t >> 1) * 720;
                    const c32 z1 = zb[k], z2 = zb[k ? 640 - k : 0];
                    c32 r = (t & 1) ? c32{0.5f * (z1.y + z2.y), 0.5f * (z2.x - z1.x)} : c32{0.5f * (z1.x + z2.x), 0.5f * (z1.y - z2.y)};
                    __builtin_amdgcn_raw_buffer_store_b64(__builtin_bit_cast(u32x2, r), rs, (unsigned)(OFF_X1T + ((size_t)k * S_ + task * 16 + t) * 8), 0, 16);
                }
                __syncthreads();
            }
        }
        for (int i = tid; i < 2048; i += NT) { float sn, cs_; sincospif(-(float)i / 1024.f, &sn, &cs_); tw[i] = c32{cs_, sn}; }
        obar(0);
        {
            const int j = tid & 255, hf = tid >> 8;
            c32* buf = (c32*)lds + hf * 2304;
            auto pd = [](int p) { return p + ((p >> 5) << 2); };
            for (int b = o; b < 161; b += NO) {
                const int col = min(2 * b + hf, 320);
                const unsigned cb = (unsigned)(OFF_X1T + (size_t)col * S_ * 8);
                c32 v[8];
#pragma unroll
                for (int q = 0; q < 8; ++q) v[q] = __builtin_bit_cast(c32, __builtin_amdgcn_raw_buffer_load_b64(rs, cb + (q * 256 + j) * 8, 0, 16));
                const u32x4 wa = __builtin_amdgcn_raw_buffer_load_b128(rs, (unsigned)(OFF_WT + ((size_t)col * S_ + 8 * j) * 4), 0, 16);
                const u32x4 wb = __builtin_amdgcn_raw_buffer_load_b128(rs, (unsigned)(OFF_WT + ((size_t)col * S_ + 8 * j + 4) * 4), 0, 16);
                dft8<-1>(v);
#pragma unroll
                for (int q = 0; q < 8; ++q) buf[pd(q * 256 + j)] = q ? cmul(v[q], tw[j * q]) : v[q];
                __syncthreads();
                const int b2 = (j >> 5) * 256 + (j & 31), lo = j & 31;
#pragma unroll
                for (int q = 0; q < 8; ++q) v[q] = buf[pd(b2 + q * 32)];
                dft8<-1>(v);
#pragma unroll
                for (int q = 0; q < 8; ++q) buf[pd(b2 + q * 32)] = q ? cmul(v[q], tw[8 * lo * q]) : v[q];
                __syncthreads();
                const int b3 = (j >> 2) * 32 + (j & 3), n0 = j & 3;
#pragma unroll
                for (int q = 0; q < 8; ++q) v[q] = buf[pd(b3 + q * 4)];
                dft8<-1>(v);
#pragma unroll
                for (int q = 0; q < 8; ++q) buf[pd(b3 + q * 4)] = q ? cmul(v[q], tw[64 * n0 * q]) : v[q];
                __syncthreads();
#pragma unroll
                for (int q = 0; q < 8; ++q) v[q] = buf[pd(8 * j + q)];
                dft4<-1>(v[0], v[1], v[2], v[3]); dft4<-1>(v[4], v[5], v[6], v[7]);
                const unsigned wv8[8] = {wa.x, wa.y, wa.z, wa.w, wb.x, wb.y, wb.z, wb.w};
#pragma unroll
                for (int q = 0; q < 8; ++q) v[q] = cmul(v[q], c32{u2f(wv8[q] << 16), u2f(wv8[q] & 0xFFFF0000u)});
                dft4<1>(v[0], v[1], v[2], v[3]); dft4<1>(v[4], v[5], v[6], v[7]);
#pragma unroll
                for (int q = 0; q < 8; ++q) buf[pd(8 * j + q)] = v[q];
                __syncthreads();
#pragma unroll
                for (int q = 0; q < 8; ++q) { c32 t = buf[pd(b3 + q * 4)]; v[q] = q ? cmulc(t, tw[64 * n0 * q]) : t; }
                dft8<1>(v);
#pragma unroll
                for (int q = 0; q < 8; ++q) buf[pd(b3 + q * 4)] = v[q];
                __syncthreads();
#pragma unroll
                for (int q = 0; q < 8; ++q) { c32 t = buf[pd(b2 + q * 32)]; v[q] = q ? cmulc(t, tw[8 * lo * q]) : t; }
                dft8<1>(v);
#pragma unroll
                for (int q = 0; q < 8; ++q) buf[pd(b2 + q * 32)] = v[q];
                __syncthreads();
#pragma unroll
                for (int q = 0; q < 8; ++q) { c32 t = buf[pd(q * 256 + j)]; v[q] = q ? cmulc(t, tw[j * q]) : t; }
                dft8<1>(v);
                if (2 * b + hf <= 320) {
#pragma unroll
                    for (int q = 0; q < 8; ++q) __builtin_amdgcn_raw_buffer_store_b64(__builtin_bit_cast(u32x2, v[q]), rs, cb + (q * 256 + j) * 8, 0, 16);
                }
                __syncthreads();
            }
        }
        for (int i = tid; i < 640; i += NT) { float sn, cs_; sincospif(-(float)i / 320.f, &sn, &cs_); tw[i] = c32{cs_, sn}; }
        obar(1);
        for (int task = o; task < 128; task += NO) {
            for (int i = tid; i < 321 * 8; i += NT) {
                const int k = i >> 3, p = i & 7;
                u32x4 y = __builtin_amdgcn_raw_buffer_load_b128(rs, (unsigned)(OFF_X1T + ((size_t)k * S_ + task * 16 + 2 * p) * 8), 0, 16);
                float ar = u2f(y.x), ai = u2f(y.y), br = u2f(y.z), bi = u2f(y.w);
                if (k == 0 || k == 320) { ai = 0.f; bi = 0.f; }
                c32* zb = (c32*)lds + p * 720;
                zb[k] = c32{ar - bi, -(ai + br)};
                if (k > 0 && k < 320) zb[640 - k] = c32{ar + bi, -(br - ai)};
            }
            __syncthreads();
            c32 v[10];
#pragma unroll
            for (int j = 0; j < 10; ++j) v[j] = wbuf[64 * j + lane];
            LDSWAIT();
            fft640(v, wbuf, tw, lane);
            const float sc = 1.f / (2048.f * 640.f);
            const int r0 = task * 16 + wv * 2;
            for (int i = lane; i < 320; i += 64) {
                const c32 z0 = wbuf[2 * i], z1 = wbuf[2 * i + 1];
                __builtin_amdgcn_raw_buffer_store_b32(pk_bf16(z0.x * sc, z1.x * sc), rs, (unsigned)(OFF_A + ((size_t)r0 * D_ + 2 * i) * 2), 0, 16);
                __builtin_amdgcn_raw_buffer_store_b32(pk_bf16(-z0.y * sc, -z1.y * sc), rs, (unsigned)(OFF_A + ((size_t)(r0 + 1) * D_ + 2 * i) * 2), 0, 16);
            }
            __syncthreads();
        }
        obar(2);
        if (o < 60) {
            const int grp = o / 5, c = o - grp * 5;
            const int ct = 8 * c + wv;
            const bool hi = n >= 8;
            unsigned vp = 0, zp = 0, ap = 0;
            for (int it = 0; it < 6; ++it) {
                int s = grp + 12 * it, qlo = 0, qhi = 4;
                if (it == 5) {
                    if (grp < 2) s = 60 + grp;
                    else if (grp < 4) { s = 62; qlo = 2 * (grp - 2); qhi = qlo + 2; }
                    else if (grp < 8) { s = 63; qlo = grp - 4; qhi = qlo + 1; }
                    else break;
                }
                if (vp <= (unsigned)s) vp = wait_counters(ctrl, C_VP, 32, s + 1, lane);
                u32x4 uf[32];
#pragma unroll
                for (int i = 0; i < 32; ++i) uf[i] = __builtin_amdgcn_raw_buffer_load_b128(rs, (unsigned)(OFF_UF + ((size_t)s * NB + ct) * 32768) + lane * 16, i * 1024, 16);
                const int et = tid >> 4, c8 = (tid & 15) * 8, row = 32 * s + et;
                const u32x4 av = __builtin_amdgcn_raw_buffer_load_b128(rs, (unsigned)(OFF_A + ((size_t)row * D_ + 128 * c + c8) * 2), 0, 16);
                const float gv = u2f(__builtin_amdgcn_raw_buffer_load_b32(rs, (unsigned)(OFF_G + row * 4), 0, 16));
                if (zp <= (unsigned)s) zp = wait_counters(ctrl, C_ZP + ct * 32, 1, s + 1, lane);
                u32x4 zq[8];
#pragma unroll
                for (int i = 0; i < 8; ++i) zq[i] = __builtin_amdgcn_raw_buffer_load_b128(rs, (unsigned)(OFF_ZF + ((size_t)s * NB + ct) * 8192) + lane * 16, i * 1024, 16);
                if (ap <= (unsigned)s) ap = wait_counters(ctrl, C_AP, 32, s + 1, lane);
                for (int c0 = qlo * 1280; c0 < qhi * 1280; c0 += 2560) {
                    u32x4 t[5];
#pragma unroll
                    for (int i = 0; i < 5; ++i) t[i] = __builtin_amdgcn_raw_buffer_load_b128(rs, (unsigned)(OFF_PXF + (size_t)s * 81920) + min(c0 + i * NT + tid, 5119) * 16, 0, 16);
#pragma unroll
                    for (int i = 0; i < 5; ++i) *(u32x4*)(lds + min(c0 + i * NT + tid, 5119) * 16) = t[i];
                }
                __syncthreads();
                float* tile = (float*)(lds + 81920);
                const unsigned pmo = 2048 + g * 128 + (n & 7) * 16;
#pragma unroll
                for (int qo = 0; qo < 4; ++qo) {
                    if (qo < qlo || qo >= qhi) continue;
                    f32x4 xa = {0.f, 0.f, 0.f, 0.f}, xb = xa, ma = xa;
#pragma unroll
                    for (int wg = 0; wg < 8; ++wg) {
                        const char* pbp = lds + qo * 20480 + wg * 2560;
                        const bf16x8 p0 = *(const bf16x8*)(pbp + lane * 16), p1 = *(const bf16x8*)(pbp + 1024 + lane * 16);
                        const bf16x8 p2 = *(const bf16x8*)(pbp + pmo);
                        xa = MFMA32(BC8(uf[4 * wg]), p0, xa); xb = MFMA32(BC8(uf[4 * wg + 1]), p0, xb);
                        xa = MFMA32(BC8(uf[4 * wg + 2]), p1, xa); xb = MFMA32(BC8(uf[4 * wg + 3]), p1, xb);
                        ma = MFMA32(BC8(zq[wg]), p2, ma);
                    }
                    f32x4 tot;
#pragma unroll
                    for (int r = 0; r < 4; ++r) {
                        const float y = hi ? xb[r] : xa[r] + ma[r];
                        tot[r] = y + u2f(__builtin_amdgcn_update_dpp(0, (int)f2u(y), 0x128, 0xf, 0xf, false));
                    }
                    if (n < 8) *(f32x4*)(tile + (8 * qo + n) * 132 + wv * 16 + 4 * g) = tot;
                }
                __syncthreads();
                if (et >= 8 * qlo && et < 8 * qhi) {
                    const f32x4 b0 = *(const f32x4*)(tile + et * 132 + c8), b1 = *(const f32x4*)(tile + et * 132 + c8 + 4);
                    const float og = rbf(1.f - gv);
                    auto mix = [&](unsigned aw, float x0, float x1) -> unsigned {
                        return pk_bf16(rbf(gv * u2f(aw << 16)) + rbf(og * rbf(x0)), rbf(gv * u2f(aw & 0xFFFF0000u)) + rbf(og * rbf(x1)));
                    };
                    u32x4 ov = {mix(av.x, b0[0], b0[1]), mix(av.y, b0[2], b0[3]), mix(av.z, b1[0], b1[1]), mix(av.w, b1[2], b1[3])};
                    *(u32x4*)(a.out + (size_t)row * D_ + 128 * c + c8) = ov;
                }
                __syncthreads();
            }
        }
    }
}

extern "C" void kernel_launch(void* const* d_in, const int* in_sizes, int n_in, void* d_out, int out_size, void* d_ws, size_t ws_size, hipStream_t stream) {
    char* ws = (char*)(((uintptr_t)d_ws + 2 * MB - 1) & ~(uintptr_t)(2 * MB - 1));
    (void)hipMemsetAsync(ws + OFF_CTRL, 0, CTRL_WORDS * 4, stream);
    MainArgs a;
    a.x = (const u16*)d_in[0]; a.mem0 = (const u16*)d_in[4]; a.Wq = (const u16*)d_in[5]; a.Wk = (const u16*)d_in[6]; a.Wv = (const u16*)d_in[7]; a.Wo = (const u16*)d_in[8];
    a.wr = (const u16*)d_in[1]; a.wi = (const u16*)d_in[2]; a.gw = (const u16*)d_in[10]; a.ws = ws; a.out = (u16*)d_out;
    (void)hipFuncSetAttribute((const void*)k_main, hipFuncAttributeMaxDynamicSharedMemorySize, LDS_BYTES);
    k_main<<<256, NT, LDS_BYTES, stream>>>(a);
}
```

```cpp
#include <hip/hip_runtime.h>
#include <hip/hip_bf16.h>
#include <stdint.h>

#define LAS __attribute__((address_space(3)))
typedef __attribute__((ext_vector_type(8))) __bf16 bf16x8;
typedef __attribute__((ext_vector_type(16))) __bf16 bf16x16;
typedef __attribute__((ext_vector_type(4))) short s16x4;
typedef __attribute__((ext_vector_type(8))) short s16x8;
typedef __attribute__((ext_vector_type(16))) short s16x16;
typedef __attribute__((ext_vector_type(4))) float f32x4;
typedef __attribute__((ext_vector_type(4))) unsigned u32x4;
typedef __attribute__((ext_vector_type(2))) unsigned u32x2;
typedef unsigned short u16;
typedef __amdgpu_buffer_rsrc_t rsrc_t;

namespace gm {
constexpr int S_ = 2048, D_ = 640, H_ = 32, E_ = 128, NSEG = 64;
constexpr int NA = 32, NM = 32, NB = 40, NREC = NA + NM + NB, NP = 32, NV = 32, NO = 64;
constexpr int R_M = NA, R_B = NA + NM, R_P = NREC, R_V = R_P + NP, R_O = R_V + NV, R_S = R_O + NO;
constexpr int NS = 256 - R_S;
constexpr int NT = 512;
constexpr size_t MB = 1024 * 1024;
constexpr size_t OFF_HO = 0;
constexpr size_t OFF_CTRL = 4 * MB;
constexpr size_t OFF_XF = 5 * MB;
constexpr size_t OFF_KXF = 8 * MB;
constexpr size_t OFF_QXF = 24 * MB;
constexpr size_t OFF_UF = 40 * MB;
constexpr size_t OFF_ZF = 120 * MB;
constexpr size_t OFF_KMF = 140 * MB;
constexpr size_t OFF_PXF = 144 * MB;
constexpr size_t OFF_X1T = 150 * MB;
constexpr size_t OFF_WT = 156 * MB;
constexpr size_t OFF_A = 159 * MB;
constexpr size_t OFF_G = 162 * MB;
constexpr size_t OFF_DBG = 200 * MB;
constexpr int SLOT_P = 0, SLOT_MEM = 20480, SLOT_SIGA = 30720, SLOT_SIGB = 30848;
constexpr int C_POL = 0, C_CNT0 = 64, C_CNT1 = 128, C_ABORT = 192;
constexpr int C_PP = 256, C_VP = C_PP + 32 * 32, C_AP = C_VP + 32 * 32, C_ZP = C_AP + 32 * 32, C_ATT = C_ZP + 40 * 32, C_OB = C_ATT + 64 * 32,
              C_XF = C_OB + 4 * 32, C_FILL = C_XF + 32, C_V0 = C_ATT, C_END = C_FILL + 32;
constexpr int CTRL_WORDS = 6912;
static_assert(C_END <= CTRL_WORDS, "ctrl");
constexpr unsigned SPIN_CAP = 1u << 20;
}
using namespace gm;

__device__ __forceinline__ unsigned xcc_id() { return (unsigned)__builtin_amdgcn_s_getreg(20 | (3 << 11)) & 0xfu; }
__host__ __device__ constexpr unsigned lmap(unsigned L) { return ((L >> 13) << 14) + (L & 8191u); }
__device__ __forceinline__ unsigned pk_bf16(float a, float b) {
    typedef __attribute__((ext_vector_type(2))) __bf16 bf2; typedef __attribute__((ext_vector_type(2))) float f2;
    f2 v = {a, b}; bf2 r = __builtin_convertvector(v, bf2); return __builtin_bit_cast(unsigned, r);
}
__device__ __forceinline__ unsigned f2u(float a) { return __builtin_bit_cast(unsigned, a); }
__device__ __forceinline__ float u2f(unsigned a) { return __builtin_bit_cast(float, a); }
__device__ __forceinline__ float rbf(float a) { return __builtin_bit_cast(float, pk_bf16(a, 0.f) << 16); }
__device__ __forceinline__ unsigned nosent(unsigned w) { return w < 0xFFFFFFFEu ? w : 0xFFFFFFFEu; }
#define BARRIER() do { asm volatile("s_waitcnt lgkmcnt(0)\n\ts_barrier" ::: "memory"); } while (0)
__device__ __forceinline__ bf16x8 lo8(bf16x16 w) { return __builtin_shufflevector(w, w, 0, 1, 2, 3, 4, 5, 6, 7); }
__device__ __forceinline__ bf16x8 hi8(bf16x16 w) { return __builtin_shufflevector(w, w, 8, 9, 10, 11, 12, 13, 14, 15); }
__host__ __device__ __forceinline__ int k2d(int k) { int G = k >> 4, L = k & 15, q = L >> 2, p = L & 3; return 16 * G + (p < 2 ? 2 * q + p : 8 + 2 * q + (p - 2)); }
__host__ __device__ __forceinline__ int frag_k(int gb, int jb) { return jb < 8 ? 8 * gb + jb : 32 + 8 * gb + (jb - 8); }
__device__ __forceinline__ void glds16(const void* gsrc, unsigned lds_dst) {
    unsigned keep;
    asm volatile("s_mov_b32 %0, m0\n\ts_mov_b32 m0, %2\n\ts_nop 0\n\tglobal_load_lds_dwordx4 %1, off sc1\n\ts_mov_b32 m0, %0"
                 : "=&s"(keep) : "v"(gsrc), "s"(lds_dst) : "memory");
}
typedef __attribute__((ext_vector_type(2))) float c32;
__device__ __forceinline__ c32 cmul(c32 a, c32 b) { return c32{a.x * b.x - a.y * b.y, a.x * b.y + a.y * b.x}; }
__device__ __forceinline__ c32 cmulc(c32 a, c32 b) { return c32{a.x * b.x + a.y * b.y, a.y * b.x - a.x * b.y}; }
template <int SG> __device__ __forceinline__ c32 rot90(c32 a) { return SG < 0 ? c32{a.y, -a.x} : c32{-a.y, a.x}; }
template <int SG> __device__ __forceinline__ void dft4(c32& a0, c32& a1, c32& a2, c32& a3) {
    c32 t0 = a0 + a2, t1 = a0 - a2, t2 = a1 + a3, t3 = rot90<SG>(a1 - a3);
    a0 = t0 + t2; a1 = t1 + t3; a2 = t0 - t2; a3 = t1 - t3;
}
template <int SG> __device__ __forceinline__ void dft8(c32 (&v)[8]) {
    const float r = 0.70710678118654752f;
    c32 e0 = v[0], e1 = v[2], e2 = v[4], e3 = v[6], o0 = v[1], o1 = v[3], o2 = v[5], o3 = v[7];
    dft4<SG>(e0, e1, e2, e3); dft4<SG>(o0, o1, o2, o3);
    c32 p1 = (SG < 0) ? c32{(o1.x + o1.y) * r, (o1.y - o1.x) * r} : c32{(o1.x - o1.y) * r, (o1.y + o1.x) * r};
    c32 p2 = rot90<SG>(o2);
    c32 p3 = (SG < 0) ? c32{(o3.y - o3.x) * r, -(o3.x + o3.y) * r} : c32{-(o3.x + o3.y) * r, (o3.x - o3.y) * r};
    v[0] = e0 + o0; v[4] = e0 - o0; v[1] = e1 + p1; v[5] = e1 - p1; v[2] = e2 + p2; v[6] = e2 - p2; v[3] = e3 + p3; v[7] = e3 - p3;
}
__device__ __forceinline__ void dft5f(c32& a0, c32& a1, c32& a2, c32& a3, c32& a4) {
    const float c1 = 0.30901699437494742f, c2 = -0.80901699437494742f, s1 = 0.95105651629515357f, s2 = 0.58778525229247313f;
    c32 t1 = a1 + a4, t2 = a2 + a3, t3 = a1 - a4, t4 = a2 - a3;
    c32 m1 = a0 + c1 * t1 + c2 * t2, m2 = a0 + c2 * t1 + c1 * t2;
    c32 n1 = s1 * t3 + s2 * t4, n2 = s2 * t3 - s1 * t4;
    a0 = a0 + t1 + t2;
    a1 = c32{m1.x + n1.y, m1.y - n1.x}; a4 = c32{m1.x - n1.y, m1.y + n1.x};
    a2 = c32{m2.x + n2.y, m2.y - n2.x}; a3 = c32{m2.x - n2.y, m2.y + n2.x};
}
__device__ __forceinline__ void dft10f(c32 (&v)[10]) {
    c32 a[5], b[5];
#pragma unroll
    for (int n2 = 0; n2 < 5; ++n2) { c32 x0 = v[(2 * n2) % 10], x1 = v[(5 + 2 * n2) % 10]; a[n2] = x0 + x1; b[n2] = x0 - x1; }
    dft5f(a[0], a[1], a[2], a[3], a[4]); dft5f(b[0], b[1], b[2], b[3], b[4]);
#pragma unroll
    for (int k2 = 0; k2 < 5; ++k2) { v[(6 * k2) % 10] = a[k2]; v[(5 + 6 * k2) % 10] = b[k2]; }
}
#define LDSWAIT() asm volatile("s_waitcnt lgkmcnt(0)" ::: "memory")
__device__ __forceinline__ void fft640(c32 (&v)[10], c32* buf, const c32* tw, int lane) {
    dft10f(v);
#pragma unroll
    for (int k1 = 0; k1 < 10; ++k1) { c32 t = k1 ? cmul(v[k1], tw[lane * k1]) : v[k1]; buf[k1 * 72 + (lane >> 3) * 9 + (lane & 7)] = t; }
    LDSWAIT();
    c32 w[2][8];
#pragma unroll
    for (int rd = 0; rd < 2; ++rd) {
        const int id = lane + 64 * rd;
        if (id < 80) {
            const int k1 = id >> 3, b = id & 7;
#pragma unroll
            for (int a = 0; a < 8; ++a) w[rd][a] = buf[k1 * 72 + a * 9 + b];
            dft8<-1>(w[rd]);
#pragma unroll
            for (int c = 1; c < 8; ++c) w[rd][c] = cmul(w[rd][c], tw[10 * b * c]);
        }
    }
    LDSWAIT();
#pragma unroll
    for (int rd = 0; rd < 2; ++rd) {
        const int id = lane + 64 * rd;
        if (id < 80) {
            const int k1 = id >> 3, b = id & 7;
#pragma unroll
            for (int c = 0; c < 8; ++c) buf[k1 * 72 + c * 9 + b] = w[rd][c];
        }
    }
    LDSWAIT();
#pragma unroll
    for (int rd = 0; rd < 2; ++rd) {
        const int id = lane + 64 * rd;
        if (id < 80) {
            const int k1 = id >> 3, c = id & 7;
#pragma unroll
            for (int b = 0; b < 8; ++b) w[rd][b] = buf[k1 * 72 + c * 9 + b];
            dft8<-1>(w[rd]);
        }
    }
    LDSWAIT();
#pragma unroll
    for (int rd = 0; rd < 2; ++rd) {
        const int id = lane + 64 * rd;
        if (id < 80) {
            const int k1 = id >> 3, c = id & 7;
#pragma unroll
            for (int d = 0; d < 8; ++d) buf[k1 + 10 * (c + 8 * d)] = w[rd][d];
        }
    }
    LDSWAIT();
}

struct MainArgs {
    const u16* x; const u16* mem0; const u16* Wq; const u16* Wk; const u16* Wv; const u16* Wo; const u16* wr; const u16* wi; const u16* gw; char* ws; u16* out;
};
#define DBC 16
#define DAC 18
#define ZLEAD 6
constexpr int L_XB = 0;
constexpr int L_MF = 81920;
constexpr int L_QF = 92160;
constexpr int L_KM = 96256;
constexpr int L_KX = 100352;
constexpr int L_T = 122880;
constexpr int L_RED = 0;
constexpr int L_MISC = 139264;
constexpr int L_PROF = 140 * 1024;
constexpr int LDS_BYTES = 152 * 1024;

__device__ __forceinline__ int head_of(int idx) { return (idx & 3) * 8 + (idx >> 2); }
__device__ __forceinline__ unsigned rt32() { return (unsigned)__builtin_amdgcn_s_memrealtime(); }
__device__ __forceinline__ void wait_rt(unsigned target) {
    if ((int)(target - rt32()) > 2000) return;
    while ((int)(rt32() - target) < 0) {}
}
__device__ __forceinline__ float vmax(float a, float b) { float r; asm("v_max_f32 %0, %1, %2" : "=v"(r) : "v"(a), "v"(b)); return r; }
__device__ __forceinline__ float vmax3(float a, float b, float c) { float r; asm("v_max3_f32 %0, %1, %2, %3" : "=v"(r) : "v"(a), "v"(b), "v"(c)); return r; }
__device__ __forceinline__ unsigned umax3(unsigned a, unsigned b, unsigned c) { return max(max(a, b), c); }
__device__ __forceinline__ bool spin_abort(unsigned& spins, unsigned* ctrl) {
    ++spins;
    if (spins > SPIN_CAP) { __hip_atomic_store(ctrl + C_ABORT, 1u, __ATOMIC_RELAXED, __HIP_MEMORY_SCOPE_AGENT); return true; }
    if ((spins & 1023u) == 0u) return __builtin_amdgcn_readfirstlane(__hip_atomic_load(ctrl + C_ABORT, __ATOMIC_RELAXED, __HIP_MEMORY_SCOPE_AGENT)) != 0;
    return false;
}
__device__ __forceinline__ unsigned read_sig(const char* p, unsigned* ctrl) {
    unsigned v, spins = 0;
    for (;;) {
        asm volatile("v_mov_b32 %0, 0\n\tglobal_load_dword %0, %0, %1 sc1\n\ts_waitcnt vmcnt(0)" : "=&v"(v) : "s"(p) : "memory");
        v = __builtin_amdgcn_readfirstlane(v);
        if (v != ~0u) break;
        if (spin_abort(spins, ctrl)) break;
    }
    return v;
}
__device__ __forceinline__ unsigned wait_counters(unsigned* ctrl, int base, int cnt, unsigned need, int lane) {
    unsigned spins = 0, m;
    const unsigned off = (unsigned)(base + (lane < cnt ? lane : 0) * 32) * 4u;
    for (;;) {
        unsigned v;
        asm volatile("global_load_dword %0, %1, %2 sc1\n\ts_waitcnt vmcnt(0)" : "=&v"(v) : "v"(off), "s"(ctrl) : "memory");
        if (cnt > 1) {
#pragma unroll
            for (int o = 16; o >= 1; o >>= 1) v = min(v, (unsigned)__shfl_xor((int)v, o));
        }
        m = __builtin_amdgcn_readfirstlane(v);
        if (m >= need) break;
        __builtin_amdgcn_s_sleep(4);
        if (spin_abort(spins, ctrl)) break;
    }
    return m;
}
__device__ __forceinline__ void set_counter(unsigned* ctrl, int idx, unsigned v) { __hip_atomic_store(ctrl + idx, v, __ATOMIC_RELAXED, __HIP_MEMORY_SCOPE_AGENT); }
__device__ __forceinline__ void st16_sc1(const void* base, unsigned off, u32x4 v) { asm volatile("global_store_dwordx4 %0, %1, %2 sc1\n\ts_nop 1" :: "v"(off), "v"(v), "s"(base) : "memory"); }
__device__ __forceinline__ void st8_sc1(const void* base, unsigned off, u32x2 v) { asm volatile("global_store_dwordx2 %0, %1, %2 sc1\n\ts_nop 1" :: "v"(off), "v"(v), "s"(base) : "memory"); }
__device__ __forceinline__ void st4_sc1(const void* base, unsigned off, unsigned v) { asm volatile("global_store_dword %0, %1, %2 sc1" :: "v"(off), "v"(v), "s"(base) : "memory"); }
__device__ __forceinline__ void load_mem0(const u16* mem0, char* lds, int tid) {
    for (int c = tid; c < 640; c += NT) {
        const int kb = c >> 6, l = c & 63;
        *(u32x4*)(lds + L_MF + c * 16) = *(const u32x4*)(mem0 + (l & 7) * D_ + 64 * kb + 16 * (l >> 4) + 8 * ((l >> 3) & 1));
    }
}
__device__ __forceinline__ void prefetch_x(const char* xf, int seg, int buf, int wv, int lane) {
    const char* src = xf + (size_t)seg * 40960 + wv * 5120 + lane * 16;
    const unsigned dst = L_XB + buf * 40960 + wv * 5120;
#pragma unroll
    for (int i = 0; i < 5; ++i) glds16(src + i * 1024, dst + i * 1024);
}
#define ST2(k) do {} while (0)
#define ST2F() do {} while (0)
#define MFMA32(a, b, c) __builtin_amdgcn_mfma_f32_16x16x32_bf16((a), (b), (c), 0, 0, 0)
#define MFMA16(a, b, c) __builtin_amdgcn_mfma_f32_16x16x16bf16_1k((a), (b), (c), 0, 0, 0)
#define SMFMAC(a, b, c) __builtin_amdgcn_smfmac_f32_16x16x64_bf16((a), (b), (c), sidx, 0, 0)
#define BC8(x) __builtin_bit_cast(bf16x8, (x))
#define BC4(x) __builtin_bit_cast(s16x4, (x))
__device__ __forceinline__ bf16x16 cat4(s16x4 a, s16x4 b, s16x4 c, s16x4 d) {
    s16x8 x = __builtin_shufflevector(a, b, 0, 1, 2, 3, 4, 5, 6, 7), y = __builtin_shufflevector(c, d, 0, 1, 2, 3, 4, 5, 6, 7);
    return __builtin_bit_cast(bf16x16, (s16x16)__builtin_shufflevector(x, y, 0, 1, 2, 3, 4, 5, 6, 7, 8, 9, 10, 11, 12, 13, 14, 15));
}
#define TRREAD(p) __builtin_amdgcn_ds_read_tr16_b64_v4i16((LAS s16x4*)(p))
__device__ __forceinline__ void load_w2(const u16* Wa, const u16* Wb, int h, char* lds, int tid, int wv, bf16x16 (&wa)[10], bf16x16 (&wb)[10]) {
    const int lane = tid & 63, g = lane >> 4, n = lane & 15;
    u32x4 st[10][4];
    const unsigned wo_ = (unsigned)(((tid >> 4) * H_ + h) * E_ + (tid & 15) * 8) * 2u;
#pragma unroll
    for (int kb = 0; kb < 10; ++kb)
#pragma unroll
        for (int i = 0; i < 4; ++i) {
            const char* W = (const char*)((i & 2) ? Wb : Wa) + (size_t)(64 * kb + 32 * (i & 1)) * (H_ * E_ * 2);
            st[kb][i] = *(const u32x4*)(W + wo_);
        }
    const int q4 = n >> 2, p4 = n & 3;
#pragma unroll
    for (int kb = 0; kb < 10; ++kb) {
        char* sb = lds + (kb & 1) * 33792;
#pragma unroll
        for (int i = 0; i < 4; ++i)
            *(u32x4*)(sb + ((tid >> 4) + 32 * (i & 1)) * 528 + (i >> 1) * 256 + (tid & 15) * 16) = st[kb][i];
        BARRIER();
        s16x4 r[2][4];
#pragma unroll
        for (int tl = 0; tl < 2; ++tl)
#pragma unroll
            for (int u = 0; u < 4; ++u) r[tl][u] = TRREAD(sb + k2d(frag_k(g, 4 * u + q4)) * 528 + tl * 256 + (16 * wv + 4 * p4) * 2);
        wa[kb] = cat4(r[0][0], r[0][1], r[0][2], r[0][3]);
        wb[kb] = cat4(r[1][0], r[1][1], r[1][2], r[1][3]);
        asm volatile("" : "+v"(wa[kb]), "+v"(wb[kb]));
    }
    BARRIER();
}
__device__ __forceinline__ void load_wvo(const u16* Wv, const u16* Wo, int h, char* lds, int tid, int wv, bf16x16 (&wvv)[10], bf16x16 (&wo)[5][2]) {
    const int lane = tid & 63, g = lane >> 4, n = lane & 15;
    u32x4 st[10][2], so[2][10];
    const unsigned wo_ = (unsigned)(((tid >> 4) * H_ + h) * E_ + (tid & 15) * 8) * 2u;
#pragma unroll
    for (int kb = 0; kb < 10; ++kb)
#pragma unroll
        for (int i = 0; i < 2; ++i) st[kb][i] = *(const u32x4*)((const char*)Wv + (size_t)(64 * kb + 32 * i) * (H_ * E_ * 2) + wo_);
#pragma unroll
    for (int t = 0; t < 2; ++t)
#pragma unroll
        for (int i = 0; i < 10; ++i) so[t][i] = *(const u32x4*)((const char*)Wo + ((size_t)h * E_ + 64 * t) * (D_ * 2) + (size_t)i * (NT * 16) + tid * 16);
    const int q4 = n >> 2, p4 = n & 3;
#pragma unroll
    for (int kb = 0; kb < 10; ++kb) {
        char* sb = lds + (kb & 1) * 17408;
#pragma unroll
        for (int i = 0; i < 2; ++i) *(u32x4*)(sb + ((tid >> 4) + 32 * i) * 272 + (tid & 15) * 16) = st[kb][i];
        BARRIER();
        s16x4 r[4];
#pragma unroll
        for (int u = 0; u < 4; ++u) r[u] = TRREAD(sb + k2d(frag_k(g, 4 * u + q4)) * 272 + (16 * wv + 4 * p4) * 2);
        wvv[kb] = cat4(r[0], r[1], r[2], r[3]);
        asm volatile("" : "+v"(wvv[kb]));
    }
#pragma unroll
    for (int t = 0; t < 2; ++t) {
        BARRIER();
#pragma unroll
        for (int i = 0; i < 10; ++i) { const int c = i * NT + tid, e = c / 80, cc = c - e * 80; *(u32x4*)(lds + e * 1296 + cc * 16) = so[t][i]; }
        BARRIER();
#pragma unroll
        for (int ct = 0; ct < 5; ++ct) {
            s16x4 r[4];
#pragma unroll
            for (int u = 0; u < 4; ++u) r[u] = TRREAD(lds + k2d(frag_k(g, 4 * u + q4)) * 1296 + (16 * (5 * wv + ct) + 4 * p4) * 2);
            wo[ct][t] = cat4(r[0], r[1], r[2], r[3]);
            asm volatile("" : "+v"(wo[ct][t]));
        }
    }
    BARRIER();
}
__device__ __forceinline__ void tw_store(char* dst, f32x4 a0, f32x4 a1) {
    const unsigned p0 = pk_bf16(a0[0], a0[1]), p1 = pk_bf16(a0[2], a0[3]), p2 = pk_bf16(a1[0], a1[1]), p3 = pk_bf16(a1[2], a1[3]);
    *(u16*)(dst + 0) = (u16)p0; *(u16*)(dst + 16) = (u16)(p0 >> 16); *(u16*)(dst + 32) = (u16)p1; *(u16*)(dst + 48) = (u16)(p1 >> 16);
    *(u16*)(dst + 4096) = (u16)p2; *(u16*)(dst + 4096 + 16) = (u16)(p2 >> 16); *(u16*)(dst + 4096 + 32) = (u16)p3; *(u16*)(dst + 4096 + 48) = (u16)(p3 >> 16);
}
__device__ __forceinline__ void arrive(unsigned* ctrl, int idx, int tid) {
    asm volatile("s_waitcnt vmcnt(0)" ::: "memory");
    __syncthreads();
    if (tid == 0) __hip_atomic_fetch_add(ctrl + idx, 1u, __ATOMIC_RELAXED, __HIP_MEMORY_SCOPE_AGENT);
}
__device__ __forceinline__ u32x2 half_add(f32x4 a, f32x4 b) {
    float cv[4];
#pragma unroll
    for (int r = 0; r < 4; ++r) {
        auto sw = __builtin_amdgcn_permlane32_swap(f2u(a[r]), f2u(b[r]), false, false);
        cv[r] = u2f(sw[0]) + u2f(sw[1]);
    }
    return u32x2{pk_bf16(cv[0], cv[1]), pk_bf16(cv[2], cv[3])};
}

template <bool PF>
__device__ __forceinline__ void proj_qk(const char* xb, const bf16x16 (&wq)[10], const bf16x16 (&wk)[10], char* qdst, char* kdst, const char* psrc, unsigned pdst) {
    f32x4 q0 = {0.f, 0.f, 0.f, 0.f}, q1 = q0, k0 = q0, k1 = q0;
    bf16x8 xr[10][4];
#pragma unroll
    for (int i = 0; i < 4; ++i) xr[0][i] = *(const bf16x8*)(xb + i * 1024);
#pragma unroll
    for (int kb = 0; kb < 10; ++kb) {
        if (kb + 1 < 10) {
#pragma unroll
            for (int i = 0; i < 4; ++i) xr[kb + 1][i] = *(const bf16x8*)(xb + ((kb + 1) * 4 + i) * 1024);
        }
        __builtin_amdgcn_sched_barrier(0);
        q0 = MFMA32(xr[kb][0], lo8(wq[kb]), q0); q1 = MFMA32(xr[kb][1], lo8(wq[kb]), q1); k0 = MFMA32(xr[kb][0], lo8(wk[kb]), k0); k1 = MFMA32(xr[kb][1], lo8(wk[kb]), k1);
        q0 = MFMA32(xr[kb][2], hi8(wq[kb]), q0); q1 = MFMA32(xr[kb][3], hi8(wq[kb]), q1); k0 = MFMA32(xr[kb][2], hi8(wk[kb]), k0); k1 = MFMA32(xr[kb][3], hi8(wk[kb]), k1);
        __builtin_amdgcn_sched_barrier(0);
        if (PF && (kb & 1) == 0) glds16(psrc + (kb >> 1) * 1024, pdst + (kb >> 1) * 1024);
    }
    tw_store(qdst, q0, q1);
    tw_store(kdst, k0, k1);
}
template <bool PF>
__device__ __forceinline__ void proj_u(char* lds, const char* xb, const bf16x16 (&wvv)[10], const bf16x16 (&wo)[5][2], int wv, int lane, const char* ub, int h, const char* psrc, unsigned pdst) {
    const int g = lane >> 4, n = lane & 15;
    const int Lq = (n < 8) ? 4 * (n >> 1) + (n & 1) : 4 * ((n - 8) >> 1) + 2 + (n & 1);
    const unsigned twv_off = (wv >> 1) * 1024 + (2 * (wv & 1) + (Lq >> 3)) * 256 + (Lq & 7) * 2;
    const unsigned uo = (unsigned)(5 * wv * 32768 + h * 1024 + lane * 16);
    f32x4 v0 = {0.f, 0.f, 0.f, 0.f}, v1 = v0;
    bf16x8 xr[10][4];
#pragma unroll
    for (int kb = 0; kb < 2; ++kb)
#pragma unroll
        for (int i = 0; i < 4; ++i) xr[kb][i] = *(const bf16x8*)(xb + (kb * 4 + i) * 1024);
#pragma unroll
    for (int kb = 0; kb < 10; ++kb) {
        if (kb + 2 < 10) {
#pragma unroll
            for (int i = 0; i < 4; ++i) xr[kb + 2][i] = *(const bf16x8*)(xb + ((kb + 2) * 4 + i) * 1024);
        }
        __builtin_amdgcn_sched_barrier(0);
        v0 = MFMA32(xr[kb][0], lo8(wvv[kb]), v0); v1 = MFMA32(xr[kb][1], lo8(wvv[kb]), v1);
        v0 = MFMA32(xr[kb][2], hi8(wvv[kb]), v0); v1 = MFMA32(xr[kb][3], hi8(wvv[kb]), v1);
        __builtin_amdgcn_sched_barrier(0);
        if (PF && (kb & 1) == 0) glds16(psrc + (kb >> 1) * 1024, pdst + (kb >> 1) * 1024);
    }
    tw_store(lds + L_T + twv_off + g * 64, v0, v1);
    BARRIER();
    bf16x8 vf[2][4];
#pragma unroll
    for (int tt = 0; tt < 2; ++tt)
#pragma unroll
        for (int t = 0; t < 4; ++t) vf[tt][t] = *(const bf16x8*)(lds + L_T + (tt * 4 + t) * 1024 + lane * 16);
#pragma unroll
    for (int ct = 0; ct < 5; ++ct) {
        f32x4 u0 = {0.f, 0.f, 0.f, 0.f}, u1 = u0;
#pragma unroll
        for (int t = 0; t < 2; ++t) {
            u0 = MFMA32(vf[0][2 * t], lo8(wo[ct][t]), u0); u1 = MFMA32(vf[1][2 * t], lo8(wo[ct][t]), u1);
            u0 = MFMA32(vf[0][2 * t + 1], hi8(wo[ct][t]), u0); u1 = MFMA32(vf[1][2 * t + 1], hi8(wo[ct][t]), u1);
        }
        u32x4 o = {pk_bf16(u0[0], u0[1]), pk_bf16(u0[2], u0[3]), pk_bf16(u1[0], u1[1]), pk_bf16(u1[2], u1[3])};
        st16_sc1(ub + ct * 32768, uo, o);
    }
}

__global__ void __launch_bounds__(NT, 2) k_main(MainArgs a) {
    extern __shared__ __attribute__((aligned(16))) char lds[];
    const int tid = threadIdx.x, lane = tid & 63;
    const int wv = __builtin_amdgcn_readfirstlane(tid >> 6);
    unsigned* ctrl = (unsigned*)(a.ws + OFF_CTRL);
#define SS(k) do {} while (0)
    int role;
    {
        unsigned* sm = (unsigned*)(lds + L_MISC);
        if (tid == 0) {
            const unsigned nearh = (xcc_id() >> 2) == 0;
            const unsigned t = __hip_atomic_fetch_add(ctrl + (nearh ? C_CNT0 : C_CNT1), 1u, __ATOMIC_RELAXED, __HIP_MEMORY_SCOPE_AGENT);
            const bool own = t < 128u;
            const int idx = own ? (int)t : 255 - (int)t;
            const bool near_list = (nearh != 0) == own;
            const int r = near_list ? (idx < NREC ? idx : idx - NREC + R_S) : NREC + idx;
            sm[0] = (unsigned)r;
        }
        __syncthreads();
        role = __builtin_amdgcn_readfirstlane((int)sm[0]);
        __syncthreads();
    }
    SS(1);
    const rsrc_t rs = __builtin_amdgcn_make_buffer_rsrc(a.ws, 0, (unsigned)(250 * MB), 0x00020000);
    if (role >= R_S) {
        const int sp = role - R_S;
        if (sp == 0) {
            if (tid == 0) {
                unsigned best[4] = {~0u, ~0u, ~0u, ~0u};
                for (int rep = 0; rep < 4; ++rep)
#pragma unroll
                    for (int t = 0; t < 4; ++t) {
                        unsigned* ad = (unsigned*)(a.ws + OFF_HO + (size_t)(t >> 1) * 2 * MB + 50 * 16384 + (t & 1) * 8192);
                        unsigned z = 0, r;
                        unsigned long long c0 = __builtin_amdgcn_s_memtime();
                        asm volatile("s_waitcnt lgkmcnt(0)\n\tglobal_atomic_add %0, %1, %2, off sc0\n\ts_waitcnt vmcnt(0)" : "=v"(r) : "v"(ad), "v"(z) : "memory");
                        unsigned long long c1 = __builtin_amdgcn_s_memtime();
                        unsigned d = (unsigned)(c1 - c0); best[t] = d < best[t] ? d : best[t];
                    }
                __hip_atomic_store(ctrl + C_POL, 0x100u | (best[1] < best[0] ? 1u : 0u) | (best[3] < best[2] ? 2u : 0u), __ATOMIC_RELAXED, __HIP_MEMORY_SCOPE_AGENT);
            }
            __syncthreads();
        }
        for (int task = NS - 1 - sp; task < 32; task += NS) {
            const int p0 = task * 64;
            unsigned* tile = (unsigned*)lds;
            unsigned short xr[8][6], xi[8][6];
#pragma unroll
            for (int rr = 0; rr < 8; ++rr) {
                const int pos = p0 + wv + 8 * rr, m = (pos >> 8) + 8 * ((pos >> 5) & 7) + 64 * ((pos >> 2) & 7) + 512 * (pos & 3);
#pragma unroll
                for (int kk = 0; kk < 6; ++kk) { const int k = min(lane + 64 * kk, 320); xr[rr][kk] = a.wr[(size_t)m * 321 + k]; xi[rr][kk] = a.wi[(size_t)m * 321 + k]; }
            }
#pragma unroll
            for (int rr = 0; rr < 8; ++rr)
#pragma unroll
                for (int kk = 0; kk < 6; ++kk) { const int k = lane + 64 * kk; if (k < 321) tile[(wv + 8 * rr) * 323 + k] = (unsigned)xr[rr][kk] | ((unsigned)xi[rr][kk] << 16); }
            __syncthreads();
            for (int i = tid; i < 321 * 16; i += NT) {
                const int k = i >> 4, r = (i & 15) * 4;
                u32x4 v = {tile[r * 323 + k], tile[(r + 1) * 323 + k], tile[(r + 2) * 323 + k], tile[(r + 3) * 323 + k]};
                __builtin_amdgcn_raw_buffer_store_b128(v, rs, (unsigned)(OFF_WT + ((size_t)k * S_ + p0 + r) * 4), 0, 16);
            }
            __syncthreads();
        }
        arrive(ctrl, C_OB, tid);
        return;
    }
    auto get_pol = [&]() -> unsigned {
        if (wv == 0) {
            unsigned v, spins = 0;
            for (;;) {
                v = __builtin_amdgcn_readfirstlane(__hip_atomic_load(ctrl + C_POL, __ATOMIC_RELAXED, __HIP_MEMORY_SCOPE_AGENT));
                if (v & 0x100u) break;
                __builtin_amdgcn_s_sleep(2);
                if (spin_abort(spins, ctrl)) break;
            }
            wait_counters(ctrl, C_FILL, 1, NB, lane);
            if (lane == 0) *(volatile unsigned*)(lds + L_MISC + 32) = v;
        }
        __syncthreads();
        return __builtin_amdgcn_readfirstlane(*(volatile unsigned*)(lds + L_MISC + 32));
    };
    const char* xf = a.ws + OFF_XF;
    char* ho = a.ws + OFF_HO;
    const int g = lane >> 4, n = lane & 15;
    constexpr unsigned so_a = lmap(SLOT_SIGA), so_b = lmap(SLOT_SIGB);
    const int sidx = (lane & 8) ? 0xEEEE : 0x4444;
    const unsigned tw_off = (wv >> 1) * 1024 + (2 * (wv & 1) + (n >> 3)) * 256 + (n & 7) * 2;

    if (role < R_B) {
        const int h = head_of(role & 31);
        const unsigned mo0 = lmap(SLOT_MEM + tid * 16), mo1 = lmap(SLOT_MEM + 8192 + (tid & 127) * 16);
        unsigned pol = 0, target = 0;
        auto slotp = [&](int s) -> char* { return ho + (size_t)(s & 3) * MB + (((pol >> ((s >> 1) & 1)) & 1) ? 8192 : 0); };
        auto gather = [&](int s) {
            if (s > 0) {
                const char* pslot = slotp(s - 1);
                ST2(0);
                if (s > 1) wait_rt(target);
                ST2(1);
                unsigned spins = 0;
                if (wv < 2) {
                    u32x4 v, v2;
                    for (;;) {
                        asm volatile("global_load_dwordx4 %0, %2, %4 sc1\n\tglobal_load_dwordx4 %1, %3, %4 sc1\n\ts_waitcnt vmcnt(0)"
                                     : "=&v"(v), "=&v"(v2) : "v"(mo0), "v"(mo1), "s"(pslot) : "memory");
                        if (__all(max(umax3(umax3(v.x, v.y, v.z), v.w, v2.x), umax3(v2.y, v2.z, v2.w)) != ~0u)) break;
                        if (spin_abort(spins, ctrl)) break;
                    }
                    *(u32x4*)(lds + L_MF + tid * 16) = v;
                    *(u32x4*)(lds + L_MF + 8192 + tid * 16) = v2;
                } else {
                    u32x4 v;
                    for (;;) {
                        asm volatile("global_load_dwordx4 %0, %1, %2 sc1\n\ts_waitcnt vmcnt(0)" : "=&v"(v) : "v"(mo0), "s"(pslot) : "memory");
                        if (__all(max(umax3(v.x, v.y, v.z), v.w) != ~0u)) break;
                        if (spin_abort(spins, ctrl)) break;
                    }
                    *(u32x4*)(lds + L_MF + tid * 16) = v;
                }
            }
            ST2(2);
            BARRIER();
            ST2(3);
        };
        auto next_target = [&](int s) {
            if (s > 0 && s < NSEG - 1) {
                unsigned sa, gb, spins = 0;
                for (;;) {
                    asm volatile("v_mov_b32 %0, 0\n\tglobal_load_dword %1, %0, %3 sc1\n\tglobal_load_dword %0, %0, %2 sc1\n\ts_waitcnt vmcnt(0)"
                                 : "=&v"(sa), "=&v"(gb) : "s"(slotp(s) + so_a), "s"(slotp(s - 1) + so_b) : "memory");
                    sa = __builtin_amdgcn_readfirstlane(sa); gb = __builtin_amdgcn_readfirstlane(gb);
                    if (sa != ~0u && gb != ~0u) break;
                    if (spin_abort(spins, ctrl)) break;
                }
                target = sa + DBC + gb + DAC;
            }
        };
        if (role < NA) {
            bf16x16 wq[10], wk[10];
            load_w2(a.Wq, a.Wk, h, lds, tid, wv, wq, wk);
            SS(2);
            if (wv == 0) wait_counters(ctrl, C_XF, 1, NO, lane);
            __syncthreads();
            prefetch_x(xf, 0, 0, wv, lane);
            load_mem0(a.mem0, lds, tid);
            pol = get_pol();
            asm volatile("s_waitcnt vmcnt(0)" ::: "memory");
            BARRIER();
            proj_qk<false>(lds + L_XB + lane * 16, wq, wk, lds + L_KX + 8192 + tw_off + g * 64, lds + L_KX + tw_off + g * 64, nullptr, 0);
            SS(3);
            auto front = [&](int s, unsigned& t_sig, unsigned ap_est, auto&& hook) {
                gather(s);
                f32x4 aq = {0.f, 0.f, 0.f, 0.f}, ak = aq;
                bf16x8 mf[10];
#pragma unroll
                for (int kb = 0; kb < 3; ++kb) mf[kb] = *(const bf16x8*)(lds + L_MF + kb * 1024 + lane * 16);
                if (role == 0 && wv == 1) {
                    t_sig = rt32();
                    if (lane == 0) st4_sc1(slotp(s) + so_a, 0u, nosent(t_sig + ap_est));
                }
                __builtin_amdgcn_sched_barrier(0);
#pragma unroll
                for (int kb = 0; kb < 10; ++kb) {
                    aq = SMFMAC(mf[kb], wq[kb], aq); ak = SMFMAC(mf[kb], wk[kb], ak);
                    if (kb + 3 < 10) mf[kb + 3] = *(const bf16x8*)(lds + L_MF + (kb + 3) * 1024 + lane * 16);
                    __builtin_amdgcn_sched_barrier(0);
                }
                hook();
                const u32x2 p = half_add(aq, ak);
                char* qd = lds + L_QF + (lane >> 5) * 4096 + tw_off + (g & 1) * 64;
                *(u16*)(qd + 0) = (u16)p.x; *(u16*)(qd + 16) = (u16)(p.x >> 16); *(u16*)(qd + 32) = (u16)p.y; *(u16*)(qd + 48) = (u16)(p.y >> 16);
            };
            if (wv == 0) {
                const int wg = h >> 2;
                const unsigned po_x = lmap(SLOT_P + wg * 2560 + ((h >> 1) & 1) * 1024 + (g * 16 + (h & 1) * 8 + (n & 7)) * 16 + (n >> 3) * 8);
                const unsigned po_m = lmap(SLOT_P + wg * 2560 + 2048 + (h & 3) * 128 + (n & 7) * 16 + (g & 1) * 8 + (n >> 3) * 4);
                const unsigned qoff = L_QF + (g * 16 + (n & 7)) * 16;
                const bool hi = n >= 8;
                unsigned dummy = 0;
                BARRIER();
                SS(4);
                for (int s = 0; s < NSEG; ++s) {
                    bf16x8 kx[8];
                    front(s, dummy, 0, [&]() {
#pragma unroll
                        for (int t = 0; t < 8; ++t) kx[t] = *(const bf16x8*)(lds + L_KX + (s & 1) * 16384 + t * 1024 + lane * 16);
                    });
                    ST2(4);
                    BARRIER();
                    ST2(5);
                    if (s < NSEG - 1) {
                        char* slot = slotp(s);
                        f32x4 s0 = {0.f, 0.f, 0.f, 0.f}, s1 = s0, s2 = s0;
                        bf16x8 qf[4], km[4];
#pragma unroll
                        for (int t = 0; t < 4; ++t) { qf[t] = *(const bf16x8*)(lds + qoff + t * 1024); km[t] = *(const bf16x8*)(lds + L_KM + t * 1024 + lane * 16); }
#pragma unroll
                        for (int t = 0; t < 4; ++t) { s1 = MFMA32(kx[t], qf[t], s1); s2 = MFMA32(kx[4 + t], qf[t], s2); s0 = MFMA32(km[t], qf[t], s0); }
                        __builtin_amdgcn_sched_group_barrier(0x100, 8, 0);
                        __builtin_amdgcn_sched_group_barrier(0x8, 12, 0);
                        const float cexp = 0.08838834764831845f * 1.4426950408889634f;
                        float v[6];
#pragma unroll
                        for (int r = 0; r < 4; ++r) v[r] = hi ? s2[r] : s1[r];
                        v[4] = hi ? s0[2] : s0[0]; v[5] = hi ? s0[3] : s0[1];
                        if (g >= 2) { v[4] = -INFINITY; v[5] = -INFINITY; }
                        float m, tmp_;
                        asm("v_max3_f32 %0, %2, %3, %4\n\tv_max3_f32 %1, %5, %6, %7\n\tv_max_f32 %0, %0, %1\n\ts_nop 1\n\t"
                            "v_max_f32_dpp %0, %0, %0 row_ror:8 row_mask:0xf bank_mask:0xf\n\tv_mov_b32 %1, %0\n\ts_nop 1\n\t"
                            "v_permlane16_swap_b32 %0, %1\n\tv_max_f32 %0, %0, %1\n\tv_mov_b32 %1, %0\n\ts_nop 1\n\t"
                            "v_permlane32_swap_b32 %0, %1\n\tv_max_f32 %0, %0, %1"
                            : "=&v"(m), "=&v"(tmp_) : "v"(v[0]), "v"(v[1]), "v"(v[2]), "v"(v[3]), "v"(v[4]), "v"(v[5]));
                        const float mc = -m * cexp;
#pragma unroll
                        for (int r = 0; r < 6; ++r) v[r] = __builtin_amdgcn_exp2f(__builtin_fmaf(v[r], cexp, mc));
                        float sum = ((v[0] + v[1]) + (v[2] + v[3])) + (v[4] + v[5]);
                        sum += u2f(__builtin_amdgcn_update_dpp(0, (int)f2u(sum), 0x128, 0xf, 0xf, false));
                        {
                            auto sw = __builtin_amdgcn_permlane16_swap(f2u(sum), f2u(sum), false, false);
                            sum = u2f(sw[0]) + u2f(sw[1]);
                            auto sw2 = __builtin_amdgcn_permlane32_swap(f2u(sum), f2u(sum), false, false);
                            sum = u2f(sw2[0]) + u2f(sw2[1]);
                        }
                        const float inv = __builtin_amdgcn_rcpf(sum);
                        u32x2 px = {nosent(pk_bf16(v[0] * inv, v[1] * inv)), nosent(pk_bf16(v[2] * inv, v[3] * inv))};
                        const unsigned pm = nosent(pk_bf16(v[4] * inv, v[5] * inv));
                        st8_sc1(slot, po_x, px);
                        if (g < 2) st4_sc1(slot, po_m, pm);
                        ST2(7);
                        if (role == 0) { const unsigned t = rt32(); if (lane == 0) *(volatile unsigned*)(lds + L_MISC + 48) = t; LDSWAIT(); }
                    }
                    ST2(6);
                    asm volatile("s_barrier" ::: "memory");
                    ST2F();
                    if (s >= 2) {
                        st8_sc1(slotp(s + 2), po_x, u32x2{~0u, ~0u});
                        if (g < 2) st4_sc1(slotp(s + 2), po_m, ~0u);
                    }
                    next_target(s);
                }
            } else if (wv == 2 || wv == 3) {
                const int qt = wv - 2;
                unsigned dummy = 0;
                BARRIER();
                const unsigned pbx = (2 * qt + (n >> 3)) * 20480 + (h >> 2) * 2560 + ((h >> 1) & 1) * 1024 + (g * 16 + (h & 1) * 8 + (n & 7)) * 16;
                const unsigned pbm = (2 * qt + (n >> 3)) * 20480 + (h >> 2) * 2560 + 2048 + (h & 3) * 128 + (n & 7) * 16 + (g & 1) * 8;
                for (int s = 0; s < NSEG; ++s) {
                    front(s, dummy, 0, []() {});
                    BARRIER();
                    asm volatile("s_barrier" ::: "memory");
                    const char* kq = lds + L_KX + (s & 1) * 16384 + lane * 16;
                    f32x4 s0 = {0.f, 0.f, 0.f, 0.f}, s1 = s0, s2 = s0;
#pragma unroll
                    for (int t = 0; t < 4; ++t) {
                        const bf16x8 km = *(const bf16x8*)(lds + L_KM + t * 1024 + lane * 16), qf = *(const bf16x8*)(kq + 8192 + (qt * 4 + t) * 1024);
                        const bf16x8 k1 = *(const bf16x8*)(kq + t * 1024), k2 = *(const bf16x8*)(kq + (4 + t) * 1024);
                        s1 = MFMA32(k1, qf, s1); s2 = MFMA32(k2, qf, s2); s0 = MFMA32(km, qf, s0);
                    }
                    const float scale = 0.08838834764831845f;
                    const int qi = 16 * qt + n;
                    float v[12];
#pragma unroll
                    for (int r = 0; r < 4; ++r) {
                        v[r] = (g < 2) ? rbf(rbf(s0[r]) * scale) : -INFINITY;
                        v[4 + r] = (4 * g + r <= qi) ? rbf(rbf(s1[r]) * scale) : -999292928.f;
                        v[8 + r] = (16 + 4 * g + r <= qi) ? rbf(rbf(s2[r]) * scale) : -999292928.f;
                    }
                    float m = v[0];
#pragma unroll
                    for (int r = 1; r < 12; ++r) m = fmaxf(m, v[r]);
                    {
                        auto sw = __builtin_amdgcn_permlane16_swap(f2u(m), f2u(m), false, false);
                        m = fmaxf(u2f(sw[0]), u2f(sw[1]));
                        auto sw2 = __builtin_amdgcn_permlane32_swap(f2u(m), f2u(m), false, false);
                        m = fmaxf(u2f(sw2[0]), u2f(sw2[1]));
                    }
                    float sum = 0.f;
#pragma unroll
                    for (int r = 0; r < 12; ++r) { v[r] = __expf(v[r] - m); sum += v[r]; }
                    {
                        auto sw = __builtin_amdgcn_permlane16_swap(f2u(sum), f2u(sum), false, false);
                        sum = u2f(sw[0]) + u2f(sw[1]);
                        auto sw2 = __builtin_amdgcn_permlane32_swap(f2u(sum), f2u(sum), false, false);
                        sum = u2f(sw2[0]) + u2f(sw2[1]);
                    }
                    const float inv = 1.f / sum;
                    u32x4 px = {pk_bf16(v[4] * inv, v[5] * inv), pk_bf16(v[6] * inv, v[7] * inv), pk_bf16(v[8] * inv, v[9] * inv), pk_bf16(v[10] * inv, v[11] * inv)};
                    u32x2 pm = {pk_bf16(v[0] * inv, v[1] * inv), pk_bf16(v[2] * inv, v[3] * inv)};
                    const char* pxb = a.ws + OFF_PXF + (size_t)s * 81920;
                    st16_sc1(pxb, pbx, px);
                    if (g < 2) st8_sc1(pxb, pbm, pm);
                    next_target(s);
                }
                asm volatile("s_waitcnt vmcnt(0)" ::: "memory");
            } else {
                unsigned ap_est = 70, t_sig = 0, pp = 0;
                auto load_qk = [&](int s) {
                    if (wv >= 4) {
                        if (pp <= (unsigned)s) pp = wait_counters(ctrl, C_PP + h * 32, 1, s + 1, lane);
                        const char* src = a.ws + (wv < 6 ? OFF_KXF : OFF_QXF) + ((size_t)s * H_ + h) * 8192 + (wv & 1) * 4096 + lane * 16;
#pragma unroll
                        for (int i = 0; i < 4; ++i) glds16(src + i * 1024, L_KX + (s & 1) * 16384 + (wv - 4) * 4096 + i * 1024);
                    }
                };
                BARRIER();
                for (int s = 0; s < NSEG; ++s) {
                    front(s, t_sig, ap_est, []() {});
                    BARRIER();
                    asm volatile("s_barrier" ::: "memory");
                    if (s + 1 < NSEG) load_qk(s + 1);
                    if (role == 0 && wv == 1) {
                        ap_est = __builtin_amdgcn_readfirstlane(*(volatile unsigned*)(lds + L_MISC + 48)) - t_sig;
                        if (s >= 2 && lane == 0) st4_sc1(slotp(s + 2) + so_a, 0u, ~0u);
                    }
                    if (wv == 4 && lane == 0 && s > 0) set_counter(ctrl, C_AP + h * 32, s);
                    next_target(s);
                }
            }
            __syncthreads();
            if (tid == 0) set_counter(ctrl, C_AP + h * 32, NSEG);
        } else {
            bf16x16 wvv[10], wo[5][2];
            load_wvo(a.Wv, a.Wo, h, lds, tid, wv, wvv, wo);
            SS(2);
            if (wv == 0) wait_counters(ctrl, C_XF, 1, NO, lane);
            __syncthreads();
            prefetch_x(xf, 0, 0, wv, lane);
            load_mem0(a.mem0, lds, tid);
            pol = get_pol();
            asm volatile("s_waitcnt vmcnt(0)" ::: "memory");
            BARRIER();
            proj_u<false>(lds, lds + L_XB + lane * 16, wvv, wo, wv, lane, a.ws + OFF_UF, h, nullptr, 0);
            asm volatile("s_waitcnt vmcnt(0)" ::: "memory");
            __syncthreads();
            if (tid == 0) set_counter(ctrl, C_V0 + h * 32, 1u);
            SS(3);
            const unsigned vm_off = L_QF + (wv >> 2) * 1024 + ((wv & 3) * 16 + (n >> 3) * 8 + 4 * (g & 1)) * 16 + (n & 7) * 2;
            const unsigned zo4 = (4 + 5 * wv + (g >> 1) + 2 * (g & 1)) * 16384 + h * 256 + n * 16;
            const unsigned zo1 = (4 + 5 * wv + 4) * 16384 + h * 256 + n * 16 + (g & 1) * 8;
            for (int s = 0; s < NSEG; ++s) {
                gather(s);
                f32x4 av = {0.f, 0.f, 0.f, 0.f};
                bf16x8 mf[10];
#pragma unroll
                for (int kb = 0; kb < 4; ++kb) mf[kb] = *(const bf16x8*)(lds + L_MF + kb * 1024 + lane * 16);
                __builtin_amdgcn_sched_barrier(0);
#pragma unroll
                for (int kb = 0; kb < 10; ++kb) {
                    av = SMFMAC(mf[kb], wvv[kb], av);
                    if (kb + 4 < 10) mf[kb + 4] = *(const bf16x8*)(lds + L_MF + (kb + 4) * 1024 + lane * 16);
                    __builtin_amdgcn_sched_barrier(0);
                }
                const u32x2 p = half_add(av, av);
                char* vd = lds + vm_off;
                *(u16*)(vd + 0) = (u16)p.x; *(u16*)(vd + 16) = (u16)(p.x >> 16); *(u16*)(vd + 32) = (u16)p.y; *(u16*)(vd + 48) = (u16)(p.y >> 16);
                ST2(4);
                BARRIER();
                ST2(5);
                const bf16x8 vf0 = *(const bf16x8*)(lds + L_QF + lane * 16), vf1 = *(const bf16x8*)(lds + L_QF + 1024 + lane * 16);
                f32x4 z[5];
#pragma unroll
                for (int ct = 0; ct < 5; ++ct) { z[ct] = f32x4{0.f, 0.f, 0.f, 0.f}; z[ct] = SMFMAC(vf0, wo[ct][0], z[ct]); }
#pragma unroll
                for (int ct = 0; ct < 5; ++ct) z[ct] = SMFMAC(vf1, wo[ct][1], z[ct]);
                {
                    char* slot = slotp(s);
                    const u32x2 z01 = half_add(z[0], z[1]), z23 = half_add(z[2], z[3]);
                    auto sx = __builtin_amdgcn_permlane16_swap(z01.x, z23.x, false, false);
                    auto sy = __builtin_amdgcn_permlane16_swap(z01.y, z23.y, false, false);
                    st16_sc1(slot, zo4, u32x4{nosent(sx[0]), nosent(sy[0]), nosent(sx[1]), nosent(sy[1])});
                    const u32x2 z4 = half_add(z[4], z[4]);
                    if (lane < 32) st8_sc1(slot, zo1, u32x2{nosent(z4.x), nosent(z4.y)});
                }
                ST2(6);
                ST2F();
                next_target(s);
            }
        }
    } else if (role < NREC) {
        const int ct = role - R_B;
        {
            const u32x4 sv = {~0u, ~0u, ~0u, ~0u};
            for (int q = ct; q < 176; q += NB) {
                const unsigned base = (unsigned)OFF_HO + (q / 44) * (unsigned)MB + (q % 44) * 16384 + tid * 16;
                __builtin_amdgcn_raw_buffer_store_b128(sv, rs, base, 0, 16);
                __builtin_amdgcn_raw_buffer_store_b128(sv, rs, base, 8192, 16);
            }
            arrive(ctrl, C_FILL, tid);
        }
        SS(2);
        const unsigned pol = get_pol();
        SS(3);
        auto slotp = [&](int s) -> char* { return ho + (size_t)(s & 3) * MB + (((pol >> ((s >> 1) & 1)) & 1) ? 8192 : 0); };
        const unsigned po0 = lmap(SLOT_P + wv * 2560 + lane * 16), po1 = lmap(SLOT_P + wv * 2560 + 1024 + lane * 16);
        const unsigned po2 = lmap(SLOT_P + wv * 2560 + 2048 + g * 128 + (n & 7) * 16);
        const unsigned zo = (4 + ct) * 16384 + wv * 1024 + lane * 16;
        const unsigned mo_pub = lmap(SLOT_MEM + (2 * ct + (wv & 1)) * 128 + (lane & 31) * 4);
        const bool hi = n >= 8;
        unsigned vp = 0;
        u32x4 uf[4];
        auto load_u = [&](int s) {
            if (s == 0) wait_counters(ctrl, C_V0 + 4 * wv * 32, 4, 1, lane);
            else if (vp <= (unsigned)s) vp = wait_counters(ctrl, C_VP + 4 * wv * 32, 4, s + 1, lane);
            const char* src = a.ws + OFF_UF + ((size_t)s * NB + ct) * 32768 + wv * 4096;
            const unsigned o = lane * 16;
            asm volatile("global_load_dwordx4 %0, %4, %5 sc1\n\tglobal_load_dwordx4 %1, %4, %5 offset:1024 sc1\n\tglobal_load_dwordx4 %2, %4, %5 offset:2048 sc1\n\tglobal_load_dwordx4 %3, %4, %5 offset:3072 sc1"
                         : "=&v"(uf[0]), "=&v"(uf[1]), "=&v"(uf[2]), "=&v"(uf[3]) : "v"(o), "s"(src) : "memory");
        };
        load_u(0);
        SS(4);
        unsigned gbmin = 85;
        for (int s = 0; s < NSEG - 1; ++s) {
            char* slot = slotp(s);
            ST2(5);
            const unsigned target = read_sig(slot + so_a, ctrl) + DBC;
            ST2(6);
            asm volatile("" : "+v"(uf[0]), "+v"(uf[1]), "+v"(uf[2]), "+v"(uf[3]));
            u32x4 p0, p1, p2, zz; unsigned spins = 0, spz = 0;
            ST2(0);
            wait_rt(target - ZLEAD);
            asm volatile("global_load_dwordx4 %0, %1, %2 sc1" : "=&v"(zz) : "v"(zo), "s"(slot) : "memory");
            wait_rt(target);
            ST2(1);
            asm volatile("global_load_dwordx4 %0, %4, %7 sc1\n\tglobal_load_dwordx4 %1, %5, %7 sc1\n\tglobal_load_dwordx4 %2, %6, %7 sc1\n\ts_waitcnt vmcnt(0)"
                         : "=&v"(p0), "=&v"(p1), "=&v"(p2), "+v"(zz) : "v"(po0), "v"(po1), "v"(po2), "s"(slot) : "memory");
            for (;;) {
                const unsigned m0 = umax3(umax3(p0.x, p0.y, p0.z), p0.w, p1.x), m1 = umax3(p1.y, p1.z, p1.w), m2 = umax3(umax3(zz.x, zz.y, zz.z), zz.w, p2.x), m3 = umax3(p2.y, p2.z, p2.w);
                if (__all(max(umax3(m0, m1, m2), m3) != ~0u)) break;
                if (spin_abort(spins, ctrl)) break;
                asm volatile("global_load_dwordx4 %3, %7, %8 sc1\n\tglobal_load_dwordx4 %0, %4, %8 sc1\n\tglobal_load_dwordx4 %1, %5, %8 sc1\n\tglobal_load_dwordx4 %2, %6, %8 sc1\n\ts_waitcnt vmcnt(0)"
                             : "=&v"(p0), "=&v"(p1), "=&v"(p2), "=&v"(zz) : "v"(po0), "v"(po1), "v"(po2), "v"(zo), "s"(slot) : "memory");
            }
            ST2(2);
            const f32x4 zero = {0.f, 0.f, 0.f, 0.f};
            f32x4 xa = MFMA32(BC8(zz), BC8(p2), zero), xb = MFMA32(BC8(uf[1]), BC8(p0), zero);
            xa = MFMA32(BC8(uf[0]), BC8(p0), xa); xb = MFMA32(BC8(uf[3]), BC8(p1), xb);
            xa = MFMA32(BC8(uf[2]), BC8(p1), xa);
            f32x4 acc;
#pragma unroll
            for (int r = 0; r < 4; ++r) acc[r] = hi ? xb[r] : xa[r];
            *(f32x4*)(lds + L_RED + (wv * 16 + n) * 64 + g * 16) = acc;
            BARRIER();
            ST2(3);
            if (wv < 2) {
                const char* rp = lds + L_RED + (lane >> 5) * 4096 + ((lane >> 2) & 7) * 64 + wv * 32 + (lane & 3) * 8;
                c32 t = ((*(const c32*)(rp) + *(const c32*)(rp + 512)) + (*(const c32*)(rp + 1024) + *(const c32*)(rp + 1536))) +
                        ((*(const c32*)(rp + 2048) + *(const c32*)(rp + 2560)) + (*(const c32*)(rp + 3072) + *(const c32*)(rp + 3584)));
                auto sw = __builtin_amdgcn_permlane32_swap(f2u(t.x), f2u(t.y), false, false);
                const float y = u2f(sw[0]) + u2f(sw[1]);
                auto sw2 = __builtin_amdgcn_permlane32_swap(f2u(y), f2u(y), false, false);
                const unsigned o = nosent(pk_bf16(u2f(sw2[0]), u2f(sw2[1])));
                if (lane < 32) st4_sc1(slot, mo_pub, o);
                ST2(7);
                if (ct == 0 && wv == 0) {
                    const unsigned gb = rt32() - target;
                    gbmin = gb < gbmin ? gb : gbmin;
                    if (lane == 0) { st4_sc1(slot + so_b, 0u, gbmin); if (s >= 2) st4_sc1(slotp(s + 2) + so_b, 0u, ~0u); }
                }
                if (s >= 2 && lane < 32) st4_sc1(slotp(s + 2), mo_pub, ~0u);
            }
            ST2(4);
            asm volatile("s_barrier" ::: "memory");
            ST2F();
            if (wv == 2 && lane == 0 && s > 0) set_counter(ctrl, C_ZP + ct * 32, s);
            st16_sc1(a.ws + OFF_ZF + ((size_t)s * NB + ct) * 8192, tid * 16, zz);
            st16_sc1(slot, zo, u32x4{~0u, ~0u, ~0u, ~0u});
            if (s + 1 < NSEG - 1) load_u(s + 1);
        }
        {
            char* slot = slotp(NSEG - 1);
            u32x4 zz; unsigned spins = 0;
            for (;;) {
                asm volatile("global_load_dwordx4 %0, %1, %2 sc1\n\ts_waitcnt vmcnt(0)" : "=&v"(zz) : "v"(zo), "s"(slot) : "memory");
                if (__all(max(umax3(zz.x, zz.y, zz.z), zz.w) != ~0u)) break;
                __builtin_amdgcn_s_sleep(2);
                if (spin_abort(spins, ctrl)) break;
            }
            st16_sc1(a.ws + OFF_ZF + ((size_t)(NSEG - 1) * NB + ct) * 8192, tid * 16, zz);
            asm volatile("s_waitcnt vmcnt(0)" ::: "memory");
            __syncthreads();
            if (tid == 0) set_counter(ctrl, C_ZP + ct * 32, NSEG);
        }
    } else if (role < R_V) {
        const int h = head_of(role - R_P);
        bf16x16 wq[10], wk[10];
        load_w2(a.Wq, a.Wk, h, lds, tid, wv, wq, wk);
        SS(2);
        if (wv == 0) wait_counters(ctrl, C_XF, 1, NO, lane);
        __syncthreads();
        prefetch_x(xf, 1, 0, wv, lane);
        prefetch_x(xf, 2, 1, wv, lane);
        int b3 = 0;
        for (int s = 1; s < NSEG; ++s) {
            if (s < 5) asm volatile("s_waitcnt vmcnt(0)" ::: "memory"); else asm volatile("s_waitcnt vmcnt(7)" ::: "memory");
            BARRIER();
            if (s > 1 && tid == 0) set_counter(ctrl, C_PP + h * 32, s < 5 ? s : s - 1);
            if (s < 5) SS(2 + s);
            proj_qk<true>(lds + L_XB + b3 * 40960 + lane * 16, wq, wk, lds + L_T + tw_off + g * 64, lds + L_T + 8192 + tw_off + g * 64,
                          xf + (size_t)min(s + 2, NSEG - 1) * 40960 + wv * 5120 + lane * 16, L_XB + (b3 == 0 ? 2 : b3 - 1) * 40960 + wv * 5120);
            b3 = b3 == 2 ? 0 : b3 + 1;
            BARRIER();
            const u32x4 vq = *(const u32x4*)(lds + L_T + tid * 16), vk = *(const u32x4*)(lds + L_T + 8192 + tid * 16);
            st16_sc1(a.ws + OFF_KXF + ((size_t)s * H_ + h) * 8192, tid * 16, vk);
            st16_sc1(a.ws + OFF_QXF + ((size_t)s * H_ + h) * 8192, tid * 16, vq);
        }
        asm volatile("s_waitcnt vmcnt(0)" ::: "memory");
        __syncthreads();
        if (tid == 0) set_counter(ctrl, C_PP + h * 32, NSEG);
    } else if (role < R_O) {
        const int h = head_of(role - R_V);
        bf16x16 wvv[10], wo[5][2];
        load_wvo(a.Wv, a.Wo, h, lds, tid, wv, wvv, wo);
        SS(2);
        if (wv == 0) wait_counters(ctrl, C_XF, 1, NO, lane);
        __syncthreads();
        prefetch_x(xf, 1, 0, wv, lane);
        prefetch_x(xf, 2, 1, wv, lane);
        int b3 = 0;
        for (int s = 1; s < NSEG; ++s) {
            if (s < 5) asm volatile("s_waitcnt vmcnt(0)" ::: "memory"); else asm volatile("s_waitcnt vmcnt(10)" ::: "memory");
            BARRIER();
            if (s > 1 && tid == 0) set_counter(ctrl, C_VP + h * 32, s < 5 ? s : s - 1);
            if (s < 5) SS(2 + s);
            proj_u<true>(lds, lds + L_XB + b3 * 40960 + lane * 16, wvv, wo, wv, lane, a.ws + OFF_UF + (size_t)s * NB * 32768, h,
                         xf + (size_t)min(s + 2, NSEG - 1) * 40960 + wv * 5120 + lane * 16, L_XB + (b3 == 0 ? 2 : b3 - 1) * 40960 + wv * 5120);
            b3 = b3 == 2 ? 0 : b3 + 1;
        }
        asm volatile("s_waitcnt vmcnt(0)" ::: "memory");
        __syncthreads();
        if (tid == 0) set_counter(ctrl, C_VP + h * 32, NSEG);
    } else {
        const int o = role - R_O;
        {
#pragma unroll
            for (int i = 0; i < 5; ++i) {
                const int id = i * NT + tid, l = id & 63, tt = (id >> 6) & 1, hf = (id >> 7) & 1, kb = id >> 8;
                const int ga = l >> 4, tok = 32 * o + 16 * tt + (l & 15), dbase = 64 * kb + 16 * (2 * hf + (ga >> 1)) + 4 * (ga & 1);
                const u32x2 c0 = *(const u32x2*)(a.x + (size_t)tok * D_ + dbase), c1 = *(const u32x2*)(a.x + (size_t)tok * D_ + dbase + 8);
                u32x4 ov = {c0.x, c1.x, c0.y, c1.y};
                __builtin_amdgcn_raw_buffer_store_b128(ov, rs, (unsigned)OFF_XF + o * 40960 + id * 16, 0, 16);
            }
            arrive(ctrl, C_XF, tid);
        }
        SS(2);
        auto obar = [&](int idx) {
            asm volatile("s_waitcnt vmcnt(0)" ::: "memory");
            __syncthreads();
            if (wv == 0) {
                if (lane == 0) __hip_atomic_fetch_add(ctrl + C_OB + idx * 32, 1u, __ATOMIC_RELAXED, __HIP_MEMORY_SCOPE_AGENT);
                wait_counters(ctrl, C_OB + idx * 32, 1, idx == 0 ? NO + NS : NO, lane);
            }
            __syncthreads();
        };
        auto bf2f = [](unsigned short u) -> float { return u2f((unsigned)u << 16); };
        c32* tw = (c32*)(lds + 96 * 1024);
        c32* wbuf = (c32*)(lds) + wv * 720;
        for (int i = tid; i < 640; i += NT) { float sn, cs_; sincospif(-(float)i / 320.f, &sn, &cs_); tw[i] = c32{cs_, sn}; }
        float gwv[10];
#pragma unroll
        for (int j = 0; j < 10; ++j) gwv[j] = bf2f(a.gw[64 * j + lane]);
        __syncthreads();
        for (int task = o; task < 128; task += NO) {
            {
                const int r0 = task * 16 + wv * 2;
                c32 v[10]; float ga = 0.f, gb_ = 0.f;
#pragma unroll
                for (int j = 0; j < 10; ++j) {
                    v[j] = c32{bf2f(a.x[(size_t)r0 * D_ + 64 * j + lane]), bf2f(a.x[(size_t)(r0 + 1) * D_ + 64 * j + lane])};
                    ga += rbf(v[j].x * gwv[j]); gb_ += rbf(v[j].y * gwv[j]);
                }
#pragma unroll
                for (int of = 32; of >= 1; of >>= 1) { ga += __shfl_xor(ga, of); gb_ += __shfl_xor(gb_, of); }
                if (lane < 2) {
                    const float z = rbf(lane ? gb_ : ga);
                    const float gv = rbf(1.f / (1.f + __expf(-z)));
                    __builtin_amdgcn_raw_buffer_store_b32(f2u(gv), rs, (unsigned)(OFF_G + (r0 + lane) * 4), 0, 16);
                }
                fft640(v, wbuf, tw, lane);
                __syncthreads();
                for (int i = tid; i < 321 * 16; i += NT) {
                    const int k = i >> 4, t = i & 15;
                    const c32* zb = (const c32*)lds + (t >> 1) * 720;
                    const c32 z1 = zb[k], z2 = zb[k ? 640 - k : 0];
                    c32 r = (t & 1) ? c32{0.5f * (z1.y + z2.y), 0.5f * (z2.x - z1.x)} : c32{0.5f * (z1.x + z2.x), 0.5f * (z1.y - z2.y)};
                    __builtin_amdgcn_raw_buffer_store_b64(__builtin_bit_cast(u32x2, r), rs, (unsigned)(OFF_X1T + ((size_t)k * S_ + task * 16 + t) * 8), 0, 16);
                }
                __syncthreads();
            }
        }
        for (int i = tid; i < 2048; i += NT) { float sn, cs_; sincospif(-(float)i / 1024.f, &sn, &cs_); tw[i] = c32{cs_, sn}; }
        obar(0);
        {
            const int j = tid & 255, hf = tid >> 8;
            c32* buf = (c32*)lds + hf * 2304;
            auto pd = [](int p) { return p + ((p >> 5) << 2); };
            for (int b = o; b < 161; b += NO) {
                const int col = min(2 * b + hf, 320);
                const unsigned cb = (unsigned)(OFF_X1T + (size_t)col * S_ * 8);
                c32 v[8];
#pragma unroll
                for (int q = 0; q < 8; ++q) v[q] = __builtin_bit_cast(c32, __builtin_amdgcn_raw_buffer_load_b64(rs, cb + (q * 256 + j) * 8, 0, 16));
                const u32x4 wa = __builtin_amdgcn_raw_buffer_load_b128(rs, (unsigned)(OFF_WT + ((size_t)col * S_ + 8 * j) * 4), 0, 16);
                const u32x4 wb = __builtin_amdgcn_raw_buffer_load_b128(rs, (unsigned)(OFF_WT + ((size_t)col * S_ + 8 * j + 4) * 4), 0, 16);
                dft8<-1>(v);
#pragma unroll
                for (int q = 0; q < 8; ++q) buf[pd(q * 256 + j)] = q ? cmul(v[q], tw[j * q]) : v[q];
                __syncthreads();
                const int b2 = (j >> 5) * 256 + (j & 31), lo = j & 31;
#pragma unroll
                for (int q = 0; q < 8; ++q) v[q] = buf[pd(b2 + q * 32)];
                dft8<-1>(v);
#pragma unroll
                for (int q = 0; q < 8; ++q) buf[pd(b2 + q * 32)] = q ? cmul(v[q], tw[8 * lo * q]) : v[q];
                __syncthreads();
                const int b3 = (j >> 2) * 32 + (j & 3), n0 = j & 3;
#pragma unroll
                for (int q = 0; q < 8; ++q) v[q] = buf[pd(b3 + q * 4)];
                dft8<-1>(v);
#pragma unroll
                for (int q = 0; q < 8; ++q) buf[pd(b3 + q * 4)] = q ? cmul(v[q], tw[64 * n0 * q]) : v[q];
                __syncthreads();
#pragma unroll
                for (int q = 0; q < 8; ++q) v[q] = buf[pd(8 * j + q)];
                dft4<-1>(v[0], v[1], v[2], v[3]); dft4<-1>(v[4], v[5], v[6], v[7]);
                const unsigned wv8[8] = {wa.x, wa.y, wa.z, wa.w, wb.x, wb.y, wb.z, wb.w};
#pragma unroll
                for (int q = 0; q < 8; ++q) v[q] = cmul(v[q], c32{u2f(wv8[q] << 16), u2f(wv8[q] & 0xFFFF0000u)});
                dft4<1>(v[0], v[1], v[2], v[3]); dft4<1>(v[4], v[5], v[6], v[7]);
#pragma unroll
                for (int q = 0; q < 8; ++q) buf[pd(8 * j + q)] = v[q];
                __syncthreads();
#pragma unroll
                for (int q = 0; q < 8; ++q) { c32 t = buf[pd(b3 + q * 4)]; v[q] = q ? cmulc(t, tw[64 * n0 * q]) : t; }
                dft8<1>(v);
#pragma unroll
                for (int q = 0; q < 8; ++q) buf[pd(b3 + q * 4)] = v[q];
                __syncthreads();
#pragma unroll
                for (int q = 0; q < 8; ++q) { c32 t = buf[pd(b2 + q * 32)]; v[q] = q ? cmulc(t, tw[8 * lo * q]) : t; }
                dft8<1>(v);
#pragma unroll
                for (int q = 0; q < 8; ++q) buf[pd(b2 + q * 32)] = v[q];
                __syncthreads();
#pragma unroll
                for (int q = 0; q < 8; ++q) { c32 t = buf[pd(q * 256 + j)]; v[q] = q ? cmulc(t, tw[j * q]) : t; }
                dft8<1>(v);
                if (2 * b + hf <= 320) {
#pragma unroll
                    for (int q = 0; q < 8; ++q) __builtin_amdgcn_raw_buffer_store_b64(__builtin_bit_cast(u32x2, v[q]), rs, cb + (q * 256 + j) * 8, 0, 16);
                }
                __syncthreads();
            }
        }
        for (int i = tid; i < 640; i += NT) { float sn, cs_; sincospif(-(float)i / 320.f, &sn, &cs_); tw[i] = c32{cs_, sn}; }
        obar(1);
        for (int task = o; task < 128; task += NO) {
            for (int i = tid; i < 321 * 8; i += NT) {
                const int k = i >> 3, p = i & 7;
                u32x4 y = __builtin_amdgcn_raw_buffer_load_b128(rs, (unsigned)(OFF_X1T + ((size_t)k * S_ + task * 16 + 2 * p) * 8), 0, 16);
                float ar = u2f(y.x), ai = u2f(y.y), br = u2f(y.z), bi = u2f(y.w);
                if (k == 0 || k == 320) { ai = 0.f; bi = 0.f; }
                c32* zb = (c32*)lds + p * 720;
                zb[k] = c32{ar - bi, -(ai + br)};
                if (k > 0 && k < 320) zb[640 - k] = c32{ar + bi, -(br - ai)};
            }
            __syncthreads();
            c32 v[10];
#pragma unroll
            for (int j = 0; j < 10; ++j) v[j] = wbuf[64 * j + lane];
            LDSWAIT();
            fft640(v, wbuf, tw, lane);
            const float sc = 1.f / (2048.f * 640.f);
            const int r0 = task * 16 + wv * 2;
            for (int i = lane; i < 320; i += 64) {
                const c32 z0 = wbuf[2 * i], z1 = wbuf[2 * i + 1];
                __builtin_amdgcn_raw_buffer_store_b32(pk_bf16(z0.x * sc, z1.x * sc), rs, (unsigned)(OFF_A + ((size_t)r0 * D_ + 2 * i) * 2), 0, 16);
                __builtin_amdgcn_raw_buffer_store_b32(pk_bf16(-z0.y * sc, -z1.y * sc), rs, (unsigned)(OFF_A + ((size_t)(r0 + 1) * D_ + 2 * i) * 2), 0, 16);
            }
            __syncthreads();
        }
        obar(2);
        if (o < 60) {
            const int grp = o / 5, c = o - grp * 5;
            const int ct = 8 * c + wv;
            const bool hi = n >= 8;
            unsigned vp = 0, zp = 0, ap = 0;
            for (int it = 0; it < 6; ++it) {
                int s = grp + 12 * it, qlo = 0, qhi = 4;
                if (it == 5) {
                    if (grp < 2) s = 60 + grp;
                    else if (grp < 4) { s = 62; qlo = 2 * (grp - 2); qhi = qlo + 2; }
                    else if (grp < 8) { s = 63; qlo = grp - 4; qhi = qlo + 1; }
                    else break;
                }
                if (s == 0) wait_counters(ctrl, C_V0, 32, 1, lane);
                else if (vp <= (unsigned)s) vp = wait_counters(ctrl, C_VP, 32, s + 1, lane);
                u32x4 uf[32];
#pragma unroll
                for (int i = 0; i < 32; ++i) uf[i] = __builtin_amdgcn_raw_buffer_load_b128(rs, (unsigned)(OFF_UF + ((size_t)s * NB + ct) * 32768) + lane * 16, i * 1024, 16);
                const int et = tid >> 4, c8 = (tid & 15) * 8, row = 32 * s + et;
                const u32x4 av = __builtin_amdgcn_raw_buffer_load_b128(rs, (unsigned)(OFF_A + ((size_t)row * D_ + 128 * c + c8) * 2), 0, 16);
                const float gv = u2f(__builtin_amdgcn_raw_buffer_load_b32(rs, (unsigned)(OFF_G + row * 4), 0, 16));
                if (zp <= (unsigned)s) zp = wait_counters(ctrl, C_ZP + ct * 32, 1, s + 1, lane);
                u32x4 zq[8];
#pragma unroll
                for (int i = 0; i < 8; ++i) zq[i] = __builtin_amdgcn_raw_buffer_load_b128(rs, (unsigned)(OFF_ZF + ((size_t)s * NB + ct) * 8192) + lane * 16, i * 1024, 16);
                if (ap <= (unsigned)s) ap = wait_counters(ctrl, C_AP, 32, s + 1, lane);
                for (int c0 = qlo * 1280; c0 < qhi * 1280; c0 += 2560) {
                    u32x4 t[5];
#pragma unroll
                    for (int i = 0; i < 5; ++i) t[i] = __builtin_amdgcn_raw_buffer_load_b128(rs, (unsigned)(OFF_PXF + (size_t)s * 81920) + min(c0 + i * NT + tid, 5119) * 16, 0, 16);
#pragma unroll
                    for (int i = 0; i < 5; ++i) *(u32x4*)(lds + min(c0 + i * NT + tid, 5119) * 16) = t[i];
                }
                __syncthreads();
                float* tile = (float*)(lds + 81920);
                const unsigned pmo = 2048 + g * 128 + (n & 7) * 16;
#pragma unroll
                for (int qo = 0; qo < 4; ++qo) {
                    if (qo < qlo || qo >= qhi) continue;
                    f32x4 xa = {0.f, 0.f, 0.f, 0.f}, xb = xa, ma = xa;
#pragma unroll
                    for (int wg = 0; wg < 8; ++wg) {
                        const char* pbp = lds + qo * 20480 + wg * 2560;
                        const bf16x8 p0 = *(const bf16x8*)(pbp + lane * 16), p1 = *(const bf16x8*)(pbp + 1024 + lane * 16);
                        const bf16x8 p2 = *(const bf16x8*)(pbp + pmo);
                        xa = MFMA32(BC8(uf[4 * wg]), p0, xa); xb = MFMA32(BC8(uf[4 * wg + 1]), p0, xb);
                        xa = MFMA32(BC8(uf[4 * wg + 2]), p1, xa); xb = MFMA32(BC8(uf[4 * wg + 3]), p1, xb);
                        ma = MFMA32(BC8(zq[wg]), p2, ma);
                    }
                    f32x4 tot;
#pragma unroll
                    for (int r = 0; r < 4; ++r) {
                        const float y = hi ? xb[r] : xa[r] + ma[r];
                        tot[r] = y + u2f(__builtin_amdgcn_update_dpp(0, (int)f2u(y), 0x128, 0xf, 0xf, false));
                    }
                    if (n < 8) *(f32x4*)(tile + (8 * qo + n) * 132 + wv * 16 + 4 * g) = tot;
                }
                __syncthreads();
                if (et >= 8 * qlo && et < 8 * qhi) {
                    const f32x4 b0 = *(const f32x4*)(tile + et * 132 + c8), b1 = *(const f32x4*)(tile + et * 132 + c8 + 4);
                    const float og = rbf(1.f - gv);
                    auto mix = [&](unsigned aw, float x0, float x1) -> unsigned {
                        return pk_bf16(rbf(gv * u2f(aw << 16)) + rbf(og * rbf(x0)), rbf(gv * u2f(aw & 0xFFFF0000u)) + rbf(og * rbf(x1)));
                    };
                    u32x4 ov = {mix(av.x, b0[0], b0[1]), mix(av.y, b0[2], b0[3]), mix(av.z, b1[0], b1[1]), mix(av.w, b1[2], b1[3])};
                    *(u32x4*)(a.out + (size_t)row * D_ + 128 * c + c8) = ov;
                }
                __syncthreads();
            }
        }
    }
}

extern "C" void kernel_launch(void* const* d_in, const int* in_sizes, int n_in, void* d_out, int out_size, void* d_ws, size_t ws_size, hipStream_t stream) {
    char* ws = (char*)(((uintptr_t)d_ws + 2 * MB - 1) & ~(uintptr_t)(2 * MB - 1));
    (void)hipMemsetAsync(ws + OFF_CTRL, 0, CTRL_WORDS * 4, stream);
    MainArgs a;
    a.x = (const u16*)d_in[0]; a.mem0 = (const u16*)d_in[4]; a.Wq = (const u16*)d_in[5]; a.Wk = (const u16*)d_in[6]; a.Wv = (const u16*)d_in[7]; a.Wo = (const u16*)d_in[8];
    a.wr = (const u16*)d_in[1]; a.wi = (const u16*)d_in[2]; a.gw = (const u16*)d_in[10]; a.ws = ws; a.out = (u16*)d_out;
    (void)hipFuncSetAttribute((const void*)k_main, hipFuncAttributeMaxDynamicSharedMemorySize, LDS_BYTES);
    k_main<<<256, NT, LDS_BYTES, stream>>>(a);
}
```

```cpp
#include <hip/hip_runtime.h>
#include <hip/hip_bf16.h>
#include <stdint.h>

#define LAS __attribute__((address_space(3)))
typedef __attribute__((ext_vector_type(8))) __bf16 bf16x8;
typedef __attribute__((ext_vector_type(16))) __bf16 bf16x16;
typedef __attribute__((ext_vector_type(4))) short s16x4;
typedef __attribute__((ext_vector_type(8))) short s16x8;
typedef __attribute__((ext_vector_type(16))) short s16x16;
typedef __attribute__((ext_vector_type(4))) float f32x4;
typedef __attribute__((ext_vector_type(4))) unsigned u32x4;
typedef __attribute__((ext_vector_type(2))) unsigned u32x2;
typedef unsigned short u16;
typedef __amdgpu_buffer_rsrc_t rsrc_t;

namespace gm {
constexpr int S_ = 2048, D_ = 640, H_ = 32, E_ = 128, NSEG = 64;
constexpr int NA = 32, NM = 32, NB = 40, NREC = NA + NM + NB, NP = 32, NV = 32, NO = 64;
constexpr int R_M = NA, R_B = NA + NM, R_P = NREC, R_V = R_P + NP, R_O = R_V + NV, R_S = R_O + NO;
constexpr int NS = 256 - R_S;
constexpr int NT = 512;
constexpr size_t MB = 1024 * 1024;
constexpr size_t OFF_HO = 0;
constexpr size_t OFF_CTRL = 4 * MB;
constexpr size_t OFF_XF = 5 * MB;
constexpr size_t OFF_KXF = 8 * MB;
constexpr size_t OFF_QXF = 24 * MB;
constexpr size_t OFF_UF = 40 * MB;
constexpr size_t OFF_ZF = 120 * MB;
constexpr size_t OFF_KMF = 140 * MB;
constexpr size_t OFF_PXF = 144 * MB;
constexpr size_t OFF_X1T = 150 * MB;
constexpr size_t OFF_WT = 156 * MB;
constexpr size_t OFF_A = 159 * MB;
constexpr size_t OFF_G = 162 * MB;
constexpr size_t OFF_DBG = 200 * MB;
constexpr int SLOT_P = 0, SLOT_MEM = 20480, SLOT_SIGA = 30720, SLOT_SIGB = 30848;
constexpr int C_POL = 0, C_ABORT = 32, C_CNT = 64, C_CNT0 = C_CNT, C_CNT1 = C_CNT + 128;
constexpr int C_PP = 320, C_VP = C_PP + 32 * 32, C_AP = C_VP + 32 * 32, C_ZP = C_AP + 32 * 32, C_ATT = C_ZP + 40 * 32, C_OB = C_ATT + 64 * 32,
              C_XF = C_OB + 4 * 32, C_FILL = C_XF + 32, C_V0 = C_ATT, C_END = C_FILL + 32;
constexpr int CTRL_WORDS = 6976;
static_assert(C_END <= CTRL_WORDS, "ctrl");
constexpr unsigned SPIN_CAP = 1u << 20;
}
using namespace gm;

__device__ __forceinline__ unsigned xcc_id() { return (unsigned)__builtin_amdgcn_s_getreg(20 | (3 << 11)) & 0xfu; }
__host__ __device__ constexpr unsigned lmap(unsigned L) { return ((L >> 13) << 14) + (L & 8191u); }
__device__ __forceinline__ unsigned pk_bf16(float a, float b) {
    typedef __attribute__((ext_vector_type(2))) __bf16 bf2; typedef __attribute__((ext_vector_type(2))) float f2;
    f2 v = {a, b}; bf2 r = __builtin_convertvector(v, bf2); return __builtin_bit_cast(unsigned, r);
}
__device__ __forceinline__ unsigned f2u(float a) { return __builtin_bit_cast(unsigned, a); }
__device__ __forceinline__ float u2f(unsigned a) { return __builtin_bit_cast(float, a); }
__device__ __forceinline__ float rbf(float a) { return __builtin_bit_cast(float, pk_bf16(a, 0.f) << 16); }
__device__ __forceinline__ unsigned nosent(unsigned w) { return w < 0xFFFFFFFEu ? w : 0xFFFFFFFEu; }
#define BARRIER() do { asm volatile("s_waitcnt lgkmcnt(0)\n\ts_barrier" ::: "memory"); } while (0)
__device__ __forceinline__ bf16x8 lo8(bf16x16 w) { return __builtin_shufflevector(w, w, 0, 1, 2, 3, 4, 5, 6, 7); }
__device__ __forceinline__ bf16x8 hi8(bf16x16 w) { return __builtin_shufflevector(w, w, 8, 9, 10, 11, 12, 13, 14, 15); }
__host__ __device__ __forceinline__ int k2d(int k) { int G = k >> 4, L = k & 15, q = L >> 2, p = L & 3; return 16 * G + (p < 2 ? 2 * q + p : 8 + 2 * q + (p - 2)); }
__host__ __device__ __forceinline__ int frag_k(int gb, int jb) { return jb < 8 ? 8 * gb + jb : 32 + 8 * gb + (jb - 8); }
__device__ __forceinline__ void glds16(const void* gsrc, unsigned lds_dst) {
    unsigned keep;
    asm volatile("s_mov_b32 %0, m0\n\ts_mov_b32 m0, %2\n\ts_nop 0\n\tglobal_load_lds_dwordx4 %1, off sc1\n\ts_mov_b32 m0, %0"
                 : "=&s"(keep) : "v"(gsrc), "s"(lds_dst) : "memory");
}
typedef __attribute__((ext_vector_type(2))) float c32;
__device__ __forceinline__ c32 cmul(c32 a, c32 b) { return c32{a.x * b.x - a.y * b.y, a.x * b.y + a.y * b.x}; }
__device__ __forceinline__ c32 cmulc(c32 a, c32 b) { return c32{a.x * b.x + a.y * b.y, a.y * b.x - a.x * b.y}; }
template <int SG> __device__ __forceinline__ c32 rot90(c32 a) { return SG < 0 ? c32{a.y, -a.x} : c32{-a.y, a.x}; }
template <int SG> __device__ __forceinline__ void dft4(c32& a0, c32& a1, c32& a2, c32& a3) {
    c32 t0 = a0 + a2, t1 = a0 - a2, t2 = a1 + a3, t3 = rot90<SG>(a1 - a3);
    a0 = t0 + t2; a1 = t1 + t3; a2 = t0 - t2; a3 = t1 - t3;
}
template <int SG> __device__ __forceinline__ void dft8(c32 (&v)[8]) {
    const float r = 0.70710678118654752f;
    c32 e0 = v[0], e1 = v[2], e2 = v[4], e3 = v[6], o0 = v[1], o1 = v[3], o2 = v[5], o3 = v[7];
    dft4<SG>(e0, e1, e2, e3); dft4<SG>(o0, o1, o2, o3);
    c32 p1 = (SG < 0) ? c32{(o1.x + o1.y) * r, (o1.y - o1.x) * r} : c32{(o1.x - o1.y) * r, (o1.y + o1.x) * r};
    c32 p2 = rot90<SG>(o2);
    c32 p3 = (SG < 0) ? c32{(o3.y - o3.x) * r, -(o3.x + o3.y) * r} : c32{-(o3.x + o3.y) * r, (o3.x - o3.y) * r};
    v[0] = e0 + o0; v[4] = e0 - o0; v[1] = e1 + p1; v[5] = e1 - p1; v[2] = e2 + p2; v[6] = e2 - p2; v[3] = e3 + p3; v[7] = e3 - p3;
}
__device__ __forceinline__ void dft5f(c32& a0, c32& a1, c32& a2, c32& a3, c32& a4) {
    const float c1 = 0.30901699437494742f, c2 = -0.80901699437494742f, s1 = 0.95105651629515357f, s2 = 0.58778525229247313f;
    c32 t1 = a1 + a4, t2 = a2 + a3, t3 = a1 - a4, t4 = a2 - a3;
    c32 m1 = a0 + c1 * t1 + c2 * t2, m2 = a0 + c2 * t1 + c1 * t2;
    c32 n1 = s1 * t3 + s2 * t4, n2 = s2 * t3 - s1 * t4;
    a0 = a0 + t1 + t2;
    a1 = c32{m1.x + n1.y, m1.y - n1.x}; a4 = c32{m1.x - n1.y, m1.y + n1.x};
    a2 = c32{m2.x + n2.y, m2.y - n2.x}; a3 = c32{m2.x - n2.y, m2.y + n2.x};
}
__device__ __forceinline__ void dft10f(c32 (&v)[10]) {
    c32 a[5], b[5];
#pragma unroll
    for (int n2 = 0; n2 < 5; ++n2) { c32 x0 = v[(2 * n2) % 10], x1 = v[(5 + 2 * n2) % 10]; a[n2] = x0 + x1; b[n2] = x0 - x1; }
    dft5f(a[0], a[1], a[2], a[3], a[4]); dft5f(b[0], b[1], b[2], b[3], b[4]);
#pragma unroll
    for (int k2 = 0; k2 < 5; ++k2) { v[(6 * k2) % 10] = a[k2]; v[(5 + 6 * k2) % 10] = b[k2]; }
}
#define LDSWAIT() asm volatile("s_waitcnt lgkmcnt(0)" ::: "memory")
__device__ __forceinline__ void fft640(c32 (&v)[10], c32* buf, const c32* tw, int lane) {
    dft10f(v);
#pragma unroll
    for (int k1 = 0; k1 < 10; ++k1) { c32 t = k1 ? cmul(v[k1], tw[lane * k1]) : v[k1]; buf[k1 * 72 + (lane >> 3) * 9 + (lane & 7)] = t; }
    LDSWAIT();
    c32 w[2][8];
#pragma unroll
    for (int rd = 0; rd < 2; ++rd) {
        const int id = lane + 64 * rd;
        if (id < 80) {
            const int k1 = id >> 3, b = id & 7;
#pragma unroll
            for (int a = 0; a < 8; ++a) w[rd][a] = buf[k1 * 72 + a * 9 + b];
            dft8<-1>(w[rd]);
#pragma unroll
            for (int c = 1; c < 8; ++c) w[rd][c] = cmul(w[rd][c], tw[10 * b * c]);
        }
    }
    LDSWAIT();
#pragma unroll
    for (int rd = 0; rd < 2; ++rd) {
        const int id = lane + 64 * rd;
        if (id < 80) {
            const int k1 = id >> 3, b = id & 7;
#pragma unroll
            for (int c = 0; c < 8; ++c) buf[k1 * 72 + c * 9 + b] = w[rd][c];
        }
    }
    LDSWAIT();
#pragma unroll
    for (int rd = 0; rd < 2; ++rd) {
        const int id = lane + 64 * rd;
        if (id < 80) {
            const int k1 = id >> 3, c = id & 7;
#pragma unroll
            for (int b = 0; b < 8; ++b) w[rd][b] = buf[k1 * 72 + c * 9 + b];
            dft8<-1>(w[rd]);
        }
    }
    LDSWAIT();
#pragma unroll
    for (int rd = 0; rd < 2; ++rd) {
        const int id = lane + 64 * rd;
        if (id < 80) {
            const int k1 = id >> 3, c = id & 7;
#pragma unroll
            for (int d = 0; d < 8; ++d) buf[k1 + 10 * (c + 8 * d)] = w[rd][d];
        }
    }
    LDSWAIT();
}

struct MainArgs {
    const u16* x; const u16* mem0; const u16* Wq; const u16* Wk; const u16* Wv; const u16* Wo; const u16* wr; const u16* wi; const u16* gw; char* ws; u16* out;
};
#define DBC 16
#define DAC 16
#define ATSLEEP 8
#define ZLEAD 6
constexpr int L_XB = 0;
constexpr int L_MF = 81920;
constexpr int L_QF = 92160;
constexpr int L_KM = 96256;
constexpr int L_KX = 100352;
constexpr int L_T = 122880;
constexpr int L_RED = 0;
constexpr int L_UB = 8192;
constexpr int L_MISC = 139264;
constexpr int L_PROF = 140 * 1024;
constexpr int LDS_BYTES = 152 * 1024;

__device__ __forceinline__ unsigned rt32() { return (unsigned)__builtin_amdgcn_s_memrealtime(); }
__device__ __forceinline__ void wait_rt(unsigned target) {
    if ((int)(target - rt32()) > 2000) return;
    while ((int)(rt32() - target) < 0) {}
}
__device__ __forceinline__ float vmax(float a, float b) { float r; asm("v_max_f32 %0, %1, %2" : "=v"(r) : "v"(a), "v"(b)); return r; }
__device__ __forceinline__ float vmax3(float a, float b, float c) { float r; asm("v_max3_f32 %0, %1, %2, %3" : "=v"(r) : "v"(a), "v"(b), "v"(c)); return r; }
__device__ __forceinline__ unsigned umax3(unsigned a, unsigned b, unsigned c) { return max(max(a, b), c); }
__device__ __forceinline__ bool spin_abort(unsigned& spins, unsigned* ctrl) {
    ++spins;
    if (spins > SPIN_CAP) { __hip_atomic_store(ctrl + C_ABORT, 1u, __ATOMIC_RELAXED, __HIP_MEMORY_SCOPE_AGENT); return true; }
    if ((spins & 1023u) == 0u) return __builtin_amdgcn_readfirstlane(__hip_atomic_load(ctrl + C_ABORT, __ATOMIC_RELAXED, __HIP_MEMORY_SCOPE_AGENT)) != 0;
    return false;
}
__device__ __forceinline__ unsigned read_sig(const char* p, unsigned* ctrl) {
    unsigned v, spins = 0;
    for (;;) {
        asm volatile("v_mov_b32 %0, 0\n\tglobal_load_dword %0, %0, %1 sc1\n\ts_waitcnt vmcnt(0)" : "=&v"(v) : "s"(p) : "memory");
        v = __builtin_amdgcn_readfirstlane(v);
        if (v != ~0u) break;
        if (spin_abort(spins, ctrl)) break;
    }
    return v;
}
__device__ __forceinline__ unsigned wait_counters(unsigned* ctrl, int base, int cnt, unsigned need, int lane) {
    unsigned spins = 0, m;
    const unsigned off = (unsigned)(base + (lane < cnt ? lane : 0) * 32) * 4u;
    for (;;) {
        unsigned v;
        asm volatile("global_load_dword %0, %1, %2 sc1\n\ts_waitcnt vmcnt(0)" : "=&v"(v) : "v"(off), "s"(ctrl) : "memory");
        if (cnt > 1) {
#pragma unroll
            for (int o = 16; o >= 1; o >>= 1) v = min(v, (unsigned)__shfl_xor((int)v, o));
        }
        m = __builtin_amdgcn_readfirstlane(v);
        if (m >= need) break;
        __builtin_amdgcn_s_sleep(4);
        if (spin_abort(spins, ctrl)) break;
    }
    return m;
}
__device__ __forceinline__ void set_counter(unsigned* ctrl, int idx, unsigned v) { __hip_atomic_store(ctrl + idx, v, __ATOMIC_RELAXED, __HIP_MEMORY_SCOPE_AGENT); }
__device__ __forceinline__ void st16_sc1(const void* base, unsigned off, u32x4 v) { asm volatile("global_store_dwordx4 %0, %1, %2 sc1\n\ts_nop 1" :: "v"(off), "v"(v), "s"(base) : "memory"); }
__device__ __forceinline__ void st8_sc1(const void* base, unsigned off, u32x2 v) { asm volatile("global_store_dwordx2 %0, %1, %2 sc1\n\ts_nop 1" :: "v"(off), "v"(v), "s"(base) : "memory"); }
__device__ __forceinline__ void st4_sc1(const void* base, unsigned off, unsigned v) { asm volatile("global_store_dword %0, %1, %2 sc1" :: "v"(off), "v"(v), "s"(base) : "memory"); }
__device__ __forceinline__ void load_mem0(const u16* mem0, char* lds, int tid) {
    for (int c = tid; c < 640; c += NT) {
        const int kb = c >> 6, l = c & 63;
        *(u32x4*)(lds + L_MF + c * 16) = *(const u32x4*)(mem0 + (l & 7) * D_ + 64 * kb + 16 * (l >> 4) + 8 * ((l >> 3) & 1));
    }
}
__device__ __forceinline__ void prefetch_x(const char* xf, int seg, int buf, int wv, int lane) {
    const char* src = xf + (size_t)seg * 40960 + wv * 5120 + lane * 16;
    const unsigned dst = L_XB + buf * 40960 + wv * 5120;
#pragma unroll
    for (int i = 0; i < 5; ++i) glds16(src + i * 1024, dst + i * 1024);
}
#define ST2(k) do {} while (0)
#define ST2F() do {} while (0)
#define MFMA32(a, b, c) __builtin_amdgcn_mfma_f32_16x16x32_bf16((a), (b), (c), 0, 0, 0)
#define MFMA16(a, b, c) __builtin_amdgcn_mfma_f32_16x16x16bf16_1k((a), (b), (c), 0, 0, 0)
#define SMFMAC(a, b, c) __builtin_amdgcn_smfmac_f32_16x16x64_bf16((a), (b), (c), sidx, 0, 0)
#define BC8(x) __builtin_bit_cast(bf16x8, (x))
#define BC4(x) __builtin_bit_cast(s16x4, (x))
__device__ __forceinline__ bf16x16 cat4(s16x4 a, s16x4 b, s16x4 c, s16x4 d) {
    s16x8 x = __builtin_shufflevector(a, b, 0, 1, 2, 3, 4, 5, 6, 7), y = __builtin_shufflevector(c, d, 0, 1, 2, 3, 4, 5, 6, 7);
    return __builtin_bit_cast(bf16x16, (s16x16)__builtin_shufflevector(x, y, 0, 1, 2, 3, 4, 5, 6, 7, 8, 9, 10, 11, 12, 13, 14, 15));
}
#define TRREAD(p) __builtin_amdgcn_ds_read_tr16_b64_v4i16((LAS s16x4*)(p))
__device__ __forceinline__ void load_w2(const u16* Wa, const u16* Wb, int h, char* lds, int tid, int wv, bf16x16 (&wa)[10], bf16x16 (&wb)[10]) {
    const int lane = tid & 63, g = lane >> 4, n = lane & 15;
    u32x4 st[10][4];
    const unsigned wo_ = (unsigned)(((tid >> 4) * H_ + h) * E_ + (tid & 15) * 8) * 2u;
#pragma unroll
    for (int kb = 0; kb < 10; ++kb)
#pragma unroll
        for (int i = 0; i < 4; ++i) {
            const char* W = (const char*)((i & 2) ? Wb : Wa) + (size_t)(64 * kb + 32 * (i & 1)) * (H_ * E_ * 2);
            st[kb][i] = *(const u32x4*)(W + wo_);
        }
    const int q4 = n >> 2, p4 = n & 3;
#pragma unroll
    for (int kb = 0; kb < 10; ++kb) {
        char* sb = lds + (kb & 1) * 33792;
#pragma unroll
        for (int i = 0; i < 4; ++i)
            *(u32x4*)(sb + ((tid >> 4) + 32 * (i & 1)) * 528 + (i >> 1) * 256 + (tid & 15) * 16) = st[kb][i];
        BARRIER();
        s16x4 r[2][4];
#pragma unroll
        for (int tl = 0; tl < 2; ++tl)
#pragma unroll
            for (int u = 0; u < 4; ++u) r[tl][u] = TRREAD(sb + k2d(frag_k(g, 4 * u + q4)) * 528 + tl * 256 + (16 * wv + 4 * p4) * 2);
        wa[kb] = cat4(r[0][0], r[0][1], r[0][2], r[0][3]);
        wb[kb] = cat4(r[1][0], r[1][1], r[1][2], r[1][3]);
        asm volatile("" : "+v"(wa[kb]), "+v"(wb[kb]));
    }
    BARRIER();
}
__device__ __forceinline__ void load_wvo(const u16* Wv, const u16* Wo, int h, char* lds, int tid, int wv, bf16x16 (&wvv)[10], bf16x16 (&wo)[5][2]) {
    const int lane = tid & 63, g = lane >> 4, n = lane & 15;
    u32x4 st[10][2], so[2][10];
    const unsigned wo_ = (unsigned)(((tid >> 4) * H_ + h) * E_ + (tid & 15) * 8) * 2u;
#pragma unroll
    for (int kb = 0; kb < 10; ++kb)
#pragma unroll
        for (int i = 0; i < 2; ++i) st[kb][i] = *(const u32x4*)((const char*)Wv + (size_t)(64 * kb + 32 * i) * (H_ * E_ * 2) + wo_);
#pragma unroll
    for (int t = 0; t < 2; ++t)
#pragma unroll
        for (int i = 0; i < 10; ++i) so[t][i] = *(const u32x4*)((const char*)Wo + ((size_t)h * E_ + 64 * t) * (D_ * 2) + (size_t)i * (NT * 16) + tid * 16);
    const int q4 = n >> 2, p4 = n & 3;
#pragma unroll
    for (int kb = 0; kb < 10; ++kb) {
        char* sb = lds + (kb & 1) * 17408;
#pragma unroll
        for (int i = 0; i < 2; ++i) *(u32x4*)(sb + ((tid >> 4) + 32 * i) * 272 + (tid & 15) * 16) = st[kb][i];
        BARRIER();
        s16x4 r[4];
#pragma unroll
        for (int u = 0; u < 4; ++u) r[u] = TRREAD(sb + k2d(frag_k(g, 4 * u + q4)) * 272 + (16 * wv + 4 * p4) * 2);
        wvv[kb] = cat4(r[0], r[1], r[2], r[3]);
        asm volatile("" : "+v"(wvv[kb]));
    }
#pragma unroll
    for (int t = 0; t < 2; ++t) {
        BARRIER();
#pragma unroll
        for (int i = 0; i < 10; ++i) { const int c = i * NT + tid, e = c / 80, cc = c - e * 80; *(u32x4*)(lds + e * 1296 + cc * 16) = so[t][i]; }
        BARRIER();
#pragma unroll
        for (int ct = 0; ct < 5; ++ct) {
            s16x4 r[4];
#pragma unroll
            for (int u = 0; u < 4; ++u) r[u] = TRREAD(lds + k2d(frag_k(g, 4 * u + q4)) * 1296 + (16 * (5 * wv + ct) + 4 * p4) * 2);
            wo[ct][t] = cat4(r[0], r[1], r[2], r[3]);
            asm volatile("" : "+v"(wo[ct][t]));
        }
    }
    BARRIER();
}
__device__ __forceinline__ void tw_store(char* dst, f32x4 a0, f32x4 a1) {
    const unsigned p0 = pk_bf16(a0[0], a0[1]), p1 = pk_bf16(a0[2], a0[3]), p2 = pk_bf16(a1[0], a1[1]), p3 = pk_bf16(a1[2], a1[3]);
    *(u16*)(dst + 0) = (u16)p0; *(u16*)(dst + 16) = (u16)(p0 >> 16); *(u16*)(dst + 32) = (u16)p1; *(u16*)(dst + 48) = (u16)(p1 >> 16);
    *(u16*)(dst + 4096) = (u16)p2; *(u16*)(dst + 4096 + 16) = (u16)(p2 >> 16); *(u16*)(dst + 4096 + 32) = (u16)p3; *(u16*)(dst + 4096 + 48) = (u16)(p3 >> 16);
}
__device__ __forceinline__ void arrive(unsigned* ctrl, int idx, int tid) {
    asm volatile("s_waitcnt vmcnt(0)" ::: "memory");
    __syncthreads();
    if (tid == 0) __hip_atomic_fetch_add(ctrl + idx, 1u, __ATOMIC_RELAXED, __HIP_MEMORY_SCOPE_AGENT);
}
__device__ __forceinline__ u32x2 half_add(f32x4 a, f32x4 b) {
    float cv[4];
#pragma unroll
    for (int r = 0; r < 4; ++r) {
        auto sw = __builtin_amdgcn_permlane32_swap(f2u(a[r]), f2u(b[r]), false, false);
        cv[r] = u2f(sw[0]) + u2f(sw[1]);
    }
    return u32x2{pk_bf16(cv[0], cv[1]), pk_bf16(cv[2], cv[3])};
}

template <bool PF>
__device__ __forceinline__ void proj_qk(const char* xb, const bf16x16 (&wq)[10], const bf16x16 (&wk)[10], char* qdst, char* kdst, const char* psrc, unsigned pdst) {
    f32x4 q0 = {0.f, 0.f, 0.f, 0.f}, q1 = q0, k0 = q0, k1 = q0;
    bf16x8 xr[10][4];
#pragma unroll
    for (int i = 0; i < 4; ++i) xr[0][i] = *(const bf16x8*)(xb + i * 1024);
#pragma unroll
    for (int kb = 0; kb < 10; ++kb) {
        if (kb + 1 < 10) {
#pragma unroll
            for (int i = 0; i < 4; ++i) xr[kb + 1][i] = *(const bf16x8*)(xb + ((kb + 1) * 4 + i) * 1024);
        }
        __builtin_amdgcn_sched_barrier(0);
        q0 = MFMA32(xr[kb][0], lo8(wq[kb]), q0); q1 = MFMA32(xr[kb][1], lo8(wq[kb]), q1); k0 = MFMA32(xr[kb][0], lo8(wk[kb]), k0); k1 = MFMA32(xr[kb][1], lo8(wk[kb]), k1);
        q0 = MFMA32(xr[kb][2], hi8(wq[kb]), q0); q1 = MFMA32(xr[kb][3], hi8(wq[kb]), q1); k0 = MFMA32(xr[kb][2], hi8(wk[kb]), k0); k1 = MFMA32(xr[kb][3], hi8(wk[kb]), k1);
        __builtin_amdgcn_sched_barrier(0);
        if (PF && (kb & 1) == 0) glds16(psrc + (kb >> 1) * 1024, pdst + (kb >> 1) * 1024);
    }
    tw_store(qdst, q0, q1);
    tw_store(kdst, k0, k1);
}
template <bool PF>
__device__ __forceinline__ void proj_u(char* lds, const char* xb, const bf16x16 (&wvv)[10], const bf16x16 (&wo)[5][2], int wv, int lane, const char* ub, int h, const char* psrc, unsigned pdst, unsigned* cnt, unsigned cval) {
    const int g = lane >> 4, n = lane & 15;
    const int Lq = (n < 8) ? 4 * (n >> 1) + (n & 1) : 4 * ((n - 8) >> 1) + 2 + (n & 1);
    const unsigned twv_off = (wv >> 1) * 1024 + (2 * (wv & 1) + (Lq >> 3)) * 256 + (Lq & 7) * 2;
    const unsigned uo = (unsigned)(5 * wv * 32768 + h * 1024 + lane * 16);
    f32x4 v0 = {0.f, 0.f, 0.f, 0.f}, v1 = v0;
    bf16x8 xr[10][4];
#pragma unroll
    for (int kb = 0; kb < 2; ++kb)
#pragma unroll
        for (int i = 0; i < 4; ++i) xr[kb][i] = *(const bf16x8*)(xb + (kb * 4 + i) * 1024);
#pragma unroll
    for (int kb = 0; kb < 10; ++kb) {
        if (kb + 2 < 10) {
#pragma unroll
            for (int i = 0; i < 4; ++i) xr[kb + 2][i] = *(const bf16x8*)(xb + ((kb + 2) * 4 + i) * 1024);
        }
        __builtin_amdgcn_sched_barrier(0);
        v0 = MFMA32(xr[kb][0], lo8(wvv[kb]), v0); v1 = MFMA32(xr[kb][1], lo8(wvv[kb]), v1);
        v0 = MFMA32(xr[kb][2], hi8(wvv[kb]), v0); v1 = MFMA32(xr[kb][3], hi8(wvv[kb]), v1);
        __builtin_amdgcn_sched_barrier(0);
        if (PF && (kb & 1) == 0) glds16(psrc + (kb >> 1) * 1024, pdst + (kb >> 1) * 1024);
    }
    tw_store(lds + L_T + twv_off + g * 64, v0, v1);
    if (PF) asm volatile("s_waitcnt vmcnt(5)" ::: "memory");
    BARRIER();
    if (PF && wv == 0 && lane == 0) __hip_atomic_store(cnt, cval, __ATOMIC_RELAXED, __HIP_MEMORY_SCOPE_AGENT);
    bf16x8 vf[2][4];
#pragma unroll
    for (int tt = 0; tt < 2; ++tt)
#pragma unroll
        for (int t = 0; t < 4; ++t) vf[tt][t] = *(const bf16x8*)(lds + L_T + (tt * 4 + t) * 1024 + lane * 16);
#pragma unroll
    for (int ct = 0; ct < 5; ++ct) {
        f32x4 u0 = {0.f, 0.f, 0.f, 0.f}, u1 = u0;
#pragma unroll
        for (int t = 0; t < 2; ++t) {
            u0 = MFMA32(vf[0][2 * t], lo8(wo[ct][t]), u0); u1 = MFMA32(vf[1][2 * t], lo8(wo[ct][t]), u1);
            u0 = MFMA32(vf[0][2 * t + 1], hi8(wo[ct][t]), u0); u1 = MFMA32(vf[1][2 * t + 1], hi8(wo[ct][t]), u1);
        }
        u32x4 o = {pk_bf16(u0[0], u0[1]), pk_bf16(u0[2], u0[3]), pk_bf16(u1[0], u1[1]), pk_bf16(u1[2], u1[3])};
        st16_sc1(ub + ct * 32768, uo, o);
    }
}

__global__ void __launch_bounds__(NT, 2) k_main(MainArgs a) {
    extern __shared__ __attribute__((aligned(16))) char lds[];
    const int tid = threadIdx.x, lane = tid & 63;
    const int wv = __builtin_amdgcn_readfirstlane(tid >> 6);
    unsigned* ctrl = (unsigned*)(a.ws + OFF_CTRL);
#define SS(k) do {} while (0)
    int role;
    {
        unsigned* sm = (unsigned*)(lds + L_MISC);
        if (tid == 0) {
            const unsigned xc = xcc_id();
            int r = 0;
            for (unsigned k = 0; k < 8; ++k) {
                const int x = (int)((xc + k) & 7u);
                const int t = (int)__hip_atomic_fetch_add(ctrl + C_CNT + x * 32, 1u, __ATOMIC_RELAXED, __HIP_MEMORY_SCOPE_AGENT);
                if (t < 32) {
                    if (x < 4) r = t < 8 ? x * 8 + t : t < 16 ? R_M + x * 8 + t - 8 : t < 26 ? R_B + x * 10 + t - 16 : R_S + x * 6 + t - 26;
                    else r = t < 8 ? R_P + (x - 4) * 8 + t : t < 16 ? R_V + (x - 4) * 8 + t - 8 : R_O + (x - 4) * 16 + t - 16;
                    break;
                }
            }
            sm[0] = (unsigned)r;
        }
        __syncthreads();
        role = __builtin_amdgcn_readfirstlane((int)sm[0]);
        __syncthreads();
    }
    SS(1);
    const rsrc_t rs = __builtin_amdgcn_make_buffer_rsrc(a.ws, 0, (unsigned)(250 * MB), 0x00020000);
    if (role >= R_S) {
        const int sp = role - R_S;
        if (sp == 0) {
            if (tid == 0) {
                unsigned best[4] = {~0u, ~0u, ~0u, ~0u};
                for (int rep = 0; rep < 4; ++rep)
#pragma unroll
                    for (int t = 0; t < 4; ++t) {
                        unsigned* ad = (unsigned*)(a.ws + OFF_HO + (size_t)(t >> 1) * 2 * MB + 50 * 16384 + (t & 1) * 8192);
                        unsigned z = 0, r;
                        unsigned long long c0 = __builtin_amdgcn_s_memtime();
                        asm volatile("s_waitcnt lgkmcnt(0)\n\tglobal_atomic_add %0, %1, %2, off sc0\n\ts_waitcnt vmcnt(0)" : "=v"(r) : "v"(ad), "v"(z) : "memory");
                        unsigned long long c1 = __builtin_amdgcn_s_memtime();
                        unsigned d = (unsigned)(c1 - c0); best[t] = d < best[t] ? d : best[t];
                    }
                __hip_atomic_store(ctrl + C_POL, 0x100u | (best[1] < best[0] ? 1u : 0u) | (best[3] < best[2] ? 2u : 0u), __ATOMIC_RELAXED, __HIP_MEMORY_SCOPE_AGENT);
            }
            __syncthreads();
        }
        for (int task = NS - 1 - sp; task < 32; task += NS) {
            const int p0 = task * 64;
            unsigned* tile = (unsigned*)lds;
            unsigned short xr[8][6], xi[8][6];
#pragma unroll
            for (int rr = 0; rr < 8; ++rr) {
                const int pos = p0 + wv + 8 * rr, m = (pos >> 8) + 8 * ((pos >> 5) & 7) + 64 * ((pos >> 2) & 7) + 512 * (pos & 3);
#pragma unroll
                for (int kk = 0; kk < 6; ++kk) { const int k = min(lane + 64 * kk, 320); xr[rr][kk] = a.wr[(size_t)m * 321 + k]; xi[rr][kk] = a.wi[(size_t)m * 321 + k]; }
            }
#pragma unroll
            for (int rr = 0; rr < 8; ++rr)
#pragma unroll
                for (int kk = 0; kk < 6; ++kk) { const int k = lane + 64 * kk; if (k < 321) tile[(wv + 8 * rr) * 323 + k] = (unsigned)xr[rr][kk] | ((unsigned)xi[rr][kk] << 16); }
            __syncthreads();
            for (int i = tid; i < 321 * 16; i += NT) {
                const int k = i >> 4, r = (i & 15) * 4;
                u32x4 v = {tile[r * 323 + k], tile[(r + 1) * 323 + k], tile[(r + 2) * 323 + k], tile[(r + 3) * 323 + k]};
                __builtin_amdgcn_raw_buffer_store_b128(v, rs, (unsigned)(OFF_WT + ((size_t)k * S_ + p0 + r) * 4), 0, 16);
            }
            __syncthreads();
        }
        arrive(ctrl, C_OB, tid);
        return;
    }
    auto get_pol = [&]() -> unsigned {
        if (wv == 0) {
            unsigned v, spins = 0;
            for (;;) {
                v = __builtin_amdgcn_readfirstlane(__hip_atomic_load(ctrl + C_POL, __ATOMIC_RELAXED, __HIP_MEMORY_SCOPE_AGENT));
                if (v & 0x100u) break;
                __builtin_amdgcn_s_sleep(2);
                if (spin_abort(spins, ctrl)) break;
            }
            wait_counters(ctrl, C_FILL, 1, NB, lane);
            if (lane == 0) *(volatile unsigned*)(lds + L_MISC + 32) = v;
        }
        __syncthreads();
        return __builtin_amdgcn_readfirstlane(*(volatile unsigned*)(lds + L_MISC + 32));
    };
    const char* xf = a.ws + OFF_XF;
    char* ho = a.ws + OFF_HO;
    const int g = lane >> 4, n = lane & 15;
    constexpr unsigned so_a = lmap(SLOT_SIGA), so_b = lmap(SLOT_SIGB);
    const int sidx = (lane & 8) ? 0xEEEE : 0x4444;
    const unsigned tw_off = (wv >> 1) * 1024 + (2 * (wv & 1) + (n >> 3)) * 256 + (n & 7) * 2;

    if (role < R_B) {
        const int h = (role & 31);
        const int gw = role < NA ? (wv < 2 ? wv : wv - 2) : wv;
        const int gn = role < NA ? ((wv == 2 || wv == 3) ? 0 : (gw < 4 ? 2 : 1)) : (wv < 2 ? 2 : 1);
        const int f0 = gw, f1 = role < NA ? gw + 6 : gw + 8;
        const unsigned mo0 = lmap(SLOT_MEM + f0 * 1024 + lane * 16), mo1 = lmap(SLOT_MEM + (f1 < 10 ? f1 : 0) * 1024 + lane * 16);
        unsigned pol = 0, target = 0;
        auto slotp = [&](int s) -> char* { return ho + (size_t)(s & 3) * MB + (((pol >> ((s >> 1) & 1)) & 1) ? 8192 : 0); };
        auto gather = [&](int s) {
            if (s > 0 && gn > 0) {
                const char* pslot = slotp(s - 1);
                ST2(0);
                if (s > 1) wait_rt(target);
                ST2(1);
                unsigned spins = 0;
                if (gn == 2) {
                    u32x4 v, v2;
                    for (;;) {
                        asm volatile("global_load_dwordx4 %0, %2, %4 sc1\n\tglobal_load_dwordx4 %1, %3, %4 sc1\n\ts_waitcnt vmcnt(0)"
                                     : "=&v"(v), "=&v"(v2) : "v"(mo0), "v"(mo1), "s"(pslot) : "memory");
                        *(u32x4*)(lds + L_MF + f0 * 1024 + lane * 16) = v;
                        *(u32x4*)(lds + L_MF + f1 * 1024 + lane * 16) = v2;
                        __builtin_amdgcn_sched_barrier(0);
                        if (__all(max(umax3(umax3(v.x, v.y, v.z), v.w, v2.x), umax3(v2.y, v2.z, v2.w)) != ~0u)) break;
                        if (spin_abort(spins, ctrl)) break;
                    }
                } else {
                    u32x4 v;
                    for (;;) {
                        asm volatile("global_load_dwordx4 %0, %1, %2 sc1\n\ts_waitcnt vmcnt(0)" : "=&v"(v) : "v"(mo0), "s"(pslot) : "memory");
                        *(u32x4*)(lds + L_MF + f0 * 1024 + lane * 16) = v;
                        __builtin_amdgcn_sched_barrier(0);
                        if (__all(max(umax3(v.x, v.y, v.z), v.w) != ~0u)) break;
                        if (spin_abort(spins, ctrl)) break;
                    }
                }
            }
            ST2(2);
            BARRIER();
            ST2(3);
        };
        auto next_target = [&](int s) {
            if (s > 0 && s < NSEG - 1) {
                unsigned sa, gb, spins = 0;
                for (;;) {
                    asm volatile("v_mov_b32 %0, 0\n\tglobal_load_dword %1, %0, %3 sc1\n\tglobal_load_dword %0, %0, %2 sc1\n\ts_waitcnt vmcnt(0)"
                                 : "=&v"(sa), "=&v"(gb) : "s"(slotp(s) + so_a), "s"(slotp(s - 1) + so_b) : "memory");
                    sa = __builtin_amdgcn_readfirstlane(sa); gb = __builtin_amdgcn_readfirstlane(gb);
                    if (sa != ~0u && gb != ~0u) break;
                    if (spin_abort(spins, ctrl)) break;
                }
                target = sa + DBC + gb + DAC;
            }
        };
        if (role < NA) {
            bf16x16 wq[10], wk[10];
            load_w2(a.Wq, a.Wk, h, lds, tid, wv, wq, wk);
            SS(2);
            if (wv == 0) wait_counters(ctrl, C_XF, 1, NO, lane);
            __syncthreads();
            prefetch_x(xf, 0, 0, wv, lane);
            load_mem0(a.mem0, lds, tid);
            pol = get_pol();
            asm volatile("s_waitcnt vmcnt(0)" ::: "memory");
            BARRIER();
            proj_qk<false>(lds + L_XB + lane * 16, wq, wk, lds + L_KX + 8192 + tw_off + g * 64, lds + L_KX + tw_off + g * 64, nullptr, 0);
            SS(3);
            auto front = [&](int s, unsigned& t_sig, unsigned ap_est, auto&& hook) {
                gather(s);
                f32x4 aq = {0.f, 0.f, 0.f, 0.f}, ak = aq;
                bf16x8 mf[10];
#pragma unroll
                for (int kb = 0; kb < 3; ++kb) mf[kb] = *(const bf16x8*)(lds + L_MF + kb * 1024 + lane * 16);
                if (role == 0 && wv == 1) {
                    t_sig = rt32();
                    if (lane == 0) st4_sc1(slotp(s) + so_a, 0u, nosent(t_sig + ap_est));
                }
                __builtin_amdgcn_sched_barrier(0);
#pragma unroll
                for (int kb = 0; kb < 10; ++kb) {
                    aq = SMFMAC(mf[kb], wq[kb], aq); ak = SMFMAC(mf[kb], wk[kb], ak);
                    if (kb + 3 < 10) mf[kb + 3] = *(const bf16x8*)(lds + L_MF + (kb + 3) * 1024 + lane * 16);
                    __builtin_amdgcn_sched_barrier(0);
                }
                hook();
                const u32x2 p = half_add(aq, ak);
                char* qd = lds + L_QF + (lane >> 5) * 4096 + tw_off + (g & 1) * 64;
                *(u16*)(qd + 0) = (u16)p.x; *(u16*)(qd + 16) = (u16)(p.x >> 16); *(u16*)(qd + 32) = (u16)p.y; *(u16*)(qd + 48) = (u16)(p.y >> 16);
            };
            if (wv == 0) {
                const int wg = h >> 2;
                const unsigned po_x = lmap(SLOT_P + wg * 2560 + ((h >> 1) & 1) * 1024 + (g * 16 + (h & 1) * 8 + (n & 7)) * 16 + (n >> 3) * 8);
                const unsigned po_m = lmap(SLOT_P + wg * 2560 + 2048 + (h & 3) * 128 + (n & 7) * 16 + (g & 1) * 8 + (n >> 3) * 4);
                const unsigned qoff = L_QF + (g * 16 + (n & 7)) * 16;
                const bool hi = n >= 8;
                unsigned dummy = 0;
                BARRIER();
                SS(4);
                for (int s = 0; s < NSEG; ++s) {
                    bf16x8 kx[8];
                    front(s, dummy, 0, [&]() {
#pragma unroll
                        for (int t = 0; t < 8; ++t) kx[t] = *(const bf16x8*)(lds + L_KX + (s & 1) * 16384 + t * 1024 + lane * 16);
                    });
                    ST2(4);
                    BARRIER();
                    ST2(5);
                    if (s < NSEG - 1) {
                        char* slot = slotp(s);
                        f32x4 s0 = {0.f, 0.f, 0.f, 0.f}, s1 = s0, s2 = s0;
                        bf16x8 qf[4], km[4];
#pragma unroll
                        for (int t = 0; t < 4; ++t) { qf[t] = *(const bf16x8*)(lds + qoff + t * 1024); km[t] = *(const bf16x8*)(lds + L_KM + t * 1024 + lane * 16); }
#pragma unroll
                        for (int t = 0; t < 4; ++t) { s1 = MFMA32(kx[t], qf[t], s1); s2 = MFMA32(kx[4 + t], qf[t], s2); s0 = MFMA32(km[t], qf[t], s0); }
                        __builtin_amdgcn_sched_group_barrier(0x100, 8, 0);
                        __builtin_amdgcn_sched_group_barrier(0x8, 12, 0);
                        const float cexp = 0.08838834764831845f * 1.4426950408889634f;
                        float v[6];
#pragma unroll
                        for (int r = 0; r < 4; ++r) v[r] = hi ? s2[r] : s1[r];
                        v[4] = hi ? s0[2] : s0[0]; v[5] = hi ? s0[3] : s0[1];
                        if (g >= 2) { v[4] = -INFINITY; v[5] = -INFINITY; }
                        float m, tmp_;
                        asm("v_max3_f32 %0, %2, %3, %4\n\tv_max3_f32 %1, %5, %6, %7\n\tv_max_f32 %0, %0, %1\n\ts_nop 1\n\t"
                            "v_max_f32_dpp %0, %0, %0 row_ror:8 row_mask:0xf bank_mask:0xf\n\tv_mov_b32 %1, %0\n\ts_nop 1\n\t"
                            "v_permlane16_swap_b32 %0, %1\n\tv_max_f32 %0, %0, %1\n\tv_mov_b32 %1, %0\n\ts_nop 1\n\t"
                            "v_permlane32_swap_b32 %0, %1\n\tv_max_f32 %0, %0, %1"
                            : "=&v"(m), "=&v"(tmp_) : "v"(v[0]), "v"(v[1]), "v"(v[2]), "v"(v[3]), "v"(v[4]), "v"(v[5]));
                        const float mc = -m * cexp;
#pragma unroll
                        for (int r = 0; r < 6; ++r) v[r] = __builtin_amdgcn_exp2f(__builtin_fmaf(v[r], cexp, mc));
                        float sum = ((v[0] + v[1]) + (v[2] + v[3])) + (v[4] + v[5]);
                        sum += u2f(__builtin_amdgcn_update_dpp(0, (int)f2u(sum), 0x128, 0xf, 0xf, false));
                        {
                            auto sw = __builtin_amdgcn_permlane16_swap(f2u(sum), f2u(sum), false, false);
                            sum = u2f(sw[0]) + u2f(sw[1]);
                            auto sw2 = __builtin_amdgcn_permlane32_swap(f2u(sum), f2u(sum), false, false);
                            sum = u2f(sw2[0]) + u2f(sw2[1]);
                        }
                        const float inv = __builtin_amdgcn_rcpf(sum);
                        u32x2 px = {nosent(pk_bf16(v[0] * inv, v[1] * inv)), nosent(pk_bf16(v[2] * inv, v[3] * inv))};
                        const unsigned pm = nosent(pk_bf16(v[4] * inv, v[5] * inv));
                        st8_sc1(slot, po_x, px);
                        if (g < 2) st4_sc1(slot, po_m, pm);
                        ST2(7);
                        if (role == 0) { const unsigned t = rt32(); if (lane == 0) *(volatile unsigned*)(lds + L_MISC + 48) = t; LDSWAIT(); }
                    }
                    ST2(6);
                    asm volatile("s_barrier" ::: "memory");
                    ST2F();
                    if (s >= 2) {
                        st8_sc1(slotp(s + 2), po_x, u32x2{~0u, ~0u});
                        if (g < 2) st4_sc1(slotp(s + 2), po_m, ~0u);
                    }
                    target = __builtin_amdgcn_readfirstlane(*(volatile unsigned*)(lds + L_MISC + 64 + (s & 1) * 4));
                }
            } else if (wv == 2 || wv == 3) {
                const int qt = wv - 2;
                unsigned dummy = 0;
                BARRIER();
                const unsigned pbx = (2 * qt + (n >> 3)) * 20480 + (h >> 2) * 2560 + ((h >> 1) & 1) * 1024 + (g * 16 + (h & 1) * 8 + (n & 7)) * 16;
                const unsigned pbm = (2 * qt + (n >> 3)) * 20480 + (h >> 2) * 2560 + 2048 + (h & 3) * 128 + (n & 7) * 16 + (g & 1) * 8;
                for (int s = 0; s < NSEG; ++s) {
                    front(s, dummy, 0, []() {});
                    asm volatile("s_waitcnt vmcnt(0)" ::: "memory");
                    BARRIER();
                    __builtin_amdgcn_s_sleep(ATSLEEP);
                    const char* kq = lds + L_KX + (s & 1) * 16384 + lane * 16;
                    f32x4 s0 = {0.f, 0.f, 0.f, 0.f}, s1 = s0, s2 = s0;
#pragma unroll
                    for (int t = 0; t < 4; ++t) {
                        const bf16x8 km = *(const bf16x8*)(lds + L_KM + t * 1024 + lane * 16), qf = *(const bf16x8*)(kq + 8192 + (qt * 4 + t) * 1024);
                        const bf16x8 k1 = *(const bf16x8*)(kq + t * 1024), k2 = *(const bf16x8*)(kq + (4 + t) * 1024);
                        s1 = MFMA32(k1, qf, s1); s2 = MFMA32(k2, qf, s2); s0 = MFMA32(km, qf, s0);
                    }
                    const float cexp = 0.08838834764831845f * 1.4426950408889634f, masked = -999292928.f / 0.08838834764831845f;
                    const int qi = 16 * qt + n;
                    float v[12];
#pragma unroll
                    for (int r = 0; r < 4; ++r) {
                        v[r] = (g < 2) ? s0[r] : -INFINITY;
                        v[4 + r] = (4 * g + r <= qi) ? s1[r] : masked;
                        v[8 + r] = (16 + 4 * g + r <= qi) ? s2[r] : masked;
                    }
                    float m, tmp_;
                    asm("v_max3_f32 %0, %2, %3, %4\n\tv_max3_f32 %1, %5, %6, %7\n\tv_max3_f32 %0, %0, %8, %9\n\tv_max3_f32 %1, %1, %10, %11\n\tv_max3_f32 %0, %0, %12, %13\n\tv_max_f32 %0, %0, %1\n\t"
                        "v_mov_b32 %1, %0\n\ts_nop 1\n\tv_permlane16_swap_b32 %0, %1\n\tv_max_f32 %0, %0, %1\n\tv_mov_b32 %1, %0\n\ts_nop 1\n\t"
                        "v_permlane32_swap_b32 %0, %1\n\tv_max_f32 %0, %0, %1"
                        : "=&v"(m), "=&v"(tmp_) : "v"(v[0]), "v"(v[1]), "v"(v[2]), "v"(v[3]), "v"(v[4]), "v"(v[5]), "v"(v[6]), "v"(v[7]), "v"(v[8]), "v"(v[9]), "v"(v[10]), "v"(v[11]));
                    const float mc = -m * cexp;
#pragma unroll
                    for (int r = 0; r < 12; ++r) v[r] = __builtin_amdgcn_exp2f(__builtin_fmaf(v[r], cexp, mc));
                    float sum = ((v[0] + v[1]) + (v[2] + v[3])) + ((v[4] + v[5]) + (v[6] + v[7])) + ((v[8] + v[9]) + (v[10] + v[11]));
                    {
                        auto sw = __builtin_amdgcn_permlane16_swap(f2u(sum), f2u(sum), false, false);
                        sum = u2f(sw[0]) + u2f(sw[1]);
                        auto sw2 = __builtin_amdgcn_permlane32_swap(f2u(sum), f2u(sum), false, false);
                        sum = u2f(sw2[0]) + u2f(sw2[1]);
                    }
                    const float inv = __builtin_amdgcn_rcpf(sum);
                    u32x4 px = {pk_bf16(v[4] * inv, v[5] * inv), pk_bf16(v[6] * inv, v[7] * inv), pk_bf16(v[8] * inv, v[9] * inv), pk_bf16(v[10] * inv, v[11] * inv)};
                    u32x2 pm = {pk_bf16(v[0] * inv, v[1] * inv), pk_bf16(v[2] * inv, v[3] * inv)};
                    const char* pxb = a.ws + OFF_PXF + (size_t)s * 81920;
                    st16_sc1(pxb, pbx, px);
                    if (g < 2) st8_sc1(pxb, pbm, pm);
                    asm volatile("s_barrier" ::: "memory");
                }
                asm volatile("s_waitcnt vmcnt(0)" ::: "memory");
            } else {
                unsigned ap_est = 70, t_sig = 0, pp = 0;
                auto load_qk = [&](int s) {
                    if (wv >= 4) {
                        if (pp <= (unsigned)s) pp = wait_counters(ctrl, C_PP + h * 32, 1, s + 1, lane);
                        const char* src = a.ws + (wv < 6 ? OFF_KXF : OFF_QXF) + ((size_t)s * H_ + h) * 8192 + (wv & 1) * 4096 + lane * 16;
#pragma unroll
                        for (int i = 0; i < 4; ++i) glds16(src + i * 1024, L_KX + (s & 1) * 16384 + (wv - 4) * 4096 + i * 1024);
                    }
                };
                BARRIER();
                for (int s = 0; s < NSEG; ++s) {
                    front(s, t_sig, ap_est, []() {});
                    BARRIER();
                    if (wv == 1) { next_target(s); if (lane == 0) *(volatile unsigned*)(lds + L_MISC + 64 + (s & 1) * 4) = target; }
                    if (s + 1 < NSEG) load_qk(s + 1);
                    BARRIER();
                    if (role == 0 && wv == 1) {
                        ap_est = __builtin_amdgcn_readfirstlane(*(volatile unsigned*)(lds + L_MISC + 48)) - t_sig;
                        if (s >= 2 && lane == 0) st4_sc1(slotp(s + 2) + so_a, 0u, ~0u);
                    }
                    if (wv == 4 && lane == 0 && s > 0) set_counter(ctrl, C_AP + h * 32, s);
                    target = __builtin_amdgcn_readfirstlane(*(volatile unsigned*)(lds + L_MISC + 64 + (s & 1) * 4));
                }
            }
            __syncthreads();
            if (tid == 0) set_counter(ctrl, C_AP + h * 32, NSEG);
        } else {
            bf16x16 wvv[10], wo[5][2];
            load_wvo(a.Wv, a.Wo, h, lds, tid, wv, wvv, wo);
            SS(2);
            if (wv == 0) wait_counters(ctrl, C_XF, 1, NO, lane);
            __syncthreads();
            prefetch_x(xf, 0, 0, wv, lane);
            load_mem0(a.mem0, lds, tid);
            pol = get_pol();
            asm volatile("s_waitcnt vmcnt(0)" ::: "memory");
            BARRIER();
            proj_u<false>(lds, lds + L_XB + lane * 16, wvv, wo, wv, lane, a.ws + OFF_UF, h, nullptr, 0, nullptr, 0);
            asm volatile("s_waitcnt vmcnt(0)" ::: "memory");
            __syncthreads();
            if (tid == 0) set_counter(ctrl, C_V0 + h * 32, 1u);
            SS(3);
            const unsigned vm_off = L_QF + (wv >> 2) * 1024 + ((wv & 3) * 16 + (n >> 3) * 8 + 4 * (g & 1)) * 16 + (n & 7) * 2;
            const unsigned zo4 = (4 + 5 * wv + (g >> 1) + 2 * (g & 1)) * 16384 + h * 256 + n * 16;
            const unsigned zo1 = (4 + 5 * wv + 4) * 16384 + h * 256 + n * 16 + (g & 1) * 8;
            for (int s = 0; s < NSEG; ++s) {
                gather(s);
                f32x4 av = {0.f, 0.f, 0.f, 0.f};
                bf16x8 mf[10];
#pragma unroll
                for (int kb = 0; kb < 4; ++kb) mf[kb] = *(const bf16x8*)(lds + L_MF + kb * 1024 + lane * 16);
                __builtin_amdgcn_sched_barrier(0);
#pragma unroll
                for (int kb = 0; kb < 10; ++kb) {
                    av = SMFMAC(mf[kb], wvv[kb], av);
                    if (kb + 4 < 10) mf[kb + 4] = *(const bf16x8*)(lds + L_MF + (kb + 4) * 1024 + lane * 16);
                    __builtin_amdgcn_sched_barrier(0);
                }
                const u32x2 p = half_add(av, av);
                char* vd = lds + vm_off;
                *(u16*)(vd + 0) = (u16)p.x; *(u16*)(vd + 16) = (u16)(p.x >> 16); *(u16*)(vd + 32) = (u16)p.y; *(u16*)(vd + 48) = (u16)(p.y >> 16);
                ST2(4);
                BARRIER();
                ST2(5);
                const bf16x8 vf0 = *(const bf16x8*)(lds + L_QF + lane * 16), vf1 = *(const bf16x8*)(lds + L_QF + 1024 + lane * 16);
                f32x4 z[5];
#pragma unroll
                for (int ct = 0; ct < 5; ++ct) { z[ct] = f32x4{0.f, 0.f, 0.f, 0.f}; z[ct] = SMFMAC(vf0, wo[ct][0], z[ct]); }
#pragma unroll
                for (int ct = 0; ct < 5; ++ct) z[ct] = SMFMAC(vf1, wo[ct][1], z[ct]);
                {
                    char* slot = slotp(s);
                    const u32x2 z01 = half_add(z[0], z[1]), z23 = half_add(z[2], z[3]);
                    auto sx = __builtin_amdgcn_permlane16_swap(z01.x, z23.x, false, false);
                    auto sy = __builtin_amdgcn_permlane16_swap(z01.y, z23.y, false, false);
                    st16_sc1(slot, zo4, u32x4{nosent(sx[0]), nosent(sy[0]), nosent(sx[1]), nosent(sy[1])});
                    const u32x2 z4 = half_add(z[4], z[4]);
                    if (lane < 32) st8_sc1(slot, zo1, u32x2{nosent(z4.x), nosent(z4.y)});
                }
                ST2(6);
                ST2F();
                next_target(s);
            }
        }
    } else if (role < NREC) {
        const int ct = role - R_B;
        {
            const u32x4 sv = {~0u, ~0u, ~0u, ~0u};
            for (int q = ct; q < 176; q += NB) {
                const unsigned base = (unsigned)OFF_HO + (q / 44) * (unsigned)MB + (q % 44) * 16384 + tid * 16;
                __builtin_amdgcn_raw_buffer_store_b128(sv, rs, base, 0, 16);
                __builtin_amdgcn_raw_buffer_store_b128(sv, rs, base, 8192, 16);
            }
            arrive(ctrl, C_FILL, tid);
        }
        SS(2);
        if (tid == 0) *(volatile unsigned*)(lds + L_MISC + 68) = 0u;
        const unsigned pol = get_pol();
        SS(3);
        auto slotp = [&](int s) -> char* { return ho + (size_t)(s & 3) * MB + (((pol >> ((s >> 1) & 1)) & 1) ? 8192 : 0); };
        if (wv < 2) {
            unsigned pox[8], pom[2];
#pragma unroll
            for (int i = 0; i < 8; ++i) pox[i] = lmap(SLOT_P + (4 * wv + (i >> 1)) * 2560 + (i & 1) * 1024 + lane * 16);
#pragma unroll
            for (int i = 0; i < 2; ++i) pom[i] = lmap(SLOT_P + (4 * wv + 2 * i + (n >> 3)) * 2560 + 2048 + g * 128 + (n & 7) * 16);
            const unsigned zo = (4 + ct) * 16384 + wv * 4096 + lane * 16;
            const unsigned mo_pub = lmap(SLOT_MEM + (2 * ct + (g >> 1)) * 128 + (n & 7) * 16 + (g & 1) * 8);
            const bool hi = n >= 8;
            unsigned vp = 0;
            u32x4 uf[16];
            auto load_u = [&](int s) {
                if (s == 0) wait_counters(ctrl, C_V0 + 16 * wv * 32, 16, 1, lane);
                else if (vp <= (unsigned)s) vp = wait_counters(ctrl, C_VP + 16 * wv * 32, 16, s + 1, lane);
                const unsigned src = (unsigned)(OFF_UF + ((size_t)s * NB + ct) * 32768) + wv * 16384 + lane * 16;
#pragma unroll
                for (int q = 0; q < 16; ++q) uf[q] = __builtin_amdgcn_raw_buffer_load_b128(rs, src, q * 1024, 16);
#pragma unroll
                for (int q = 0; q < 16; ++q) asm volatile("" : "+v"(uf[q]));
            };
            load_u(0);
            SS(4);
            unsigned gbmin = 85;
            for (int s = 0; s < NSEG; ++s) {
                char* slot = slotp(s);
                u32x4 px[8], pm[2], zz[4];
                unsigned spins = 0, target = 0;
#define B_LOADZ() asm volatile("global_load_dwordx4 %0, %4, %5 sc1\n\tglobal_load_dwordx4 %1, %4, %5 offset:1024 sc1\n\tglobal_load_dwordx4 %2, %4, %5 offset:2048 sc1\n\tglobal_load_dwordx4 %3, %4, %5 offset:3072 sc1" \
                             : "=&v"(zz[0]), "=&v"(zz[1]), "=&v"(zz[2]), "=&v"(zz[3]) : "v"(zo), "s"(slot) : "memory")
#define B_LOADP() asm volatile("global_load_dwordx4 %8, %18, %20 sc1\n\tglobal_load_dwordx4 %9, %19, %20 sc1\n\t" \
                             "global_load_dwordx4 %0, %10, %20 sc1\n\tglobal_load_dwordx4 %1, %11, %20 sc1\n\tglobal_load_dwordx4 %2, %12, %20 sc1\n\tglobal_load_dwordx4 %3, %13, %20 sc1\n\t" \
                             "global_load_dwordx4 %4, %14, %20 sc1\n\tglobal_load_dwordx4 %5, %15, %20 sc1\n\tglobal_load_dwordx4 %6, %16, %20 sc1\n\tglobal_load_dwordx4 %7, %17, %20 sc1" \
                             : "=&v"(px[0]), "=&v"(px[1]), "=&v"(px[2]), "=&v"(px[3]), "=&v"(px[4]), "=&v"(px[5]), "=&v"(px[6]), "=&v"(px[7]), "=&v"(pm[0]), "=&v"(pm[1]) \
                             : "v"(pox[0]), "v"(pox[1]), "v"(pox[2]), "v"(pox[3]), "v"(pox[4]), "v"(pox[5]), "v"(pox[6]), "v"(pox[7]), "v"(pom[0]), "v"(pom[1]), "s"(slot) : "memory")
                if (s < NSEG - 1) {
                    {
                        unsigned spt = 0;
                        while (__builtin_amdgcn_readfirstlane(*(volatile unsigned*)(lds + L_MISC + 68)) != (unsigned)(s + 1)) { __builtin_amdgcn_s_sleep(1); if (++spt > (SPIN_CAP << 4)) break; }
                        target = __builtin_amdgcn_readfirstlane(*(volatile unsigned*)(lds + L_MISC + 64));
                    }
                    ST2(0);
                    wait_rt(target - ZLEAD);
                    B_LOADZ();
                    wait_rt(target);
                    ST2(1);
                    B_LOADP();
                    const f32x4 zero = {0.f, 0.f, 0.f, 0.f};
                    f32x4 xa, xb;
                    unsigned mx;
#define B_MXZ() umax3(umax3(umax3(zz[0].x, zz[0].y, zz[0].z), umax3(zz[0].w, zz[1].x, zz[1].y), umax3(zz[1].z, zz[1].w, zz[2].x)), \
                      umax3(umax3(zz[2].y, zz[2].z, zz[2].w), umax3(zz[3].x, zz[3].y, zz[3].z), umax3(zz[3].w, pm[0].x, pm[0].y)), \
                      umax3(umax3(pm[0].z, pm[0].w, pm[1].x), pm[1].y, max(pm[1].z, pm[1].w)))
#define B_MM() xa = MFMA32(BC8(zz[0]), BC8(pm[0]), zero); xb = MFMA32(BC8(zz[1]), BC8(pm[0]), zero); \
               xa = MFMA32(BC8(zz[2]), BC8(pm[1]), xa); xb = MFMA32(BC8(zz[3]), BC8(pm[1]), xb);
                    asm volatile("s_waitcnt vmcnt(8)" : "+v"(zz[0]), "+v"(zz[1]), "+v"(zz[2]), "+v"(zz[3]), "+v"(pm[0]), "+v"(pm[1]) :: "memory");
                    B_MM();
                    mx = B_MXZ();
                    __builtin_amdgcn_sched_barrier(0);
#define B_STEP(i, cnt) asm volatile("s_waitcnt vmcnt(" #cnt ")" : "+v"(px[i]) :: "memory"); \
                    xa = MFMA32(BC8(uf[2 * i]), BC8(px[i]), xa); xb = MFMA32(BC8(uf[2 * i + 1]), BC8(px[i]), xb); \
                    mx = umax3(mx, umax3(px[i].x, px[i].y, px[i].z), px[i].w); __builtin_amdgcn_sched_barrier(0);
                    B_STEP(0, 7) B_STEP(1, 6) B_STEP(2, 5) B_STEP(3, 4) B_STEP(4, 3) B_STEP(5, 2) B_STEP(6, 1) B_STEP(7, 0)
                    if (!__all(mx != ~0u)) {
                        const unsigned sb = (unsigned)(slot - a.ws);
                        do {
                            if (spin_abort(spins, ctrl)) break;
                            asm volatile("" ::: "memory");
#pragma unroll
                            for (int q = 0; q < 4; ++q) zz[q] = __builtin_amdgcn_raw_buffer_load_b128(rs, zo + q * 1024, sb, 16);
#pragma unroll
                            for (int q = 0; q < 2; ++q) pm[q] = __builtin_amdgcn_raw_buffer_load_b128(rs, pom[q], sb, 16);
#pragma unroll
                            for (int q = 0; q < 8; ++q) px[q] = __builtin_amdgcn_raw_buffer_load_b128(rs, pox[q], sb, 16);
                            mx = B_MXZ();
#pragma unroll
                            for (int q = 0; q < 8; ++q) mx = umax3(mx, umax3(px[q].x, px[q].y, px[q].z), px[q].w);
                        } while (!__all(mx != ~0u));
                        B_MM();
#pragma unroll
                        for (int q = 0; q < 8; ++q) { xa = MFMA32(BC8(uf[2 * q]), BC8(px[q]), xa); xb = MFMA32(BC8(uf[2 * q + 1]), BC8(px[q]), xb); }
                    }
                    ST2(2);
                    f32x4 tot;
#pragma unroll
                    for (int r = 0; r < 4; ++r) {
                        const float y = hi ? xb[r] : xa[r];
                        tot[r] = y + u2f(__builtin_amdgcn_update_dpp(0, (int)f2u(y), 0x128, 0xf, 0xf, false));
                    }
                    if (wv == 1) *(f32x4*)(lds + L_RED + lane * 16) = tot;
                    BARRIER();
                    ST2(3);
                    if (wv == 0) {
                        tot += *(const f32x4*)(lds + L_RED + lane * 16);
                        const u32x2 o = {nosent(pk_bf16(tot[0], tot[1])), nosent(pk_bf16(tot[2], tot[3]))};
                        if (!hi) st8_sc1(slot, mo_pub, o);
                        ST2(7);
                        if (ct == 0) {
                            const unsigned gb = rt32() - target;
                            gbmin = gb < gbmin ? gb : gbmin;
                            if (lane == 0) { st4_sc1(slot + so_b, 0u, gbmin); if (s >= 2) st4_sc1(slotp(s + 2) + so_b, 0u, ~0u); }
                        }
                        if (s >= 2 && !hi) st8_sc1(slotp(s + 2), mo_pub, u32x2{~0u, ~0u});
                    }
                    ST2(4);
                    asm volatile("s_barrier" ::: "memory");
                    ST2F();
                    if (wv == 1 && lane == 0 && s > 0) set_counter(ctrl, C_ZP + ct * 32, s);
                } else {
                    for (;;) {
                        asm volatile("" ::: "memory");
#pragma unroll
                        for (int q = 0; q < 4; ++q) zz[q] = __builtin_amdgcn_raw_buffer_load_b128(rs, zo + q * 1024, (unsigned)(slot - a.ws), 16);
                        const unsigned mx = umax3(umax3(umax3(zz[0].x, zz[0].y, zz[0].z), umax3(zz[0].w, zz[1].x, zz[1].y), umax3(zz[1].z, zz[1].w, zz[2].x)),
                                                  umax3(umax3(zz[2].y, zz[2].z, zz[2].w), umax3(zz[3].x, zz[3].y, zz[3].z), zz[3].w), 0u);
                        if (__all(mx != ~0u)) break;
                        __builtin_amdgcn_s_sleep(2);
                        if (spin_abort(spins, ctrl)) break;
                    }
                }
                const char* zc = a.ws + OFF_ZF + ((size_t)s * NB + ct) * 8192 + wv * 4096;
#pragma unroll
                for (int q = 0; q < 4; ++q) st16_sc1(zc + q * 1024, lane * 16, zz[q]);
                if (s < NSEG - 1) {
                    asm volatile("s_barrier" ::: "memory");
#pragma unroll
                    for (int q = 0; q < 16; ++q) uf[q] = *(const u32x4*)(lds + L_UB + (s & 1) * 32768 + (wv * 16 + q) * 1024 + lane * 16);
#pragma unroll
                    for (int q = 0; q < 16; ++q) asm volatile("" : "+v"(uf[q]));
                }
            }
            asm volatile("s_waitcnt vmcnt(0)" ::: "memory");
        } else {
            unsigned vp = 0;
            for (int s = 0; s < NSEG - 1; ++s) {
                if (wv == 2) {
                    const unsigned t = read_sig(slotp(s) + so_a, ctrl) + DBC;
                    if (lane == 0) { *(volatile unsigned*)(lds + L_MISC + 64) = t; LDSWAIT(); *(volatile unsigned*)(lds + L_MISC + 68) = (unsigned)(s + 1); }
                }
                if (wv >= 4) {
                    const int sn = min(s + 1, NSEG - 2);
                    if (vp <= (unsigned)sn) vp = wait_counters(ctrl, C_VP + 8 * (wv - 4) * 32, 8, sn + 1, lane);
                    unsigned spt = 0;
                    while (__builtin_amdgcn_readfirstlane(*(volatile unsigned*)(lds + L_MISC + 68)) != (unsigned)(s + 1)) { __builtin_amdgcn_s_sleep(1); if (++spt > (SPIN_CAP << 4)) break; }
                    wait_rt(__builtin_amdgcn_readfirstlane(*(volatile unsigned*)(lds + L_MISC + 64)) + 8);
                    const char* src = a.ws + OFF_UF + ((size_t)sn * NB + ct) * 32768 + (wv - 4) * 8192 + lane * 16;
#pragma unroll
                    for (int q = 0; q < 8; ++q) glds16(src + q * 1024, L_UB + (s & 1) * 32768 + (wv - 4) * 8192 + q * 1024);
                }
                asm volatile("s_waitcnt lgkmcnt(0)\n\ts_barrier" ::: "memory");
                asm volatile("s_barrier" ::: "memory");
                if (wv >= 4) {
                    const unsigned o = (4 + ct) * 16384 + (wv - 4) * 2048 + lane * 16;
                    st16_sc1(slotp(s), o, u32x4{~0u, ~0u, ~0u, ~0u}); st16_sc1(slotp(s) + 1024, o, u32x4{~0u, ~0u, ~0u, ~0u});
                }
                asm volatile("s_waitcnt vmcnt(0)\n\ts_barrier" ::: "memory");
            }
        }
        __syncthreads();
        if (tid == 0) set_counter(ctrl, C_ZP + ct * 32, NSEG);
    } else if (role < R_V) {
        const int h = role - R_P;
        bf16x16 wq[10], wk[10];
        load_w2(a.Wq, a.Wk, h, lds, tid, wv, wq, wk);
        SS(2);
        if (wv == 0) wait_counters(ctrl, C_XF, 1, NO, lane);
        __syncthreads();
        prefetch_x(xf, 1, 0, wv, lane);
        prefetch_x(xf, 2, 1, wv, lane);
        int b3 = 0;
        for (int s = 1; s < NSEG; ++s) {
            if (s < 4) asm volatile("s_waitcnt vmcnt(0)" ::: "memory");
            BARRIER();
            if (s > 1 && s < 4 && tid == 0) set_counter(ctrl, C_PP + h * 32, s);
            if (s < 5) SS(2 + s);
            proj_qk<true>(lds + L_XB + b3 * 40960 + lane * 16, wq, wk, lds + L_T + tw_off + g * 64, lds + L_T + 8192 + tw_off + g * 64,
                          xf + (size_t)min(s + 2, NSEG - 1) * 40960 + wv * 5120 + lane * 16, L_XB + (b3 == 0 ? 2 : b3 - 1) * 40960 + wv * 5120);
            b3 = b3 == 2 ? 0 : b3 + 1;
            asm volatile("s_waitcnt vmcnt(5)" ::: "memory");
            BARRIER();
            if (tid == 0) set_counter(ctrl, C_PP + h * 32, s);
            const u32x4 vq = *(const u32x4*)(lds + L_T + tid * 16), vk = *(const u32x4*)(lds + L_T + 8192 + tid * 16);
            st16_sc1(a.ws + OFF_KXF + ((size_t)s * H_ + h) * 8192, tid * 16, vk);
            st16_sc1(a.ws + OFF_QXF + ((size_t)s * H_ + h) * 8192, tid * 16, vq);
        }
        asm volatile("s_waitcnt vmcnt(0)" ::: "memory");
        __syncthreads();
        if (tid == 0) set_counter(ctrl, C_PP + h * 32, NSEG);
    } else if (role < R_O) {
        const int h = role - R_V;
        bf16x16 wvv[10], wo[5][2];
        load_wvo(a.Wv, a.Wo, h, lds, tid, wv, wvv, wo);
        SS(2);
        if (wv == 0) wait_counters(ctrl, C_XF, 1, NO, lane);
        __syncthreads();
        prefetch_x(xf, 1, 0, wv, lane);
        prefetch_x(xf, 2, 1, wv, lane);
        int b3 = 0;
        for (int s = 1; s < NSEG; ++s) {
            if (s < 4) asm volatile("s_waitcnt vmcnt(0)" ::: "memory");
            BARRIER();
            if (s > 1 && s < 4 && tid == 0) set_counter(ctrl, C_VP + h * 32, s);
            if (s < 5) SS(2 + s);
            proj_u<true>(lds, lds + L_XB + b3 * 40960 + lane * 16, wvv, wo, wv, lane, a.ws + OFF_UF + (size_t)s * NB * 32768, h,
                         xf + (size_t)min(s + 2, NSEG - 1) * 40960 + wv * 5120 + lane * 16, L_XB + (b3 == 0 ? 2 : b3 - 1) * 40960 + wv * 5120, ctrl + C_VP + h * 32, s);
            b3 = b3 == 2 ? 0 : b3 + 1;
        }
        asm volatile("s_waitcnt vmcnt(0)" ::: "memory");
        __syncthreads();
        if (tid == 0) set_counter(ctrl, C_VP + h * 32, NSEG);
    } else {
        const int o = role - R_O;
        {
#pragma unroll
            for (int i = 0; i < 5; ++i) {
                const int id = i * NT + tid, l = id & 63, tt = (id >> 6) & 1, hf = (id >> 7) & 1, kb = id >> 8;
                const int ga = l >> 4, tok = 32 * o + 16 * tt + (l & 15), dbase = 64 * kb + 16 * (2 * hf + (ga >> 1)) + 4 * (ga & 1);
                const u32x2 c0 = *(const u32x2*)(a.x + (size_t)tok * D_ + dbase), c1 = *(const u32x2*)(a.x + (size_t)tok * D_ + dbase + 8);
                u32x4 ov = {c0.x, c1.x, c0.y, c1.y};
                __builtin_amdgcn_raw_buffer_store_b128(ov, rs, (unsigned)OFF_XF + o * 40960 + id * 16, 0, 16);
            }
            arrive(ctrl, C_XF, tid);
        }
        SS(2);
        auto obar = [&](int idx) {
            asm volatile("s_waitcnt vmcnt(0)" ::: "memory");
            __syncthreads();
            if (wv == 0) {
                if (lane == 0) __hip_atomic_fetch_add(ctrl + C_OB + idx * 32, 1u, __ATOMIC_RELAXED, __HIP_MEMORY_SCOPE_AGENT);
                wait_counters(ctrl, C_OB + idx * 32, 1, idx == 0 ? NO + NS : NO, lane);
            }
            __syncthreads();
        };
        auto bf2f = [](unsigned short u) -> float { return u2f((unsigned)u << 16); };
        c32* tw = (c32*)(lds + 96 * 1024);
        c32* wbuf = (c32*)(lds) + wv * 720;
        for (int i = tid; i < 640; i += NT) { float sn, cs_; sincospif(-(float)i / 320.f, &sn, &cs_); tw[i] = c32{cs_, sn}; }
        float gwv[10];
#pragma unroll
        for (int j = 0; j < 10; ++j) gwv[j] = bf2f(a.gw[64 * j + lane]);
        __syncthreads();
        for (int task = o; task < 128; task += NO) {
            {
                const int r0 = task * 16 + wv * 2;
                c32 v[10]; float ga = 0.f, gb_ = 0.f;
#pragma unroll
                for (int j = 0; j < 10; ++j) {
                    v[j] = c32{bf2f(a.x[(size_t)r0 * D_ + 64 * j + lane]), bf2f(a.x[(size_t)(r0 + 1) * D_ + 64 * j + lane])};
                    ga += rbf(v[j].x * gwv[j]); gb_ += rbf(v[j].y * gwv[j]);
                }
#pragma unroll
                for (int of = 32; of >= 1; of >>= 1) { ga += __shfl_xor(ga, of); gb_ += __shfl_xor(gb_, of); }
                if (lane < 2) {
                    const float z = rbf(lane ? gb_ : ga);
                    const float gv = rbf(1.f / (1.f + __expf(-z)));
                    __builtin_amdgcn_raw_buffer_store_b32(f2u(gv), rs, (unsigned)(OFF_G + (r0 + lane) * 4), 0, 16);
                }
                fft640(v, wbuf, tw, lane);
                __syncthreads();
                for (int i = tid; i < 321 * 16; i += NT) {
                    const int k = i >> 4, t = i & 15;
                    const c32* zb = (const c32*)lds + (t >> 1) * 720;
                    const c32 z1 = zb[k], z2 = zb[k ? 640 - k : 0];
                    c32 r = (t & 1) ? c32{0.5f * (z1.y + z2.y), 0.5f * (z2.x - z1.x)} : c32{0.5f * (z1.x + z2.x), 0.5f * (z1.y - z2.y)};
                    __builtin_amdgcn_raw_buffer_store_b64(__builtin_bit_cast(u32x2, r), rs, (unsigned)(OFF_X1T + ((size_t)k * S_ + task * 16 + t) * 8), 0, 16);
                }
                __syncthreads();
            }
        }
        for (int i = tid; i < 2048; i += NT) { float sn, cs_; sincospif(-(float)i / 1024.f, &sn, &cs_); tw[i] = c32{cs_, sn}; }
        obar(0);
        {
            const int j = tid & 255, hf = tid >> 8;
            c32* buf = (c32*)lds + hf * 2304;
            auto pd = [](int p) { return p + ((p >> 5) << 2); };
            for (int b = o; b < 161; b += NO) {
                const int col = min(2 * b + hf, 320);
                const unsigned cb = (unsigned)(OFF_X1T + (size_t)col * S_ * 8);
                c32 v[8];
#pragma unroll
                for (int q = 0; q < 8; ++q) v[q] = __builtin_bit_cast(c32, __builtin_amdgcn_raw_buffer_load_b64(rs, cb + (q * 256 + j) * 8, 0, 16));
                const u32x4 wa = __builtin_amdgcn_raw_buffer_load_b128(rs, (unsigned)(OFF_WT + ((size_t)col * S_ + 8 * j) * 4), 0, 16);
                const u32x4 wb = __builtin_amdgcn_raw_buffer_load_b128(rs, (unsigned)(OFF_WT + ((size_t)col * S_ + 8 * j + 4) * 4), 0, 16);
                dft8<-1>(v);
#pragma unroll
                for (int q = 0; q < 8; ++q) buf[pd(q * 256 + j)] = q ? cmul(v[q], tw[j * q]) : v[q];
                __syncthreads();
                const int b2 = (j >> 5) * 256 + (j & 31), lo = j & 31;
#pragma unroll
                for (int q = 0; q < 8; ++q) v[q] = buf[pd(b2 + q * 32)];
                dft8<-1>(v);
#pragma unroll
                for (int q = 0; q < 8; ++q) buf[pd(b2 + q * 32)] = q ? cmul(v[q], tw[8 * lo * q]) : v[q];
                __syncthreads();
                const int b3 = (j >> 2) * 32 + (j & 3), n0 = j & 3;
#pragma unroll
                for (int q = 0; q < 8; ++q) v[q] = buf[pd(b3 + q * 4)];
                dft8<-1>(v);
#pragma unroll
                for (int q = 0; q < 8; ++q) buf[pd(b3 + q * 4)] = q ? cmul(v[q], tw[64 * n0 * q]) : v[q];
                __syncthreads();
#pragma unroll
                for (int q = 0; q < 8; ++q) v[q] = buf[pd(8 * j + q)];
                dft4<-1>(v[0], v[1], v[2], v[3]); dft4<-1>(v[4], v[5], v[6], v[7]);
                const unsigned wv8[8] = {wa.x, wa.y, wa.z, wa.w, wb.x, wb.y, wb.z, wb.w};
#pragma unroll
                for (int q = 0; q < 8; ++q) v[q] = cmul(v[q], c32{u2f(wv8[q] << 16), u2f(wv8[q] & 0xFFFF0000u)});
                dft4<1>(v[0], v[1], v[2], v[3]); dft4<1>(v[4], v[5], v[6], v[7]);
#pragma unroll
                for (int q = 0; q < 8; ++q) buf[pd(8 * j + q)] = v[q];
                __syncthreads();
#pragma unroll
                for (int q = 0; q < 8; ++q) { c32 t = buf[pd(b3 + q * 4)]; v[q] = q ? cmulc(t, tw[64 * n0 * q]) : t; }
                dft8<1>(v);
#pragma unroll
                for (int q = 0; q < 8; ++q) buf[pd(b3 + q * 4)] = v[q];
                __syncthreads();
#pragma unroll
                for (int q = 0; q < 8; ++q) { c32 t = buf[pd(b2 + q * 32)]; v[q] = q ? cmulc(t, tw[8 * lo * q]) : t; }
                dft8<1>(v);
#pragma unroll
                for (int q = 0; q < 8; ++q) buf[pd(b2 + q * 32)] = v[q];
                __syncthreads();
#pragma unroll
                for (int q = 0; q < 8; ++q) { c32 t = buf[pd(q * 256 + j)]; v[q] = q ? cmulc(t, tw[j * q]) : t; }
                dft8<1>(v);
                if (2 * b + hf <= 320) {
#pragma unroll
                    for (int q = 0; q < 8; ++q) __builtin_amdgcn_raw_buffer_store_b64(__builtin_bit_cast(u32x2, v[q]), rs, cb + (q * 256 + j) * 8, 0, 16);
                }
                __syncthreads();
            }
        }
        for (int i = tid; i < 640; i += NT) { float sn, cs_; sincospif(-(float)i / 320.f, &sn, &cs_); tw[i] = c32{cs_, sn}; }
        obar(1);
        for (int task = o; task < 128; task += NO) {
            for (int i = tid; i < 321 * 8; i += NT) {
                const int k = i >> 3, p = i & 7;
                u32x4 y = __builtin_amdgcn_raw_buffer_load_b128(rs, (unsigned)(OFF_X1T + ((size_t)k * S_ + task * 16 + 2 * p) * 8), 0, 16);
                float ar = u2f(y.x), ai = u2f(y.y), br = u2f(y.z), bi = u2f(y.w);
                if (k == 0 || k == 320) { ai = 0.f; bi = 0.f; }
                c32* zb = (c32*)lds + p * 720;
                zb[k] = c32{ar - bi, -(ai + br)};
                if (k > 0 && k < 320) zb[640 - k] = c32{ar + bi, -(br - ai)};
            }
            __syncthreads();
            c32 v[10];
#pragma unroll
            for (int j = 0; j < 10; ++j) v[j] = wbuf[64 * j + lane];
            LDSWAIT();
            fft640(v, wbuf, tw, lane);
            const float sc = 1.f / (2048.f * 640.f);
            const int r0 = task * 16 + wv * 2;
            for (int i = lane; i < 320; i += 64) {
                const c32 z0 = wbuf[2 * i], z1 = wbuf[2 * i + 1];
                __builtin_amdgcn_raw_buffer_store_b32(pk_bf16(z0.x * sc, z1.x * sc), rs, (unsigned)(OFF_A + ((size_t)r0 * D_ + 2 * i) * 2), 0, 16);
                __builtin_amdgcn_raw_buffer_store_b32(pk_bf16(-z0.y * sc, -z1.y * sc), rs, (unsigned)(OFF_A + ((size_t)(r0 + 1) * D_ + 2 * i) * 2), 0, 16);
            }
            __syncthreads();
        }
        obar(2);
        if (o < 60) {
            const int grp = o / 5, c = o - grp * 5;
            const int ct = 8 * c + wv;
            const bool hi = n >= 8;
            unsigned vp = 0, zp = 0, ap = 0;
            for (int it = 0; it < 6; ++it) {
                int s = grp + 12 * it, qlo = 0, qhi = 4;
                if (it == 5) {
                    if (grp < 2) s = 60 + grp;
                    else if (grp < 4) { s = 62; qlo = 2 * (grp - 2); qhi = qlo + 2; }
                    else if (grp < 8) { s = 63; qlo = grp - 4; qhi = qlo + 1; }
                    else break;
                }
                if (s == 0) wait_counters(ctrl, C_V0, 32, 1, lane);
                else if (vp <= (unsigned)s) vp = wait_counters(ctrl, C_VP, 32, s + 1, lane);
                u32x4 uf[32];
#pragma unroll
                for (int i = 0; i < 32; ++i) uf[i] = __builtin_amdgcn_raw_buffer_load_b128(rs, (unsigned)(OFF_UF + ((size_t)s * NB + ct) * 32768) + lane * 16, i * 1024, 16);
                const int et = tid >> 4, c8 = (tid & 15) * 8, row = 32 * s + et;
                const u32x4 av = __builtin_amdgcn_raw_buffer_load_b128(rs, (unsigned)(OFF_A + ((size_t)row * D_ + 128 * c + c8) * 2), 0, 16);
                const float gv = u2f(__builtin_amdgcn_raw_buffer_load_b32(rs, (unsigned)(OFF_G + row * 4), 0, 16));
                if (zp <= (unsigned)s) zp = wait_counters(ctrl, C_ZP + ct * 32, 1, s + 1, lane);
                u32x4 zq[8];
#pragma unroll
                for (int i = 0; i < 8; ++i) zq[i] = __builtin_amdgcn_raw_buffer_load_b128(rs, (unsigned)(OFF_ZF + ((size_t)s * NB + ct) * 8192) + lane * 16, i * 1024, 16);
                if (ap <= (unsigned)s) ap = wait_counters(ctrl, C_AP, 32, s + 1, lane);
                for (int c0 = qlo * 1280; c0 < qhi * 1280; c0 += 2560) {
                    u32x4 t[5];
#pragma unroll
                    for (int i = 0; i < 5; ++i) t[i] = __builtin_amdgcn_raw_buffer_load_b128(rs, (unsigned)(OFF_PXF + (size_t)s * 81920) + min(c0 + i * NT + tid, 5119) * 16, 0, 16);
#pragma unroll
                    for (int i = 0; i < 5; ++i) *(u32x4*)(lds + min(c0 + i * NT + tid, 5119) * 16) = t[i];
                }
                __syncthreads();
                float* tile = (float*)(lds + 81920);
                const unsigned pmo = 2048 + g * 128 + (n & 7) * 16;
#pragma unroll
                for (int qo = 0; qo < 4; ++qo) {
                    if (qo < qlo || qo >= qhi) continue;
                    f32x4 xa = {0.f, 0.f, 0.f, 0.f}, xb = xa, ma = xa;
#pragma unroll
                    for (int wg = 0; wg < 8; ++wg) {
                        const char* pbp = lds + qo * 20480 + wg * 2560;
                        const bf16x8 p0 = *(const bf16x8*)(pbp + lane * 16), p1 = *(const bf16x8*)(pbp + 1024 + lane * 16);
                        const bf16x8 p2 = *(const bf16x8*)(pbp + pmo);
                        xa = MFMA32(BC8(uf[4 * wg]), p0, xa); xb = MFMA32(BC8(uf[4 * wg + 1]), p0, xb);
                        xa = MFMA32(BC8(uf[4 * wg + 2]), p1, xa); xb = MFMA32(BC8(uf[4 * wg + 3]), p1, xb);
                        ma = MFMA32(BC8(zq[wg]), p2, ma);
                    }
                    f32x4 tot;
#pragma unroll
                    for (int r = 0; r < 4; ++r) {
                        const float y = hi ? xb[r] : xa[r] + ma[r];
                        tot[r] = y + u2f(__builtin_amdgcn_update_dpp(0, (int)f2u(y), 0x128, 0xf, 0xf, false));
                    }
                    if (n < 8) *(f32x4*)(tile + (8 * qo + n) * 132 + wv * 16 + 4 * g) = tot;
                }
                __syncthreads();
                if (et >= 8 * qlo && et < 8 * qhi) {
                    const f32x4 b0 = *(const f32x4*)(tile + et * 132 + c8), b1 = *(const f32x4*)(tile + et * 132 + c8 + 4);
                    const float og = rbf(1.f - gv);
                    auto mix = [&](unsigned aw, float x0, float x1) -> unsigned {
                        return pk_bf16(rbf(gv * u2f(aw << 16)) + rbf(og * rbf(x0)), rbf(gv * u2f(aw & 0xFFFF0000u)) + rbf(og * rbf(x1)));
                    };
                    u32x4 ov = {mix(av.x, b0[0], b0[1]), mix(av.y, b0[2], b0[3]), mix(av.z, b1[0], b1[1]), mix(av.w, b1[2], b1[3])};
                    *(u32x4*)(a.out + (size_t)row * D_ + 128 * c + c8) = ov;
                }
                __syncthreads();
            }
        }
    }
}

extern "C" void kernel_launch(void* const* d_in, const int* in_sizes, int n_in, void* d_out, int out_size, void* d_ws, size_t ws_size, hipStream_t stream) {
    char* ws = (char*)(((uintptr_t)d_ws + 2 * MB - 1) & ~(uintptr_t)(2 * MB - 1));
    (void)hipMemsetAsync(ws + OFF_CTRL, 0, CTRL_WORDS * 4, stream);
    MainArgs a;
    a.x = (const u16*)d_in[0]; a.mem0 = (const u16*)d_in[4]; a.Wq = (const u16*)d_in[5]; a.Wk = (const u16*)d_in[6]; a.Wv = (const u16*)d_in[7]; a.Wo = (const u16*)d_in[8];
    a.wr = (const u16*)d_in[1]; a.wi = (const u16*)d_in[2]; a.gw = (const u16*)d_in[10]; a.ws = ws; a.out = (u16*)d_out;
    (void)hipFuncSetAttribute((const void*)k_main, hipFuncAttributeMaxDynamicSharedMemorySize, LDS_BYTES);
    k_main<<<256, NT, LDS_BYTES, stream>>>(a);
}
```

```cpp
#include <hip/hip_runtime.h>
#include <hip/hip_bf16.h>
#include <stdint.h>

#define LAS __attribute__((address_space(3)))
typedef __attribute__((ext_vector_type(8))) __bf16 bf16x8;
typedef __attribute__((ext_vector_type(16))) __bf16 bf16x16;
typedef __attribute__((ext_vector_type(4))) short s16x4;
typedef __attribute__((ext_vector_type(8))) short s16x8;
typedef __attribute__((ext_vector_type(16))) short s16x16;
typedef __attribute__((ext_vector_type(4))) float f32x4;
typedef __attribute__((ext_vector_type(4))) unsigned u32x4;
typedef __attribute__((ext_vector_type(2))) unsigned u32x2;
typedef unsigned short u16;
typedef __amdgpu_buffer_rsrc_t rsrc_t;

namespace gm {
constexpr int S_ = 2048, D_ = 640, H_ = 32, E_ = 128, NSEG = 64;
constexpr int NA = 32, NM = 32, NB = 40, NREC = NA + NM + NB, NP = 32, NV = 32, NO = 64;
constexpr int R_M = NA, R_B = NA + NM, R_P = NREC, R_V = R_P + NP, R_O = R_V + NV, R_S = R_O + NO;
constexpr int NS = 256 - R_S;
constexpr int NT = 512;
constexpr size_t MB = 1024 * 1024;
constexpr size_t OFF_HO = 0;
constexpr size_t OFF_CTRL = 4 * MB;
constexpr size_t OFF_XF = 5 * MB;
constexpr size_t OFF_KXF = 8 * MB;
constexpr size_t OFF_QXF = 24 * MB;
constexpr size_t OFF_UF = 40 * MB;
constexpr size_t OFF_ZF = 120 * MB;
constexpr size_t OFF_KMF = 140 * MB;
constexpr size_t OFF_PXF = 144 * MB;
constexpr size_t OFF_X1T = 150 * MB;
constexpr size_t OFF_WT = 156 * MB;
constexpr size_t OFF_A = 159 * MB;
constexpr size_t OFF_G = 162 * MB;
constexpr size_t OFF_DBG = 200 * MB;
constexpr int SLOT_P = 0, SLOT_MEM = 20480, SLOT_SIGA = 30720, SLOT_SIGB = 30848, SLOT_PXL = 52 * 8192;
constexpr int C_POL = 0, C_ABORT = 32, C_CNT = 64, C_CNT0 = C_CNT, C_CNT1 = C_CNT + 128;
constexpr int C_PP = 320, C_VP = C_PP + 32 * 32, C_AP = C_VP + 32 * 32, C_ZP = C_AP + 32 * 32, C_ATT = C_ZP + 40 * 32, C_OB = C_ATT + 64 * 32,
              C_XF = C_OB + 4 * 32, C_FILL = C_XF + 32, C_V0 = C_ATT, C_END = C_FILL + 32;
constexpr int CTRL_WORDS = 6976;
static_assert(C_END <= CTRL_WORDS, "ctrl");
constexpr unsigned SPIN_CAP = 1u << 20;
}
using namespace gm;

__device__ __forceinline__ unsigned xcc_id() { return (unsigned)__builtin_amdgcn_s_getreg(20 | (3 << 11)) & 0xfu; }
__host__ __device__ constexpr unsigned lmap(unsigned L) { return ((L >> 13) << 14) + (L & 8191u); }
__device__ __forceinline__ unsigned pk_bf16(float a, float b) {
    typedef __attribute__((ext_vector_type(2))) __bf16 bf2; typedef __attribute__((ext_vector_type(2))) float f2;
    f2 v = {a, b}; bf2 r = __builtin_convertvector(v, bf2); return __builtin_bit_cast(unsigned, r);
}
__device__ __forceinline__ unsigned f2u(float a) { return __builtin_bit_cast(unsigned, a); }
__device__ __forceinline__ float u2f(unsigned a) { return __builtin_bit_cast(float, a); }
__device__ __forceinline__ float rbf(float a) { return __builtin_bit_cast(float, pk_bf16(a, 0.f) << 16); }
__device__ __forceinline__ unsigned nosent(unsigned w) { return w < 0xFFFFFFFEu ? w : 0xFFFFFFFEu; }
#define BARRIER() do { asm volatile("s_waitcnt lgkmcnt(0)\n\ts_barrier" ::: "memory"); } while (0)
__device__ __forceinline__ bf16x8 lo8(bf16x16 w) { return __builtin_shufflevector(w, w, 0, 1, 2, 3, 4, 5, 6, 7); }
__device__ __forceinline__ bf16x8 hi8(bf16x16 w) { return __builtin_shufflevector(w, w, 8, 9, 10, 11, 12, 13, 14, 15); }
__host__ __device__ __forceinline__ int k2d(int k) { int G = k >> 4, L = k & 15, q = L >> 2, p = L & 3; return 16 * G + (p < 2 ? 2 * q + p : 8 + 2 * q + (p - 2)); }
__host__ __device__ __forceinline__ int frag_k(int gb, int jb) { return jb < 8 ? 8 * gb + jb : 32 + 8 * gb + (jb - 8); }
__device__ __forceinline__ void glds16(const void* gsrc, unsigned lds_dst) {
    unsigned keep;
    asm volatile("s_mov_b32 %0, m0\n\ts_mov_b32 m0, %2\n\ts_nop 0\n\tglobal_load_lds_dwordx4 %1, off sc1\n\ts_mov_b32 m0, %0"
                 : "=&s"(keep) : "v"(gsrc), "s"(lds_dst) : "memory");
}
typedef __attribute__((ext_vector_type(2))) float c32;
__device__ __forceinline__ c32 cmul(c32 a, c32 b) { return c32{a.x * b.x - a.y * b.y, a.x * b.y + a.y * b.x}; }
__device__ __forceinline__ c32 cmulc(c32 a, c32 b) { return c32{a.x * b.x + a.y * b.y, a.y * b.x - a.x * b.y}; }
template <int SG> __device__ __forceinline__ c32 rot90(c32 a) { return SG < 0 ? c32{a.y, -a.x} : c32{-a.y, a.x}; }
template <int SG> __device__ __forceinline__ void dft4(c32& a0, c32& a1, c32& a2, c32& a3) {
    c32 t0 = a0 + a2, t1 = a0 - a2, t2 = a1 + a3, t3 = rot90<SG>(a1 - a3);
    a0 = t0 + t2; a1 = t1 + t3; a2 = t0 - t2; a3 = t1 - t3;
}
template <int SG> __device__ __forceinline__ void dft8(c32 (&v)[8]) {
    const float r = 0.70710678118654752f;
    c32 e0 = v[0], e1 = v[2], e2 = v[4], e3 = v[6], o0 = v[1], o1 = v[3], o2 = v[5], o3 = v[7];
    dft4<SG>(e0, e1, e2, e3); dft4<SG>(o0, o1, o2, o3);
    c32 p1 = (SG < 0) ? c32{(o1.x + o1.y) * r, (o1.y - o1.x) * r} : c32{(o1.x - o1.y) * r, (o1.y + o1.x) * r};
    c32 p2 = rot90<SG>(o2);
    c32 p3 = (SG < 0) ? c32{(o3.y - o3.x) * r, -(o3.x + o3.y) * r} : c32{-(o3.x + o3.y) * r, (o3.x - o3.y) * r};
    v[0] = e0 + o0; v[4] = e0 - o0; v[1] = e1 + p1; v[5] = e1 - p1; v[2] = e2 + p2; v[6] = e2 - p2; v[3] = e3 + p3; v[7] = e3 - p3;
}
__device__ __forceinline__ void dft5f(c32& a0, c32& a1, c32& a2, c32& a3, c32& a4) {
    const float c1 = 0.30901699437494742f, c2 = -0.80901699437494742f, s1 = 0.95105651629515357f, s2 = 0.58778525229247313f;
    c32 t1 = a1 + a4, t2 = a2 + a3, t3 = a1 - a4, t4 = a2 - a3;
    c32 m1 = a0 + c1 * t1 + c2 * t2, m2 = a0 + c2 * t1 + c1 * t2;
    c32 n1 = s1 * t3 + s2 * t4, n2 = s2 * t3 - s1 * t4;
    a0 = a0 + t1 + t2;
    a1 = c32{m1.x + n1.y, m1.y - n1.x}; a4 = c32{m1.x - n1.y, m1.y + n1.x};
    a2 = c32{m2.x + n2.y, m2.y - n2.x}; a3 = c32{m2.x - n2.y, m2.y + n2.x};
}
__device__ __forceinline__ void dft10f(c32 (&v)[10]) {
    c32 a[5], b[5];
#pragma unroll
    for (int n2 = 0; n2 < 5; ++n2) { c32 x0 = v[(2 * n2) % 10], x1 = v[(5 + 2 * n2) % 10]; a[n2] = x0 + x1; b[n2] = x0 - x1; }
    dft5f(a[0], a[1], a[2], a[3], a[4]); dft5f(b[0], b[1], b[2], b[3], b[4]);
#pragma unroll
    for (int k2 = 0; k2 < 5; ++k2) { v[(6 * k2) % 10] = a[k2]; v[(5 + 6 * k2) % 10] = b[k2]; }
}
#define LDSWAIT() asm volatile("s_waitcnt lgkmcnt(0)" ::: "memory")
__device__ __forceinline__ void fft640(c32 (&v)[10], c32* buf, const c32* tw, int lane) {
    dft10f(v);
#pragma unroll
    for (int k1 = 0; k1 < 10; ++k1) { c32 t = k1 ? cmul(v[k1], tw[lane * k1]) : v[k1]; buf[k1 * 72 + (lane >> 3) * 9 + (lane & 7)] = t; }
    LDSWAIT();
    c32 w[2][8];
#pragma unroll
    for (int rd = 0; rd < 2; ++rd) {
        const int id = lane + 64 * rd;
        if (id < 80) {
            const int k1 = id >> 3, b = id & 7;
#pragma unroll
            for (int a = 0; a < 8; ++a) w[rd][a] = buf[k1 * 72 + a * 9 + b];
            dft8<-1>(w[rd]);
#pragma unroll
            for (int c = 1; c < 8; ++c) w[rd][c] = cmul(w[rd][c], tw[10 * b * c]);
        }
    }
    LDSWAIT();
#pragma unroll
    for (int rd = 0; rd < 2; ++rd) {
        const int id = lane + 64 * rd;
        if (id < 80) {
            const int k1 = id >> 3, b = id & 7;
#pragma unroll
            for (int c = 0; c < 8; ++c) buf[k1 * 72 + c * 9 + b] = w[rd][c];
        }
    }
    LDSWAIT();
#pragma unroll
    for (int rd = 0; rd < 2; ++rd) {
        const int id = lane + 64 * rd;
        if (id < 80) {
            const int k1 = id >> 3, c = id & 7;
#pragma unroll
            for (int b = 0; b < 8; ++b) w[rd][b] = buf[k1 * 72 + c * 9 + b];
            dft8<-1>(w[rd]);
        }
    }
    LDSWAIT();
#pragma unroll
    for (int rd = 0; rd < 2; ++rd) {
        const int id = lane + 64 * rd;
        if (id < 80) {
            const int k1 = id >> 3, c = id & 7;
#pragma unroll
            for (int d = 0; d < 8; ++d) buf[k1 + 10 * (c + 8 * d)] = w[rd][d];
        }
    }
    LDSWAIT();
}

struct MainArgs {
    const u16* x; const u16* mem0; const u16* Wq; const u16* Wk; const u16* Wv; const u16* Wo; const u16* wr; const u16* wi; const u16* gw; char* ws; u16* out;
};
#define DBC 16
#define DAC 16
#define ATSLEEP 8
#define ZLEAD 6
constexpr int L_XB = 0;
constexpr int L_MF = 81920;
constexpr int L_QF = 92160;
constexpr int L_KM = 96256;
constexpr int L_KX = 100352;
constexpr int L_T = 122880;
constexpr int L_RED = 0;
constexpr int L_UB = 8192;
constexpr int L_MISC = 139264;
constexpr int L_PROF = 140 * 1024;
constexpr int LDS_BYTES = 152 * 1024;

__device__ __forceinline__ unsigned rt32() { return (unsigned)__builtin_amdgcn_s_memrealtime(); }
__device__ __forceinline__ void wait_rt(unsigned target) {
    if ((int)(target - rt32()) > 2000) return;
    while ((int)(rt32() - target) < 0) {}
}
__device__ __forceinline__ float vmax(float a, float b) { float r; asm("v_max_f32 %0, %1, %2" : "=v"(r) : "v"(a), "v"(b)); return r; }
__device__ __forceinline__ float vmax3(float a, float b, float c) { float r; asm("v_max3_f32 %0, %1, %2, %3" : "=v"(r) : "v"(a), "v"(b), "v"(c)); return r; }
__device__ __forceinline__ unsigned umax3(unsigned a, unsigned b, unsigned c) { return max(max(a, b), c); }
__device__ __forceinline__ bool spin_abort(unsigned& spins, unsigned* ctrl) {
    ++spins;
    if (spins > SPIN_CAP) { __hip_atomic_store(ctrl + C_ABORT, 1u, __ATOMIC_RELAXED, __HIP_MEMORY_SCOPE_AGENT); return true; }
    if ((spins & 1023u) == 0u) return __builtin_amdgcn_readfirstlane(__hip_atomic_load(ctrl + C_ABORT, __ATOMIC_RELAXED, __HIP_MEMORY_SCOPE_AGENT)) != 0;
    return false;
}
__device__ __forceinline__ unsigned read_sig(const char* p, unsigned* ctrl) {
    unsigned v, spins = 0;
    for (;;) {
        asm volatile("v_mov_b32 %0, 0\n\tglobal_load_dword %0, %0, %1 sc1\n\ts_waitcnt vmcnt(0)" : "=&v"(v) : "s"(p) : "memory");
        v = __builtin_amdgcn_readfirstlane(v);
        if (v != ~0u) break;
        if (spin_abort(spins, ctrl)) break;
    }
    return v;
}
__device__ __forceinline__ unsigned wait_counters(unsigned* ctrl, int base, int cnt, unsigned need, int lane) {
    unsigned spins = 0, m;
    const unsigned off = (unsigned)(base + (lane < cnt ? lane : 0) * 32) * 4u;
    for (;;) {
        unsigned v;
        asm volatile("global_load_dword %0, %1, %2 sc1\n\ts_waitcnt vmcnt(0)" : "=&v"(v) : "v"(off), "s"(ctrl) : "memory");
        if (cnt > 1) {
#pragma unroll
            for (int o = 16; o >= 1; o >>= 1) v = min(v, (unsigned)__shfl_xor((int)v, o));
        }
        m = __builtin_amdgcn_readfirstlane(v);
        if (m >= need) break;
        __builtin_amdgcn_s_sleep(4);
        if (spin_abort(spins, ctrl)) break;
    }
    return m;
}
__device__ __forceinline__ void set_counter(unsigned* ctrl, int idx, unsigned v) { __hip_atomic_store(ctrl + idx, v, __ATOMIC_RELAXED, __HIP_MEMORY_SCOPE_AGENT); }
__device__ __forceinline__ void st16_sc1(const void* base, unsigned off, u32x4 v) { asm volatile("global_store_dwordx4 %0, %1, %2 sc1\n\ts_nop 1" :: "v"(off), "v"(v), "s"(base) : "memory"); }
__device__ __forceinline__ void st8_sc1(const void* base, unsigned off, u32x2 v) { asm volatile("global_store_dwordx2 %0, %1, %2 sc1\n\ts_nop 1" :: "v"(off), "v"(v), "s"(base) : "memory"); }
__device__ __forceinline__ void st4_sc1(const void* base, unsigned off, unsigned v) { asm volatile("global_store_dword %0, %1, %2 sc1" :: "v"(off), "v"(v), "s"(base) : "memory"); }
__device__ __forceinline__ void load_mem0(const u16* mem0, char* lds, int tid) {
    for (int c = tid; c < 640; c += NT) {
        const int kb = c >> 6, l = c & 63;
        *(u32x4*)(lds + L_MF + c * 16) = *(const u32x4*)(mem0 + (l & 7) * D_ + 64 * kb + 16 * (l >> 4) + 8 * ((l >> 3) & 1));
    }
}
__device__ __forceinline__ void prefetch_x(const char* xf, int seg, int buf, int wv, int lane) {
    const char* src = xf + (size_t)seg * 40960 + wv * 5120 + lane * 16;
    const unsigned dst = L_XB + buf * 40960 + wv * 5120;
#pragma unroll
    for (int i = 0; i < 5; ++i) glds16(src + i * 1024, dst + i * 1024);
}
#define ST2(k) do {} while (0)
#define ST2F() do {} while (0)
#define MFMA32(a, b, c) __builtin_amdgcn_mfma_f32_16x16x32_bf16((a), (b), (c), 0, 0, 0)
#define MFMA16(a, b, c) __builtin_amdgcn_mfma_f32_16x16x16bf16_1k((a), (b), (c), 0, 0, 0)
#define SMFMAC(a, b, c) __builtin_amdgcn_smfmac_f32_16x16x64_bf16((a), (b), (c), sidx, 0, 0)
#define BC8(x) __builtin_bit_cast(bf16x8, (x))
#define BC4(x) __builtin_bit_cast(s16x4, (x))
__device__ __forceinline__ bf16x16 cat4(s16x4 a, s16x4 b, s16x4 c, s16x4 d) {
    s16x8 x = __builtin_shufflevector(a, b, 0, 1, 2, 3, 4, 5, 6, 7), y = __builtin_shufflevector(c, d, 0, 1, 2, 3, 4, 5, 6, 7);
    return __builtin_bit_cast(bf16x16, (s16x16)__builtin_shufflevector(x, y, 0, 1, 2, 3, 4, 5, 6, 7, 8, 9, 10, 11, 12, 13, 14, 15));
}
#define TRREAD(p) __builtin_amdgcn_ds_read_tr16_b64_v4i16((LAS s16x4*)(p))
__device__ __forceinline__ void load_w2(const u16* Wa, const u16* Wb, int h, char* lds, int tid, int wv, bf16x16 (&wa)[10], bf16x16 (&wb)[10]) {
    const int lane = tid & 63, g = lane >> 4, n = lane & 15;
    u32x4 st[10][4];
    const unsigned wo_ = (unsigned)(((tid >> 4) * H_ + h) * E_ + (tid & 15) * 8) * 2u;
#pragma unroll
    for (int kb = 0; kb < 10; ++kb)
#pragma unroll
        for (int i = 0; i < 4; ++i) {
            const char* W = (const char*)((i & 2) ? Wb : Wa) + (size_t)(64 * kb + 32 * (i & 1)) * (H_ * E_ * 2);
            st[kb][i] = *(const u32x4*)(W + wo_);
        }
    const int q4 = n >> 2, p4 = n & 3;
#pragma unroll
    for (int kb = 0; kb < 10; ++kb) {
        char* sb = lds + (kb & 1) * 33792;
#pragma unroll
        for (int i = 0; i < 4; ++i)
            *(u32x4*)(sb + ((tid >> 4) + 32 * (i & 1)) * 528 + (i >> 1) * 256 + (tid & 15) * 16) = st[kb][i];
        BARRIER();
        s16x4 r[2][4];
#pragma unroll
        for (int tl = 0; tl < 2; ++tl)
#pragma unroll
            for (int u = 0; u < 4; ++u) r[tl][u] = TRREAD(sb + k2d(frag_k(g, 4 * u + q4)) * 528 + tl * 256 + (16 * wv + 4 * p4) * 2);
        wa[kb] = cat4(r[0][0], r[0][1], r[0][2], r[0][3]);
        wb[kb] = cat4(r[1][0], r[1][1], r[1][2], r[1][3]);
        asm volatile("" : "+v"(wa[kb]), "+v"(wb[kb]));
    }
    BARRIER();
}
__device__ __forceinline__ void load_wvo(const u16* Wv, const u16* Wo, int h, char* lds, int tid, int wv, bf16x16 (&wvv)[10], bf16x16 (&wo)[5][2]) {
    const int lane = tid & 63, g = lane >> 4, n = lane & 15;
    u32x4 st[10][2], so[2][10];
    const unsigned wo_ = (unsigned)(((tid >> 4) * H_ + h) * E_ + (tid & 15) * 8) * 2u;
#pragma unroll
    for (int kb = 0; kb < 10; ++kb)
#pragma unroll
        for (int i = 0; i < 2; ++i) st[kb][i] = *(const u32x4*)((const char*)Wv + (size_t)(64 * kb + 32 * i) * (H_ * E_ * 2) + wo_);
#pragma unroll
    for (int t = 0; t < 2; ++t)
#pragma unroll
        for (int i = 0; i < 10; ++i) so[t][i] = *(const u32x4*)((const char*)Wo + ((size_t)h * E_ + 64 * t) * (D_ * 2) + (size_t)i * (NT * 16) + tid * 16);
    const int q4 = n >> 2, p4 = n & 3;
#pragma unroll
    for (int kb = 0; kb < 10; ++kb) {
        char* sb = lds + (kb & 1) * 17408;
#pragma unroll
        for (int i = 0; i < 2; ++i) *(u32x4*)(sb + ((tid >> 4) + 32 * i) * 272 + (tid & 15) * 16) = st[kb][i];
        BARRIER();
        s16x4 r[4];
#pragma unroll
        for (int u = 0; u < 4; ++u) r[u] = TRREAD(sb + k2d(frag_k(g, 4 * u + q4)) * 272 + (16 * wv + 4 * p4) * 2);
        wvv[kb] = cat4(r[0], r[1], r[2], r[3]);
        asm volatile("" : "+v"(wvv[kb]));
    }
#pragma unroll
    for (int t = 0; t < 2; ++t) {
        BARRIER();
#pragma unroll
        for (int i = 0; i < 10; ++i) { const int c = i * NT + tid, e = c / 80, cc = c - e * 80; *(u32x4*)(lds + e * 1296 + cc * 16) = so[t][i]; }
        BARRIER();
#pragma unroll
        for (int ct = 0; ct < 5; ++ct) {
            s16x4 r[4];
#pragma unroll
            for (int u = 0; u < 4; ++u) r[u] = TRREAD(lds + k2d(frag_k(g, 4 * u + q4)) * 1296 + (16 * (5 * wv + ct) + 4 * p4) * 2);
            wo[ct][t] = cat4(r[0], r[1], r[2], r[3]);
            asm volatile("" : "+v"(wo[ct][t]));
        }
    }
    BARRIER();
}
__device__ __forceinline__ void tw_store(char* dst, f32x4 a0, f32x4 a1) {
    const unsigned p0 = pk_bf16(a0[0], a0[1]), p1 = pk_bf16(a0[2], a0[3]), p2 = pk_bf16(a1[0], a1[1]), p3 = pk_bf16(a1[2], a1[3]);
    *(u16*)(dst + 0) = (u16)p0; *(u16*)(dst + 16) = (u16)(p0 >> 16); *(u16*)(dst + 32) = (u16)p1; *(u16*)(dst + 48) = (u16)(p1 >> 16);
    *(u16*)(dst + 4096) = (u16)p2; *(u16*)(dst + 4096 + 16) = (u16)(p2 >> 16); *(u16*)(dst + 4096 + 32) = (u16)p3; *(u16*)(dst + 4096 + 48) = (u16)(p3 >> 16);
}
__device__ __forceinline__ void arrive(unsigned* ctrl, int idx, int tid) {
    asm volatile("s_waitcnt vmcnt(0)" ::: "memory");
    __syncthreads();
    if (tid == 0) __hip_atomic_fetch_add(ctrl + idx, 1u, __ATOMIC_RELAXED, __HIP_MEMORY_SCOPE_AGENT);
}
__device__ __forceinline__ u32x2 half_add(f32x4 a, f32x4 b) {
    float cv[4];
#pragma unroll
    for (int r = 0; r < 4; ++r) {
        auto sw = __builtin_amdgcn_permlane32_swap(f2u(a[r]), f2u(b[r]), false, false);
        cv[r] = u2f(sw[0]) + u2f(sw[1]);
    }
    return u32x2{pk_bf16(cv[0], cv[1]), pk_bf16(cv[2], cv[3])};
}

template <bool PF>
__device__ __forceinline__ void proj_qk(const char* xb, const bf16x16 (&wq)[10], const bf16x16 (&wk)[10], char* qdst, char* kdst, const char* psrc, unsigned pdst) {
    f32x4 q0 = {0.f, 0.f, 0.f, 0.f}, q1 = q0, k0 = q0, k1 = q0;
    bf16x8 xr[10][4];
#pragma unroll
    for (int i = 0; i < 4; ++i) xr[0][i] = *(const bf16x8*)(xb + i * 1024);
#pragma unroll
    for (int kb = 0; kb < 10; ++kb) {
        if (kb + 1 < 10) {
#pragma unroll
            for (int i = 0; i < 4; ++i) xr[kb + 1][i] = *(const bf16x8*)(xb + ((kb + 1) * 4 + i) * 1024);
        }
        __builtin_amdgcn_sched_barrier(0);
        q0 = MFMA32(xr[kb][0], lo8(wq[kb]), q0); q1 = MFMA32(xr[kb][1], lo8(wq[kb]), q1); k0 = MFMA32(xr[kb][0], lo8(wk[kb]), k0); k1 = MFMA32(xr[kb][1], lo8(wk[kb]), k1);
        q0 = MFMA32(xr[kb][2], hi8(wq[kb]), q0); q1 = MFMA32(xr[kb][3], hi8(wq[kb]), q1); k0 = MFMA32(xr[kb][2], hi8(wk[kb]), k0); k1 = MFMA32(xr[kb][3], hi8(wk[kb]), k1);
        __builtin_amdgcn_sched_barrier(0);
        if (PF && (kb & 1) == 0) glds16(psrc + (kb >> 1) * 1024, pdst + (kb >> 1) * 1024);
    }
    tw_store(qdst, q0, q1);
    tw_store(kdst, k0, k1);
}
template <bool PF>
__device__ __forceinline__ void proj_u(char* lds, const char* xb, const bf16x16 (&wvv)[10], const bf16x16 (&wo)[5][2], int wv, int lane, const char* ub, int h, const char* psrc, unsigned pdst, unsigned* cnt, unsigned cval) {
    const int g = lane >> 4, n = lane & 15;
    const int Lq = (n < 8) ? 4 * (n >> 1) + (n & 1) : 4 * ((n - 8) >> 1) + 2 + (n & 1);
    const unsigned twv_off = (wv >> 1) * 1024 + (2 * (wv & 1) + (Lq >> 3)) * 256 + (Lq & 7) * 2;
    const unsigned uo = (unsigned)(5 * wv * 32768 + h * 1024 + lane * 16);
    f32x4 v0 = {0.f, 0.f, 0.f, 0.f}, v1 = v0;
    bf16x8 xr[10][4];
#pragma unroll
    for (int kb = 0; kb < 2; ++kb)
#pragma unroll
        for (int i = 0; i < 4; ++i) xr[kb][i] = *(const bf16x8*)(xb + (kb * 4 + i) * 1024);
#pragma unroll
    for (int kb = 0; kb < 10; ++kb) {
        if (kb + 2 < 10) {
#pragma unroll
            for (int i = 0; i < 4; ++i) xr[kb + 2][i] = *(const bf16x8*)(xb + ((kb + 2) * 4 + i) * 1024);
        }
        __builtin_amdgcn_sched_barrier(0);
        v0 = MFMA32(xr[kb][0], lo8(wvv[kb]), v0); v1 = MFMA32(xr[kb][1], lo8(wvv[kb]), v1);
        v0 = MFMA32(xr[kb][2], hi8(wvv[kb]), v0); v1 = MFMA32(xr[kb][3], hi8(wvv[kb]), v1);
        __builtin_amdgcn_sched_barrier(0);
        if (PF && (kb & 1) == 0) glds16(psrc + (kb >> 1) * 1024, pdst + (kb >> 1) * 1024);
    }
    tw_store(lds + L_T + twv_off + g * 64, v0, v1);
    if (PF) asm volatile("s_waitcnt vmcnt(5)" ::: "memory");
    BARRIER();
    if (PF && wv == 0 && lane == 0) __hip_atomic_store(cnt, cval, __ATOMIC_RELAXED, __HIP_MEMORY_SCOPE_AGENT);
    bf16x8 vf[2][4];
#pragma unroll
    for (int tt = 0; tt < 2; ++tt)
#pragma unroll
        for (int t = 0; t < 4; ++t) vf[tt][t] = *(const bf16x8*)(lds + L_T + (tt * 4 + t) * 1024 + lane * 16);
#pragma unroll
    for (int ct = 0; ct < 5; ++ct) {
        f32x4 u0 = {0.f, 0.f, 0.f, 0.f}, u1 = u0;
#pragma unroll
        for (int t = 0; t < 2; ++t) {
            u0 = MFMA32(vf[0][2 * t], lo8(wo[ct][t]), u0); u1 = MFMA32(vf[1][2 * t], lo8(wo[ct][t]), u1);
            u0 = MFMA32(vf[0][2 * t + 1], hi8(wo[ct][t]), u0); u1 = MFMA32(vf[1][2 * t + 1], hi8(wo[ct][t]), u1);
        }
        u32x4 o = {pk_bf16(u0[0], u0[1]), pk_bf16(u0[2], u0[3]), pk_bf16(u1[0], u1[1]), pk_bf16(u1[2], u1[3])};
        st16_sc1(ub + ct * 32768, uo, o);
    }
}

__global__ void __launch_bounds__(NT, 2) k_main(MainArgs a) {
    extern __shared__ __attribute__((aligned(16))) char lds[];
    const int tid = threadIdx.x, lane = tid & 63;
    const int wv = __builtin_amdgcn_readfirstlane(tid >> 6);
    unsigned* ctrl = (unsigned*)(a.ws + OFF_CTRL);
#define SS(k) do {} while (0)
    int role;
    {
        unsigned* sm = (unsigned*)(lds + L_MISC);
        if (tid == 0) {
            const unsigned xc = xcc_id();
            int r = 0;
            for (unsigned k = 0; k < 8; ++k) {
                const int x = (int)((xc + k) & 7u);
                const int t = (int)__hip_atomic_fetch_add(ctrl + C_CNT + x * 32, 1u, __ATOMIC_RELAXED, __HIP_MEMORY_SCOPE_AGENT);
                if (t < 32) {
                    if (x < 4) r = t < 8 ? x * 8 + t : t < 16 ? R_M + x * 8 + t - 8 : t < 26 ? R_B + x * 10 + t - 16 : R_S + x * 6 + t - 26;
                    else r = t < 8 ? R_P + (x - 4) * 8 + t : t < 16 ? R_V + (x - 4) * 8 + t - 8 : R_O + (x - 4) * 16 + t - 16;
                    break;
                }
            }
            sm[0] = (unsigned)r;
        }
        __syncthreads();
        role = __builtin_amdgcn_readfirstlane((int)sm[0]);
        __syncthreads();
    }
    SS(1);
    const rsrc_t rs = __builtin_amdgcn_make_buffer_rsrc(a.ws, 0, (unsigned)(250 * MB), 0x00020000);
    if (role >= R_S) {
        const int sp = role - R_S;
        if (sp == 0) {
            if (tid == 0) {
                unsigned best[4] = {~0u, ~0u, ~0u, ~0u};
                for (int rep = 0; rep < 4; ++rep)
#pragma unroll
                    for (int t = 0; t < 4; ++t) {
                        unsigned* ad = (unsigned*)(a.ws + OFF_HO + (size_t)(t >> 1) * 2 * MB + 50 * 16384 + (t & 1) * 8192);
                        unsigned z = 0, r;
                        unsigned long long c0 = __builtin_amdgcn_s_memtime();
                        asm volatile("s_waitcnt lgkmcnt(0)\n\tglobal_atomic_add %0, %1, %2, off sc0\n\ts_waitcnt vmcnt(0)" : "=v"(r) : "v"(ad), "v"(z) : "memory");
                        unsigned long long c1 = __builtin_amdgcn_s_memtime();
                        unsigned d = (unsigned)(c1 - c0); best[t] = d < best[t] ? d : best[t];
                    }
                const int d0 = (int)(best[0] - best[1]), d1 = (int)(best[2] - best[3]);
                unsigned p0 = d0 > 0, p1 = d1 > 0;
                if (abs(d0) < 120 && abs(d1) >= 120) p0 = !p1;
                if (abs(d1) < 120 && abs(d0) >= 120) p1 = !p0;
                __hip_atomic_store(ctrl + C_POL, 0x100u | p0 | (p1 << 1), __ATOMIC_RELAXED, __HIP_MEMORY_SCOPE_AGENT);
            }
            __syncthreads();
        }
        for (int task = NS - 1 - sp; task < 32; task += NS) {
            const int p0 = task * 64;
            unsigned* tile = (unsigned*)lds;
            unsigned short xr[8][6], xi[8][6];
#pragma unroll
            for (int rr = 0; rr < 8; ++rr) {
                const int pos = p0 + wv + 8 * rr, m = (pos >> 8) + 8 * ((pos >> 5) & 7) + 64 * ((pos >> 2) & 7) + 512 * (pos & 3);
#pragma unroll
                for (int kk = 0; kk < 6; ++kk) { const int k = min(lane + 64 * kk, 320); xr[rr][kk] = a.wr[(size_t)m * 321 + k]; xi[rr][kk] = a.wi[(size_t)m * 321 + k]; }
            }
#pragma unroll
            for (int rr = 0; rr < 8; ++rr)
#pragma unroll
                for (int kk = 0; kk < 6; ++kk) { const int k = lane + 64 * kk; if (k < 321) tile[(wv + 8 * rr) * 323 + k] = (unsigned)xr[rr][kk] | ((unsigned)xi[rr][kk] << 16); }
            __syncthreads();
            for (int i = tid; i < 321 * 16; i += NT) {
                const int k = i >> 4, r = (i & 15) * 4;
                u32x4 v = {tile[r * 323 + k], tile[(r + 1) * 323 + k], tile[(r + 2) * 323 + k], tile[(r + 3) * 323 + k]};
                __builtin_amdgcn_raw_buffer_store_b128(v, rs, (unsigned)(OFF_WT + ((size_t)k * S_ + p0 + r) * 4), 0, 16);
            }
            __syncthreads();
        }
        arrive(ctrl, C_OB, tid);
        return;
    }
    auto get_pol = [&]() -> unsigned {
        if (wv == 0) {
            unsigned v, spins = 0;
            for (;;) {
                v = __builtin_amdgcn_readfirstlane(__hip_atomic_load(ctrl + C_POL, __ATOMIC_RELAXED, __HIP_MEMORY_SCOPE_AGENT));
                if (v & 0x100u) break;
                __builtin_amdgcn_s_sleep(2);
                if (spin_abort(spins, ctrl)) break;
            }
            wait_counters(ctrl, C_FILL, 1, NB, lane);
            if (lane == 0) *(volatile unsigned*)(lds + L_MISC + 32) = v;
        }
        __syncthreads();
        return __builtin_amdgcn_readfirstlane(*(volatile unsigned*)(lds + L_MISC + 32));
    };
    const char* xf = a.ws + OFF_XF;
    char* ho = a.ws + OFF_HO;
    const int g = lane >> 4, n = lane & 15;
    constexpr unsigned so_a = lmap(SLOT_SIGA), so_b = lmap(SLOT_SIGB);
    const int sidx = (lane & 8) ? 0xEEEE : 0x4444;
    const unsigned tw_off = (wv >> 1) * 1024 + (2 * (wv & 1) + (n >> 3)) * 256 + (n & 7) * 2;

    if (role < R_B) {
        const int h = (role & 31);
        const int gw = role < NA ? (wv < 2 ? wv : wv - 2) : wv;
        const int gn = role < NA ? ((wv == 2 || wv == 3) ? 0 : (gw < 4 ? 2 : 1)) : (wv < 2 ? 2 : 1);
        const int f0 = gw, f1 = role < NA ? gw + 6 : gw + 8;
        const unsigned mo0 = lmap(SLOT_MEM + f0 * 1024 + lane * 16), mo1 = lmap(SLOT_MEM + (f1 < 10 ? f1 : 0) * 1024 + lane * 16);
        unsigned pol = 0, target = 0;
        auto slotp = [&](int s) -> char* { return ho + (size_t)(s & 3) * MB + (((pol >> ((s >> 1) & 1)) & 1) ? 8192 : 0); };
        auto gather = [&](int s) {
            if (s > 0 && gn > 0) {
                const char* pslot = slotp(s - 1);
                ST2(0);
                if (s > 1) wait_rt(target);
                ST2(1);
                unsigned spins = 0;
                if (gn == 2) {
                    u32x4 v, v2;
                    for (;;) {
                        asm volatile("global_load_dwordx4 %0, %2, %4 sc1\n\tglobal_load_dwordx4 %1, %3, %4 sc1\n\ts_waitcnt vmcnt(0)"
                                     : "=&v"(v), "=&v"(v2) : "v"(mo0), "v"(mo1), "s"(pslot) : "memory");
                        *(u32x4*)(lds + L_MF + f0 * 1024 + lane * 16) = v;
                        *(u32x4*)(lds + L_MF + f1 * 1024 + lane * 16) = v2;
                        __builtin_amdgcn_sched_barrier(0);
                        if (__all(max(umax3(umax3(v.x, v.y, v.z), v.w, v2.x), umax3(v2.y, v2.z, v2.w)) != ~0u)) break;
                        if (spin_abort(spins, ctrl)) break;
                    }
                } else {
                    u32x4 v;
                    for (;;) {
                        asm volatile("global_load_dwordx4 %0, %1, %2 sc1\n\ts_waitcnt vmcnt(0)" : "=&v"(v) : "v"(mo0), "s"(pslot) : "memory");
                        *(u32x4*)(lds + L_MF + f0 * 1024 + lane * 16) = v;
                        __builtin_amdgcn_sched_barrier(0);
                        if (__all(max(umax3(v.x, v.y, v.z), v.w) != ~0u)) break;
                        if (spin_abort(spins, ctrl)) break;
                    }
                }
            }
            ST2(2);
            BARRIER();
            ST2(3);
        };
        auto next_target = [&](int s) {
            if (s > 0 && s < NSEG - 1) {
                unsigned sa, gb, spins = 0;
                for (;;) {
                    asm volatile("v_mov_b32 %0, 0\n\tglobal_load_dword %1, %0, %3 sc1\n\tglobal_load_dword %0, %0, %2 sc1\n\ts_waitcnt vmcnt(0)"
                                 : "=&v"(sa), "=&v"(gb) : "s"(slotp(s) + so_a), "s"(slotp(s - 1) + so_b) : "memory");
                    sa = __builtin_amdgcn_readfirstlane(sa); gb = __builtin_amdgcn_readfirstlane(gb);
                    if (sa != ~0u && gb != ~0u) break;
                    if (spin_abort(spins, ctrl)) break;
                }
                target = sa + DBC + gb + DAC;
            }
        };
        if (role < NA) {
            bf16x16 wq[10], wk[10];
            load_w2(a.Wq, a.Wk, h, lds, tid, wv, wq, wk);
            SS(2);
            if (wv == 0) wait_counters(ctrl, C_XF, 1, NO, lane);
            __syncthreads();
            prefetch_x(xf, 0, 0, wv, lane);
            load_mem0(a.mem0, lds, tid);
            pol = get_pol();
            asm volatile("s_waitcnt vmcnt(0)" ::: "memory");
            BARRIER();
            proj_qk<false>(lds + L_XB + lane * 16, wq, wk, lds + L_KX + 8192 + tw_off + g * 64, lds + L_KX + tw_off + g * 64, nullptr, 0);
            SS(3);
            auto front = [&](int s, unsigned& t_sig, unsigned ap_est, auto&& hook) {
                gather(s);
                f32x4 aq = {0.f, 0.f, 0.f, 0.f}, ak = aq;
                bf16x8 mf[10];
#pragma unroll
                for (int kb = 0; kb < 3; ++kb) mf[kb] = *(const bf16x8*)(lds + L_MF + kb * 1024 + lane * 16);
                if (role == 0 && wv == 1) {
                    t_sig = rt32();
                    if (lane == 0) st4_sc1(slotp(s) + so_a, 0u, nosent(t_sig + ap_est));
                }
                __builtin_amdgcn_sched_barrier(0);
#pragma unroll
                for (int kb = 0; kb < 10; ++kb) {
                    aq = SMFMAC(mf[kb], wq[kb], aq); ak = SMFMAC(mf[kb], wk[kb], ak);
                    if (kb + 3 < 10) mf[kb + 3] = *(const bf16x8*)(lds + L_MF + (kb + 3) * 1024 + lane * 16);
                    __builtin_amdgcn_sched_barrier(0);
                }
                hook();
                const u32x2 p = half_add(aq, ak);
                char* qd = lds + L_QF + (lane >> 5) * 4096 + tw_off + (g & 1) * 64;
                *(u16*)(qd + 0) = (u16)p.x; *(u16*)(qd + 16) = (u16)(p.x >> 16); *(u16*)(qd + 32) = (u16)p.y; *(u16*)(qd + 48) = (u16)(p.y >> 16);
            };
            if (wv == 0) {
                const int wg = h >> 2;
                const unsigned po_x = lmap(SLOT_P + wg * 2560 + ((h >> 1) & 1) * 1024 + (g * 16 + (h & 1) * 8 + (n & 7)) * 16 + (n >> 3) * 8);
                const unsigned po_m = lmap(SLOT_P + wg * 2560 + 2048 + (h & 3) * 128 + (n & 7) * 16 + (g & 1) * 8 + (n >> 3) * 4);
                const unsigned qoff = L_QF + (g * 16 + (n & 7)) * 16;
                const bool hi = n >= 8;
                unsigned dummy = 0;
                BARRIER();
                SS(4);
                for (int s = 0; s < NSEG; ++s) {
                    bf16x8 kx[8];
                    front(s, dummy, 0, [&]() {
#pragma unroll
                        for (int t = 0; t < 8; ++t) kx[t] = *(const bf16x8*)(lds + L_KX + (s & 1) * 16384 + t * 1024 + lane * 16);
                    });
                    ST2(4);
                    BARRIER();
                    ST2(5);
                    if (s < NSEG - 1) {
                        char* slot = slotp(s);
                        f32x4 s0 = {0.f, 0.f, 0.f, 0.f}, s1 = s0, s2 = s0;
                        bf16x8 qf[4], km[4];
#pragma unroll
                        for (int t = 0; t < 4; ++t) { qf[t] = *(const bf16x8*)(lds + qoff + t * 1024); km[t] = *(const bf16x8*)(lds + L_KM + t * 1024 + lane * 16); }
#pragma unroll
                        for (int t = 0; t < 4; ++t) { s1 = MFMA32(kx[t], qf[t], s1); s2 = MFMA32(kx[4 + t], qf[t], s2); s0 = MFMA32(km[t], qf[t], s0); }
                        __builtin_amdgcn_sched_group_barrier(0x100, 8, 0);
                        __builtin_amdgcn_sched_group_barrier(0x8, 12, 0);
                        const float cexp = 0.08838834764831845f * 1.4426950408889634f;
                        float v[6];
#pragma unroll
                        for (int r = 0; r < 4; ++r) v[r] = hi ? s2[r] : s1[r];
                        v[4] = hi ? s0[2] : s0[0]; v[5] = hi ? s0[3] : s0[1];
                        if (g >= 2) { v[4] = -INFINITY; v[5] = -INFINITY; }
                        float m, tmp_;
                        asm("v_max3_f32 %0, %2, %3, %4\n\tv_max3_f32 %1, %5, %6, %7\n\tv_max_f32 %0, %0, %1\n\ts_nop 1\n\t"
                            "v_max_f32_dpp %0, %0, %0 row_ror:8 row_mask:0xf bank_mask:0xf\n\tv_mov_b32 %1, %0\n\ts_nop 1\n\t"
                            "v_permlane16_swap_b32 %0, %1\n\tv_max_f32 %0, %0, %1\n\tv_mov_b32 %1, %0\n\ts_nop 1\n\t"
                            "v_permlane32_swap_b32 %0, %1\n\tv_max_f32 %0, %0, %1"
                            : "=&v"(m), "=&v"(tmp_) : "v"(v[0]), "v"(v[1]), "v"(v[2]), "v"(v[3]), "v"(v[4]), "v"(v[5]));
                        const float mc = -m * cexp;
#pragma unroll
                        for (int r = 0; r < 6; ++r) v[r] = __builtin_amdgcn_exp2f(__builtin_fmaf(v[r], cexp, mc));
                        float sum = ((v[0] + v[1]) + (v[2] + v[3])) + (v[4] + v[5]);
                        sum += u2f(__builtin_amdgcn_update_dpp(0, (int)f2u(sum), 0x128, 0xf, 0xf, false));
                        {
                            auto sw = __builtin_amdgcn_permlane16_swap(f2u(sum), f2u(sum), false, false);
                            sum = u2f(sw[0]) + u2f(sw[1]);
                            auto sw2 = __builtin_amdgcn_permlane32_swap(f2u(sum), f2u(sum), false, false);
                            sum = u2f(sw2[0]) + u2f(sw2[1]);
                        }
                        const float inv = __builtin_amdgcn_rcpf(sum);
                        u32x2 px = {nosent(pk_bf16(v[0] * inv, v[1] * inv)), nosent(pk_bf16(v[2] * inv, v[3] * inv))};
                        const unsigned pm = nosent(pk_bf16(v[4] * inv, v[5] * inv));
                        st8_sc1(slot, po_x, px);
                        if (g < 2) st4_sc1(slot, po_m, pm);
                        ST2(7);
                        if (role == 0) { const unsigned t = rt32(); if (lane == 0) *(volatile unsigned*)(lds + L_MISC + 48) = t; LDSWAIT(); }
                    }
                    ST2(6);
                    asm volatile("s_barrier" ::: "memory");
                    ST2F();
                    if (s >= 2) {
                        st8_sc1(slotp(s + 2), po_x, u32x2{~0u, ~0u});
                        if (g < 2) st4_sc1(slotp(s + 2), po_m, ~0u);
                    }
                    target = __builtin_amdgcn_readfirstlane(*(volatile unsigned*)(lds + L_MISC + 64 + (s & 1) * 4));
                }
            } else if (wv == 2 || wv == 3) {
                const int qt = wv - 2;
                unsigned dummy = 0;
                BARRIER();
                const unsigned pbx = (2 * qt + (n >> 3)) * 20480 + (h >> 2) * 2560 + ((h >> 1) & 1) * 1024 + (g * 16 + (h & 1) * 8 + (n & 7)) * 16;
                const unsigned pbm = (2 * qt + (n >> 3)) * 20480 + (h >> 2) * 2560 + 2048 + (h & 3) * 128 + (n & 7) * 16 + (g & 1) * 8;
                for (int s = 0; s < NSEG; ++s) {
                    front(s, dummy, 0, []() {});
                    asm volatile("s_waitcnt vmcnt(0)" ::: "memory");
                    BARRIER();
                    __builtin_amdgcn_s_sleep(ATSLEEP);
                    const char* kq = lds + L_KX + (s & 1) * 16384 + lane * 16;
                    f32x4 s0 = {0.f, 0.f, 0.f, 0.f}, s1 = s0, s2 = s0;
#pragma unroll
                    for (int t = 0; t < 4; ++t) {
                        const bf16x8 km = *(const bf16x8*)(lds + L_KM + t * 1024 + lane * 16), qf = *(const bf16x8*)(kq + 8192 + (qt * 4 + t) * 1024);
                        const bf16x8 k1 = *(const bf16x8*)(kq + t * 1024), k2 = *(const bf16x8*)(kq + (4 + t) * 1024);
                        s1 = MFMA32(k1, qf, s1); s2 = MFMA32(k2, qf, s2); s0 = MFMA32(km, qf, s0);
                    }
                    const float cexp = 0.08838834764831845f * 1.4426950408889634f, masked = -999292928.f / 0.08838834764831845f;
                    const int qi = 16 * qt + n;
                    float v[12];
#pragma unroll
                    for (int r = 0; r < 4; ++r) {
                        v[r] = (g < 2) ? s0[r] : -INFINITY;
                        v[4 + r] = (4 * g + r <= qi) ? s1[r] : masked;
                        v[8 + r] = (16 + 4 * g + r <= qi) ? s2[r] : masked;
                    }
                    float m, tmp_;
                    asm("v_max3_f32 %0, %2, %3, %4\n\tv_max3_f32 %1, %5, %6, %7\n\tv_max3_f32 %0, %0, %8, %9\n\tv_max3_f32 %1, %1, %10, %11\n\tv_max3_f32 %0, %0, %12, %13\n\tv_max_f32 %0, %0, %1\n\t"
                        "v_mov_b32 %1, %0\n\ts_nop 1\n\tv_permlane16_swap_b32 %0, %1\n\tv_max_f32 %0, %0, %1\n\tv_mov_b32 %1, %0\n\ts_nop 1\n\t"
                        "v_permlane32_swap_b32 %0, %1\n\tv_max_f32 %0, %0, %1"
                        : "=&v"(m), "=&v"(tmp_) : "v"(v[0]), "v"(v[1]), "v"(v[2]), "v"(v[3]), "v"(v[4]), "v"(v[5]), "v"(v[6]), "v"(v[7]), "v"(v[8]), "v"(v[9]), "v"(v[10]), "v"(v[11]));
                    const float mc = -m * cexp;
#pragma unroll
                    for (int r = 0; r < 12; ++r) v[r] = __builtin_amdgcn_exp2f(__builtin_fmaf(v[r], cexp, mc));
                    float sum = ((v[0] + v[1]) + (v[2] + v[3])) + ((v[4] + v[5]) + (v[6] + v[7])) + ((v[8] + v[9]) + (v[10] + v[11]));
                    {
                        auto sw = __builtin_amdgcn_permlane16_swap(f2u(sum), f2u(sum), false, false);
                        sum = u2f(sw[0]) + u2f(sw[1]);
                        auto sw2 = __builtin_amdgcn_permlane32_swap(f2u(sum), f2u(sum), false, false);
                        sum = u2f(sw2[0]) + u2f(sw2[1]);
                    }
                    const float inv = __builtin_amdgcn_rcpf(sum);
                    u32x4 px = {pk_bf16(v[4] * inv, v[5] * inv), pk_bf16(v[6] * inv, v[7] * inv), pk_bf16(v[8] * inv, v[9] * inv), pk_bf16(v[10] * inv, v[11] * inv)};
                    u32x2 pm = {pk_bf16(v[0] * inv, v[1] * inv), pk_bf16(v[2] * inv, v[3] * inv)};
                    if (s < NSEG - 1) {
                        const char* pxb = a.ws + OFF_PXF + (size_t)s * 81920;
                        st16_sc1(pxb, pbx, px);
                        if (g < 2) st8_sc1(pxb, pbm, pm);
                    } else {
                        st16_sc1(slotp(0), lmap(SLOT_PXL + pbx), u32x4{nosent(px.x), nosent(px.y), nosent(px.z), nosent(px.w)});
                        if (g < 2) st8_sc1(slotp(0), lmap(SLOT_PXL + pbm), u32x2{nosent(pm.x), nosent(pm.y)});
                    }
                    asm volatile("s_barrier" ::: "memory");
                }
                asm volatile("s_waitcnt vmcnt(0)" ::: "memory");
            } else {
                unsigned ap_est = 70, t_sig = 0, pp = 0;
                auto load_qk = [&](int s) {
                    if (wv >= 4) {
                        __builtin_amdgcn_s_sleep(2);
                        if (pp <= (unsigned)s) pp = wait_counters(ctrl, C_PP + h * 32, 1, s + 1, lane);
                        const char* src = a.ws + (wv < 6 ? OFF_KXF : OFF_QXF) + ((size_t)s * H_ + h) * 8192 + (wv & 1) * 4096 + lane * 16;
#pragma unroll
                        for (int i = 0; i < 4; ++i) glds16(src + i * 1024, L_KX + (s & 1) * 16384 + (wv - 4) * 4096 + i * 1024);
                    }
                };
                BARRIER();
                for (int s = 0; s < NSEG; ++s) {
                    front(s, t_sig, ap_est, []() {});
                    BARRIER();
                    if (wv == 1) { next_target(s); if (lane == 0) *(volatile unsigned*)(lds + L_MISC + 64 + (s & 1) * 4) = target; }
                    if (s + 1 < NSEG) load_qk(s + 1);
                    BARRIER();
                    if (role == 0 && wv == 1) {
                        ap_est = __builtin_amdgcn_readfirstlane(*(volatile unsigned*)(lds + L_MISC + 48)) - t_sig;
                        if (s >= 2 && lane == 0) st4_sc1(slotp(s + 2) + so_a, 0u, ~0u);
                    }
                    if (wv == 4 && lane == 0 && s > 0) set_counter(ctrl, C_AP + h * 32, s);
                    target = __builtin_amdgcn_readfirstlane(*(volatile unsigned*)(lds + L_MISC + 64 + (s & 1) * 4));
                }
            }
            __syncthreads();
            if (tid == 0) set_counter(ctrl, C_AP + h * 32, NSEG);
        } else {
            bf16x16 wvv[10], wo[5][2];
            load_wvo(a.Wv, a.Wo, h, lds, tid, wv, wvv, wo);
            SS(2);
            if (wv == 0) wait_counters(ctrl, C_XF, 1, NO, lane);
            __syncthreads();
            prefetch_x(xf, 0, 0, wv, lane);
            load_mem0(a.mem0, lds, tid);
            pol = get_pol();
            asm volatile("s_waitcnt vmcnt(0)" ::: "memory");
            BARRIER();
            proj_u<false>(lds, lds + L_XB + lane * 16, wvv, wo, wv, lane, a.ws + OFF_UF, h, nullptr, 0, nullptr, 0);
            asm volatile("s_waitcnt vmcnt(0)" ::: "memory");
            __syncthreads();
            if (tid == 0) set_counter(ctrl, C_V0 + h * 32, 1u);
            SS(3);
            const unsigned vm_off = L_QF + (wv >> 2) * 1024 + ((wv & 3) * 16 + (n >> 3) * 8 + 4 * (g & 1)) * 16 + (n & 7) * 2;
            const unsigned zo4 = (4 + 5 * wv + (g >> 1) + 2 * (g & 1)) * 16384 + h * 256 + n * 16;
            const unsigned zo1 = (4 + 5 * wv + 4) * 16384 + h * 256 + n * 16 + (g & 1) * 8;
            for (int s = 0; s < NSEG; ++s) {
                gather(s);
                f32x4 av = {0.f, 0.f, 0.f, 0.f};
                bf16x8 mf[10];
#pragma unroll
                for (int kb = 0; kb < 4; ++kb) mf[kb] = *(const bf16x8*)(lds + L_MF + kb * 1024 + lane * 16);
                __builtin_amdgcn_sched_barrier(0);
#pragma unroll
                for (int kb = 0; kb < 10; ++kb) {
                    av = SMFMAC(mf[kb], wvv[kb], av);
                    if (kb + 4 < 10) mf[kb + 4] = *(const bf16x8*)(lds + L_MF + (kb + 4) * 1024 + lane * 16);
                    __builtin_amdgcn_sched_barrier(0);
                }
                const u32x2 p = half_add(av, av);
                char* vd = lds + vm_off;
                *(u16*)(vd + 0) = (u16)p.x; *(u16*)(vd + 16) = (u16)(p.x >> 16); *(u16*)(vd + 32) = (u16)p.y; *(u16*)(vd + 48) = (u16)(p.y >> 16);
                ST2(4);
                BARRIER();
                ST2(5);
                const bf16x8 vf0 = *(const bf16x8*)(lds + L_QF + lane * 16), vf1 = *(const bf16x8*)(lds + L_QF + 1024 + lane * 16);
                f32x4 z[5];
#pragma unroll
                for (int ct = 0; ct < 5; ++ct) { z[ct] = f32x4{0.f, 0.f, 0.f, 0.f}; z[ct] = SMFMAC(vf0, wo[ct][0], z[ct]); }
#pragma unroll
                for (int ct = 0; ct < 5; ++ct) z[ct] = SMFMAC(vf1, wo[ct][1], z[ct]);
                {
                    char* slot = slotp(s);
                    const u32x2 z01 = half_add(z[0], z[1]), z23 = half_add(z[2], z[3]);
                    auto sx = __builtin_amdgcn_permlane16_swap(z01.x, z23.x, false, false);
                    auto sy = __builtin_amdgcn_permlane16_swap(z01.y, z23.y, false, false);
                    st16_sc1(slot, zo4, u32x4{nosent(sx[0]), nosent(sy[0]), nosent(sx[1]), nosent(sy[1])});
                    const u32x2 z4 = half_add(z[4], z[4]);
                    if (lane < 32) st8_sc1(slot, zo1, u32x2{nosent(z4.x), nosent(z4.y)});
                }
                ST2(6);
                ST2F();
                next_target(s);
            }
        }
    } else if (role < NREC) {
        const int ct = role - R_B;
        {
            const u32x4 sv = {~0u, ~0u, ~0u, ~0u};
            for (int q = ct; q < 186; q += NB) {
                const unsigned base = (unsigned)OFF_HO + (q < 176 ? (q / 44) * (unsigned)MB + (q % 44) * 16384 : (52 + q - 176) * 16384) + tid * 16;
                __builtin_amdgcn_raw_buffer_store_b128(sv, rs, base, 0, 16);
                __builtin_amdgcn_raw_buffer_store_b128(sv, rs, base, 8192, 16);
            }
            arrive(ctrl, C_FILL, tid);
        }
        SS(2);
        if (tid == 0) *(volatile unsigned*)(lds + L_MISC + 68) = 0u;
        const unsigned pol = get_pol();
        SS(3);
        auto slotp = [&](int s) -> char* { return ho + (size_t)(s & 3) * MB + (((pol >> ((s >> 1) & 1)) & 1) ? 8192 : 0); };
        if (wv < 2) {
            unsigned pox[8], pom[2];
#pragma unroll
            for (int i = 0; i < 8; ++i) pox[i] = lmap(SLOT_P + (4 * wv + (i >> 1)) * 2560 + (i & 1) * 1024 + lane * 16);
#pragma unroll
            for (int i = 0; i < 2; ++i) pom[i] = lmap(SLOT_P + (4 * wv + 2 * i + (n >> 3)) * 2560 + 2048 + g * 128 + (n & 7) * 16);
            const unsigned zo = (4 + ct) * 16384 + wv * 4096 + lane * 16;
            const unsigned mo_pub = lmap(SLOT_MEM + (2 * ct + (g >> 1)) * 128 + (n & 7) * 16 + (g & 1) * 8);
            const bool hi = n >= 8;
            unsigned vp = 0;
            u32x4 uf[16];
            auto load_u = [&](int s) {
                if (s == 0) wait_counters(ctrl, C_V0 + 16 * wv * 32, 16, 1, lane);
                else if (vp <= (unsigned)s) vp = wait_counters(ctrl, C_VP + 16 * wv * 32, 16, s + 1, lane);
                const unsigned src = (unsigned)(OFF_UF + ((size_t)s * NB + ct) * 32768) + wv * 16384 + lane * 16;
#pragma unroll
                for (int q = 0; q < 16; ++q) uf[q] = __builtin_amdgcn_raw_buffer_load_b128(rs, src, q * 1024, 16);
#pragma unroll
                for (int q = 0; q < 16; ++q) asm volatile("" : "+v"(uf[q]));
            };
            load_u(0);
            SS(4);
            unsigned gbmin = 85;
            for (int s = 0; s < NSEG; ++s) {
                char* slot = slotp(s);
                u32x4 px[8], pm[2], zz[4];
                unsigned spins = 0, target = 0;
#define B_LOADZ() asm volatile("global_load_dwordx4 %0, %4, %5 sc1\n\tglobal_load_dwordx4 %1, %4, %5 offset:1024 sc1\n\tglobal_load_dwordx4 %2, %4, %5 offset:2048 sc1\n\tglobal_load_dwordx4 %3, %4, %5 offset:3072 sc1" \
                             : "=&v"(zz[0]), "=&v"(zz[1]), "=&v"(zz[2]), "=&v"(zz[3]) : "v"(zo), "s"(slot) : "memory")
#define B_LOADP() asm volatile("global_load_dwordx4 %8, %18, %20 sc1\n\tglobal_load_dwordx4 %9, %19, %20 sc1\n\t" \
                             "global_load_dwordx4 %0, %10, %20 sc1\n\tglobal_load_dwordx4 %1, %11, %20 sc1\n\tglobal_load_dwordx4 %2, %12, %20 sc1\n\tglobal_load_dwordx4 %3, %13, %20 sc1\n\t" \
                             "global_load_dwordx4 %4, %14, %20 sc1\n\tglobal_load_dwordx4 %5, %15, %20 sc1\n\tglobal_load_dwordx4 %6, %16, %20 sc1\n\tglobal_load_dwordx4 %7, %17, %20 sc1" \
                             : "=&v"(px[0]), "=&v"(px[1]), "=&v"(px[2]), "=&v"(px[3]), "=&v"(px[4]), "=&v"(px[5]), "=&v"(px[6]), "=&v"(px[7]), "=&v"(pm[0]), "=&v"(pm[1]) \
                             : "v"(pox[0]), "v"(pox[1]), "v"(pox[2]), "v"(pox[3]), "v"(pox[4]), "v"(pox[5]), "v"(pox[6]), "v"(pox[7]), "v"(pom[0]), "v"(pom[1]), "s"(slot) : "memory")
                if (s < NSEG - 1) {
                    {
                        unsigned spt = 0;
                        while (__builtin_amdgcn_readfirstlane(*(volatile unsigned*)(lds + L_MISC + 68)) != (unsigned)(s + 1)) { __builtin_amdgcn_s_sleep(1); if (++spt > (SPIN_CAP << 4)) break; }
                        target = __builtin_amdgcn_readfirstlane(*(volatile unsigned*)(lds + L_MISC + 64));
                    }
                    ST2(0);
                    wait_rt(target - ZLEAD);
                    B_LOADZ();
                    wait_rt(target);
                    ST2(1);
                    B_LOADP();
                    const f32x4 zero = {0.f, 0.f, 0.f, 0.f};
                    f32x4 xa, xb;
                    unsigned mx;
#define B_MXZ() umax3(umax3(umax3(zz[0].x, zz[0].y, zz[0].z), umax3(zz[0].w, zz[1].x, zz[1].y), umax3(zz[1].z, zz[1].w, zz[2].x)), \
                      umax3(umax3(zz[2].y, zz[2].z, zz[2].w), umax3(zz[3].x, zz[3].y, zz[3].z), umax3(zz[3].w, pm[0].x, pm[0].y)), \
                      umax3(umax3(pm[0].z, pm[0].w, pm[1].x), pm[1].y, max(pm[1].z, pm[1].w)))
#define B_MM() xa = MFMA32(BC8(zz[0]), BC8(pm[0]), zero); xb = MFMA32(BC8(zz[1]), BC8(pm[0]), zero); \
               xa = MFMA32(BC8(zz[2]), BC8(pm[1]), xa); xb = MFMA32(BC8(zz[3]), BC8(pm[1]), xb);
                    asm volatile("s_waitcnt vmcnt(8)" : "+v"(zz[0]), "+v"(zz[1]), "+v"(zz[2]), "+v"(zz[3]), "+v"(pm[0]), "+v"(pm[1]) :: "memory");
                    B_MM();
                    mx = B_MXZ();
                    __builtin_amdgcn_sched_barrier(0);
#define B_STEP(i, cnt) asm volatile("s_waitcnt vmcnt(" #cnt ")" : "+v"(px[i]) :: "memory"); \
                    xa = MFMA32(BC8(uf[2 * i]), BC8(px[i]), xa); xb = MFMA32(BC8(uf[2 * i + 1]), BC8(px[i]), xb); \
                    mx = umax3(mx, umax3(px[i].x, px[i].y, px[i].z), px[i].w); __builtin_amdgcn_sched_barrier(0);
                    B_STEP(0, 7) B_STEP(1, 6) B_STEP(2, 5) B_STEP(3, 4) B_STEP(4, 3) B_STEP(5, 2) B_STEP(6, 1) B_STEP(7, 0)
                    if (!__all(mx != ~0u)) {
                        const unsigned sb = (unsigned)(slot - a.ws);
                        do {
                            if (spin_abort(spins, ctrl)) break;
                            asm volatile("" ::: "memory");
#pragma unroll
                            for (int q = 0; q < 4; ++q) zz[q] = __builtin_amdgcn_raw_buffer_load_b128(rs, zo + q * 1024, sb, 16);
#pragma unroll
                            for (int q = 0; q < 2; ++q) pm[q] = __builtin_amdgcn_raw_buffer_load_b128(rs, pom[q], sb, 16);
#pragma unroll
                            for (int q = 0; q < 8; ++q) px[q] = __builtin_amdgcn_raw_buffer_load_b128(rs, pox[q], sb, 16);
                            mx = B_MXZ();
#pragma unroll
                            for (int q = 0; q < 8; ++q) mx = umax3(mx, umax3(px[q].x, px[q].y, px[q].z), px[q].w);
                        } while (!__all(mx != ~0u));
                        B_MM();
#pragma unroll
                        for (int q = 0; q < 8; ++q) { xa = MFMA32(BC8(uf[2 * q]), BC8(px[q]), xa); xb = MFMA32(BC8(uf[2 * q + 1]), BC8(px[q]), xb); }
                    }
                    ST2(2);
                    f32x4 tot;
#pragma unroll
                    for (int r = 0; r < 4; ++r) {
                        const float y = hi ? xb[r] : xa[r];
                        tot[r] = y + u2f(__builtin_amdgcn_update_dpp(0, (int)f2u(y), 0x128, 0xf, 0xf, false));
                    }
                    if (wv == 1) *(f32x4*)(lds + L_RED + lane * 16) = tot;
                    BARRIER();
                    ST2(3);
                    if (wv == 0) {
                        tot += *(const f32x4*)(lds + L_RED + lane * 16);
                        const u32x2 o = {nosent(pk_bf16(tot[0], tot[1])), nosent(pk_bf16(tot[2], tot[3]))};
                        if (!hi) st8_sc1(slot, mo_pub, o);
                        ST2(7);
                        if (ct == 0) {
                            const unsigned gb = rt32() - target;
                            gbmin = gb < gbmin ? gb : gbmin;
                            if (lane == 0) { st4_sc1(slot + so_b, 0u, gbmin); if (s >= 2) st4_sc1(slotp(s + 2) + so_b, 0u, ~0u); }
                        }
                        if (s >= 2 && !hi) st8_sc1(slotp(s + 2), mo_pub, u32x2{~0u, ~0u});
                    }
                    ST2(4);
                    asm volatile("s_barrier" ::: "memory");
                    ST2F();
                    if (wv == 1 && lane == 0 && s > 0) set_counter(ctrl, C_ZP + ct * 32, s);
                } else {
                    for (;;) {
                        asm volatile("" ::: "memory");
#pragma unroll
                        for (int q = 0; q < 4; ++q) zz[q] = __builtin_amdgcn_raw_buffer_load_b128(rs, zo + q * 1024, (unsigned)(slot - a.ws), 16);
                        const unsigned mx = umax3(umax3(umax3(zz[0].x, zz[0].y, zz[0].z), umax3(zz[0].w, zz[1].x, zz[1].y), umax3(zz[1].z, zz[1].w, zz[2].x)),
                                                  umax3(umax3(zz[2].y, zz[2].z, zz[2].w), umax3(zz[3].x, zz[3].y, zz[3].z), zz[3].w), 0u);
                        if (__all(mx != ~0u)) break;
                        __builtin_amdgcn_s_sleep(2);
                        if (spin_abort(spins, ctrl)) break;
                    }
                }
                if (s < NSEG - 1) {
                    const char* zc = a.ws + OFF_ZF + ((size_t)s * NB + ct) * 8192 + wv * 4096;
#pragma unroll
                    for (int q = 0; q < 4; ++q) st16_sc1(zc + q * 1024, lane * 16, zz[q]);
                } else {
#pragma unroll
                    for (int q = 0; q < 4; ++q) *(u32x4*)(lds + L_RED + (wv * 4 + q) * 1024 + lane * 16) = zz[q];
                }
                if (s < NSEG - 2) {
                    asm volatile("s_barrier" ::: "memory");
#pragma unroll
                    for (int q = 0; q < 16; ++q) uf[q] = *(const u32x4*)(lds + L_UB + (s & 1) * 32768 + (wv * 16 + q) * 1024 + lane * 16);
#pragma unroll
                    for (int q = 0; q < 16; ++q) asm volatile("" : "+v"(uf[q]));
                } else if (s == NSEG - 2) {
                    asm volatile("s_waitcnt vmcnt(0)\n\ts_barrier" ::: "memory");
                    if (wv == 1 && lane == 0) set_counter(ctrl, C_ZP + ct * 32, NSEG - 1);
                }
            }
            asm volatile("s_waitcnt vmcnt(0)" ::: "memory");
        } else {
            unsigned vp = 0;
            for (int s = 0; s < NSEG - 1; ++s) {
                if (wv == 2) {
                    const unsigned t = read_sig(slotp(s) + so_a, ctrl) + DBC;
                    if (lane == 0) { *(volatile unsigned*)(lds + L_MISC + 64) = t; LDSWAIT(); *(volatile unsigned*)(lds + L_MISC + 68) = (unsigned)(s + 1); }
                }
                const int sn = s + 1;
                const char* usrc = a.ws + OFF_UF + ((size_t)sn * NB + ct) * 32768 + (wv - 4) * 8192 + lane * 16;
                bool early = false;
                if (wv >= 4) {
                    if (vp <= (unsigned)sn) vp = wait_counters(ctrl, C_VP + 8 * (wv - 4) * 32, 8, 0, lane);
                    early = vp > (unsigned)sn;
                    if (early) {
                        unsigned spt = 0;
                        while (__builtin_amdgcn_readfirstlane(*(volatile unsigned*)(lds + L_MISC + 68)) != (unsigned)(s + 1)) { __builtin_amdgcn_s_sleep(1); if (++spt > (SPIN_CAP << 4)) break; }
                        wait_rt(__builtin_amdgcn_readfirstlane(*(volatile unsigned*)(lds + L_MISC + 64)) + 8);
#pragma unroll
                        for (int q = 0; q < 8; ++q) glds16(usrc + q * 1024, L_UB + (s & 1) * 32768 + (wv - 4) * 8192 + q * 1024);
                    }
                }
                asm volatile("s_waitcnt lgkmcnt(0)\n\ts_barrier" ::: "memory");
                asm volatile("s_barrier" ::: "memory");
                if (wv >= 4) {
                    const unsigned o = (4 + ct) * 16384 + (wv - 4) * 2048 + lane * 16;
                    st16_sc1(slotp(s), o, u32x4{~0u, ~0u, ~0u, ~0u}); st16_sc1(slotp(s) + 1024, o, u32x4{~0u, ~0u, ~0u, ~0u});
                    if (!early) {
                        vp = wait_counters(ctrl, C_VP + 8 * (wv - 4) * 32, 8, sn + 1, lane);
#pragma unroll
                        for (int q = 0; q < 8; ++q) glds16(usrc + q * 1024, L_UB + (s & 1) * 32768 + (wv - 4) * 8192 + q * 1024);
                    }
                }
                asm volatile("s_waitcnt vmcnt(0)\n\ts_barrier" ::: "memory");
            }
        }
        __syncthreads();
        if (tid == 0) set_counter(ctrl, C_ZP + ct * 32, NSEG);
        if (wv < 4) {
            const int row = 32 * (NSEG - 1) + 8 * wv + (n & 7);
            wait_counters(ctrl, C_OB + 2 * 32, 1, NO, lane);
            const u32x2 av = __builtin_amdgcn_raw_buffer_load_b64(rs, (unsigned)(OFF_A + ((size_t)row * D_ + 16 * ct + 4 * g) * 2), 0, 16);
            const float gv = u2f(__builtin_amdgcn_raw_buffer_load_b32(rs, (unsigned)(OFF_G + row * 4), 0, 16));
            const unsigned sb = (unsigned)(slotp(0) - a.ws);
            u32x4 px[16], pm[4];
            unsigned spins = 0;
            for (;;) {
                asm volatile("" ::: "memory");
#pragma unroll
                for (int q = 0; q < 16; ++q) px[q] = __builtin_amdgcn_raw_buffer_load_b128(rs, lmap(SLOT_PXL + wv * 20480 + (q >> 1) * 2560 + (q & 1) * 1024 + lane * 16), sb, 16);
#pragma unroll
                for (int q = 0; q < 4; ++q) pm[q] = __builtin_amdgcn_raw_buffer_load_b128(rs, lmap(SLOT_PXL + wv * 20480 + (2 * q + (n >> 3)) * 2560 + 2048 + g * 128 + (n & 7) * 16), sb, 16);
                unsigned mx = 0;
#pragma unroll
                for (int q = 0; q < 16; ++q) mx = umax3(mx, umax3(px[q].x, px[q].y, px[q].z), px[q].w);
#pragma unroll
                for (int q = 0; q < 4; ++q) mx = umax3(mx, umax3(pm[q].x, pm[q].y, pm[q].z), pm[q].w);
                if (__all(mx != ~0u)) break;
                __builtin_amdgcn_s_sleep(4);
                if (spin_abort(spins, ctrl)) break;
            }
            const char* ub = lds + L_UB + ((NSEG - 2) & 1) * 32768 + lane * 16;
            f32x4 xa = {0.f, 0.f, 0.f, 0.f}, xb = xa;
#pragma unroll
            for (int q = 0; q < 16; ++q) {
                xa = MFMA32(*(const bf16x8*)(ub + (2 * q) * 1024), BC8(px[q]), xa);
                xb = MFMA32(*(const bf16x8*)(ub + (2 * q + 1) * 1024), BC8(px[q]), xb);
            }
#pragma unroll
            for (int q = 0; q < 4; ++q) {
                xa = MFMA32(*(const bf16x8*)(lds + L_RED + (2 * q) * 1024 + lane * 16), BC8(pm[q]), xa);
                xb = MFMA32(*(const bf16x8*)(lds + L_RED + (2 * q + 1) * 1024 + lane * 16), BC8(pm[q]), xb);
            }
            float tot[4];
#pragma unroll
            for (int r = 0; r < 4; ++r) {
                const float y = n >= 8 ? xb[r] : xa[r];
                tot[r] = y + u2f(__builtin_amdgcn_update_dpp(0, (int)f2u(y), 0x128, 0xf, 0xf, false));
            }
            const float og = rbf(1.f - gv);
            auto mix = [&](unsigned aw, float x0, float x1) -> unsigned {
                return pk_bf16(rbf(gv * u2f(aw << 16)) + rbf(og * rbf(x0)), rbf(gv * u2f(aw & 0xFFFF0000u)) + rbf(og * rbf(x1)));
            };
            if (n < 8) *(u32x2*)(a.out + (size_t)row * D_ + 16 * ct + 4 * g) = u32x2{mix(av.x, tot[0], tot[1]), mix(av.y, tot[2], tot[3])};
        }
    } else if (role < R_V) {
        const int h = role - R_P;
        bf16x16 wq[10], wk[10];
        load_w2(a.Wq, a.Wk, h, lds, tid, wv, wq, wk);
        SS(2);
        if (wv == 0) wait_counters(ctrl, C_XF, 1, NO, lane);
        __syncthreads();
        prefetch_x(xf, 1, 0, wv, lane);
        prefetch_x(xf, 2, 1, wv, lane);
        int b3 = 0;
        for (int s = 1; s < NSEG; ++s) {
            if (s < 4) asm volatile("s_waitcnt vmcnt(0)" ::: "memory");
            BARRIER();
            if (s > 1 && s < 4 && tid == 0) set_counter(ctrl, C_PP + h * 32, s);
            if (s < 5) SS(2 + s);
            proj_qk<true>(lds + L_XB + b3 * 40960 + lane * 16, wq, wk, lds + L_T + tw_off + g * 64, lds + L_T + 8192 + tw_off + g * 64,
                          xf + (size_t)min(s + 2, NSEG - 1) * 40960 + wv * 5120 + lane * 16, L_XB + (b3 == 0 ? 2 : b3 - 1) * 40960 + wv * 5120);
            b3 = b3 == 2 ? 0 : b3 + 1;
            asm volatile("s_waitcnt vmcnt(5)" ::: "memory");
            BARRIER();
            if (tid == 0) set_counter(ctrl, C_PP + h * 32, s);
            const u32x4 vq = *(const u32x4*)(lds + L_T + tid * 16), vk = *(const u32x4*)(lds + L_T + 8192 + tid * 16);
            st16_sc1(a.ws + OFF_KXF + ((size_t)s * H_ + h) * 8192, tid * 16, vk);
            st16_sc1(a.ws + OFF_QXF + ((size_t)s * H_ + h) * 8192, tid * 16, vq);
        }
        asm volatile("s_waitcnt vmcnt(0)" ::: "memory");
        __syncthreads();
        if (tid == 0) set_counter(ctrl, C_PP + h * 32, NSEG);
    } else if (role < R_O) {
        const int h = role - R_V;
        bf16x16 wvv[10], wo[5][2];
        load_wvo(a.Wv, a.Wo, h, lds, tid, wv, wvv, wo);
        SS(2);
        if (wv == 0) wait_counters(ctrl, C_XF, 1, NO, lane);
        __syncthreads();
        prefetch_x(xf, 1, 0, wv, lane);
        prefetch_x(xf, 2, 1, wv, lane);
        int b3 = 0;
        for (int s = 1; s < NSEG; ++s) {
            if (s < 4) asm volatile("s_waitcnt vmcnt(0)" ::: "memory");
            BARRIER();
            if (s > 1 && s < 4 && tid == 0) set_counter(ctrl, C_VP + h * 32, s);
            if (s < 5) SS(2 + s);
            proj_u<true>(lds, lds + L_XB + b3 * 40960 + lane * 16, wvv, wo, wv, lane, a.ws + OFF_UF + (size_t)s * NB * 32768, h,
                         xf + (size_t)min(s + 2, NSEG - 1) * 40960 + wv * 5120 + lane * 16, L_XB + (b3 == 0 ? 2 : b3 - 1) * 40960 + wv * 5120, ctrl + C_VP + h * 32, s);
            b3 = b3 == 2 ? 0 : b3 + 1;
        }
        asm volatile("s_waitcnt vmcnt(0)" ::: "memory");
        __syncthreads();
        if (tid == 0) set_counter(ctrl, C_VP + h * 32, NSEG);
    } else {
        const int o = role - R_O;
        {
#pragma unroll
            for (int i = 0; i < 5; ++i) {
                const int id = i * NT + tid, l = id & 63, tt = (id >> 6) & 1, hf = (id >> 7) & 1, kb = id >> 8;
                const int ga = l >> 4, tok = 32 * o + 16 * tt + (l & 15), dbase = 64 * kb + 16 * (2 * hf + (ga >> 1)) + 4 * (ga & 1);
                const u32x2 c0 = *(const u32x2*)(a.x + (size_t)tok * D_ + dbase), c1 = *(const u32x2*)(a.x + (size_t)tok * D_ + dbase + 8);
                u32x4 ov = {c0.x, c1.x, c0.y, c1.y};
                __builtin_amdgcn_raw_buffer_store_b128(ov, rs, (unsigned)OFF_XF + o * 40960 + id * 16, 0, 16);
            }
            arrive(ctrl, C_XF, tid);
        }
        SS(2);
        auto obar = [&](int idx) {
            asm volatile("s_waitcnt vmcnt(0)" ::: "memory");
            __syncthreads();
            if (wv == 0) {
                if (lane == 0) __hip_atomic_fetch_add(ctrl + C_OB + idx * 32, 1u, __ATOMIC_RELAXED, __HIP_MEMORY_SCOPE_AGENT);
                wait_counters(ctrl, C_OB + idx * 32, 1, idx == 0 ? NO + NS : NO, lane);
            }
            __syncthreads();
        };
        auto bf2f = [](unsigned short u) -> float { return u2f((unsigned)u << 16); };
        c32* tw = (c32*)(lds + 96 * 1024);
        c32* wbuf = (c32*)(lds) + wv * 720;
        for (int i = tid; i < 640; i += NT) { float sn, cs_; sincospif(-(float)i / 320.f, &sn, &cs_); tw[i] = c32{cs_, sn}; }
        float gwv[10];
#pragma unroll
        for (int j = 0; j < 10; ++j) gwv[j] = bf2f(a.gw[64 * j + lane]);
        __syncthreads();
        for (int task = o; task < 128; task += NO) {
            {
                const int r0 = task * 16 + wv * 2;
                c32 v[10]; float ga = 0.f, gb_ = 0.f;
#pragma unroll
                for (int j = 0; j < 10; ++j) {
                    v[j] = c32{bf2f(a.x[(size_t)r0 * D_ + 64 * j + lane]), bf2f(a.x[(size_t)(r0 + 1) * D_ + 64 * j + lane])};
                    ga += rbf(v[j].x * gwv[j]); gb_ += rbf(v[j].y * gwv[j]);
                }
#pragma unroll
                for (int of = 32; of >= 1; of >>= 1) { ga += __shfl_xor(ga, of); gb_ += __shfl_xor(gb_, of); }
                if (lane < 2) {
                    const float z = rbf(lane ? gb_ : ga);
                    const float gv = rbf(1.f / (1.f + __expf(-z)));
                    __builtin_amdgcn_raw_buffer_store_b32(f2u(gv), rs, (unsigned)(OFF_G + (r0 + lane) * 4), 0, 16);
                }
                fft640(v, wbuf, tw, lane);
                __syncthreads();
                for (int i = tid; i < 321 * 16; i += NT) {
                    const int k = i >> 4, t = i & 15;
                    const c32* zb = (const c32*)lds + (t >> 1) * 720;
                    const c32 z1 = zb[k], z2 = zb[k ? 640 - k : 0];
                    c32 r = (t & 1) ? c32{0.5f * (z1.y + z2.y), 0.5f * (z2.x - z1.x)} : c32{0.5f * (z1.x + z2.x), 0.5f * (z1.y - z2.y)};
                    __builtin_amdgcn_raw_buffer_store_b64(__builtin_bit_cast(u32x2, r), rs, (unsigned)(OFF_X1T + ((size_t)k * S_ + task * 16 + t) * 8), 0, 16);
                }
                __syncthreads();
            }
        }
        for (int i = tid; i < 2048; i += NT) { float sn, cs_; sincospif(-(float)i / 1024.f, &sn, &cs_); tw[i] = c32{cs_, sn}; }
        obar(0);
        {
            const int j = tid & 255, hf = tid >> 8;
            c32* buf = (c32*)lds + hf * 2304;
            auto pd = [](int p) { return p + ((p >> 5) << 2); };
            for (int b = o; b < 161; b += NO) {
                const int col = min(2 * b + hf, 320);
                const unsigned cb = (unsigned)(OFF_X1T + (size_t)col * S_ * 8);
                c32 v[8];
#pragma unroll
                for (int q = 0; q < 8; ++q) v[q] = __builtin_bit_cast(c32, __builtin_amdgcn_raw_buffer_load_b64(rs, cb + (q * 256 + j) * 8, 0, 16));
                const u32x4 wa = __builtin_amdgcn_raw_buffer_load_b128(rs, (unsigned)(OFF_WT + ((size_t)col * S_ + 8 * j) * 4), 0, 16);
                const u32x4 wb = __builtin_amdgcn_raw_buffer_load_b128(rs, (unsigned)(OFF_WT + ((size_t)col * S_ + 8 * j + 4) * 4), 0, 16);
                dft8<-1>(v);
#pragma unroll
                for (int q = 0; q < 8; ++q) buf[pd(q * 256 + j)] = q ? cmul(v[q], tw[j * q]) : v[q];
                __syncthreads();
                const int b2 = (j >> 5) * 256 + (j & 31), lo = j & 31;
#pragma unroll
                for (int q = 0; q < 8; ++q) v[q] = buf[pd(b2 + q * 32)];
                dft8<-1>(v);
#pragma unroll
                for (int q = 0; q < 8; ++q) buf[pd(b2 + q * 32)] = q ? cmul(v[q], tw[8 * lo * q]) : v[q];
                __syncthreads();
                const int b3 = (j >> 2) * 32 + (j & 3), n0 = j & 3;
#pragma unroll
                for (int q = 0; q < 8; ++q) v[q] = buf[pd(b3 + q * 4)];
                dft8<-1>(v);
#pragma unroll
                for (int q = 0; q < 8; ++q) buf[pd(b3 + q * 4)] = q ? cmul(v[q], tw[64 * n0 * q]) : v[q];
                __syncthreads();
#pragma unroll
                for (int q = 0; q < 8; ++q) v[q] = buf[pd(8 * j + q)];
                dft4<-1>(v[0], v[1], v[2], v[3]); dft4<-1>(v[4], v[5], v[6], v[7]);
                const unsigned wv8[8] = {wa.x, wa.y, wa.z, wa.w, wb.x, wb.y, wb.z, wb.w};
#pragma unroll
                for (int q = 0; q < 8; ++q) v[q] = cmul(v[q], c32{u2f(wv8[q] << 16), u2f(wv8[q] & 0xFFFF0000u)});
                dft4<1>(v[0], v[1], v[2], v[3]); dft4<1>(v[4], v[5], v[6], v[7]);
#pragma unroll
                for (int q = 0; q < 8; ++q) buf[pd(8 * j + q)] = v[q];
                __syncthreads();
#pragma unroll
                for (int q = 0; q < 8; ++q) { c32 t = buf[pd(b3 + q * 4)]; v[q] = q ? cmulc(t, tw[64 * n0 * q]) : t; }
                dft8<1>(v);
#pragma unroll
                for (int q = 0; q < 8; ++q) buf[pd(b3 + q * 4)] = v[q];
                __syncthreads();
#pragma unroll
                for (int q = 0; q < 8; ++q) { c32 t = buf[pd(b2 + q * 32)]; v[q] = q ? cmulc(t, tw[8 * lo * q]) : t; }
                dft8<1>(v);
#pragma unroll
                for (int q = 0; q < 8; ++q) buf[pd(b2 + q * 32)] = v[q];
                __syncthreads();
#pragma unroll
                for (int q = 0; q < 8; ++q) { c32 t = buf[pd(q * 256 + j)]; v[q] = q ? cmulc(t, tw[j * q]) : t; }
                dft8<1>(v);
                if (2 * b + hf <= 320) {
#pragma unroll
                    for (int q = 0; q < 8; ++q) __builtin_amdgcn_raw_buffer_store_b64(__builtin_bit_cast(u32x2, v[q]), rs, cb + (q * 256 + j) * 8, 0, 16);
                }
                __syncthreads();
            }
        }
        for (int i = tid; i < 640; i += NT) { float sn, cs_; sincospif(-(float)i / 320.f, &sn, &cs_); tw[i] = c32{cs_, sn}; }
        obar(1);
        for (int task = o; task < 128; task += NO) {
            for (int i = tid; i < 321 * 8; i += NT) {
                const int k = i >> 3, p = i & 7;
                u32x4 y = __builtin_amdgcn_raw_buffer_load_b128(rs, (unsigned)(OFF_X1T + ((size_t)k * S_ + task * 16 + 2 * p) * 8), 0, 16);
                float ar = u2f(y.x), ai = u2f(y.y), br = u2f(y.z), bi = u2f(y.w);
                if (k == 0 || k == 320) { ai = 0.f; bi = 0.f; }
                c32* zb = (c32*)lds + p * 720;
                zb[k] = c32{ar - bi, -(ai + br)};
                if (k > 0 && k < 320) zb[640 - k] = c32{ar + bi, -(br - ai)};
            }
            __syncthreads();
            c32 v[10];
#pragma unroll
            for (int j = 0; j < 10; ++j) v[j] = wbuf[64 * j + lane];
            LDSWAIT();
            fft640(v, wbuf, tw, lane);
            const float sc = 1.f / (2048.f * 640.f);
            const int r0 = task * 16 + wv * 2;
            for (int i = lane; i < 320; i += 64) {
                const c32 z0 = wbuf[2 * i], z1 = wbuf[2 * i + 1];
                __builtin_amdgcn_raw_buffer_store_b32(pk_bf16(z0.x * sc, z1.x * sc), rs, (unsigned)(OFF_A + ((size_t)r0 * D_ + 2 * i) * 2), 0, 16);
                __builtin_amdgcn_raw_buffer_store_b32(pk_bf16(-z0.y * sc, -z1.y * sc), rs, (unsigned)(OFF_A + ((size_t)(r0 + 1) * D_ + 2 * i) * 2), 0, 16);
            }
            __syncthreads();
        }
        obar(2);
        if (o < 60) {
            const int grp = o / 5, c = o - grp * 5;
            const int ct = 8 * c + wv;
            const bool hi = n >= 8;
            unsigned vp = 0, zp = 0, ap = 0;
            for (int it = 0; it < 6; ++it) {
                int s = grp + 12 * it, qlo = 0, qhi = 4;
                if (it == 5) {
                    if (grp < 1) s = 60;
                    else if (grp < 3) { s = 61; qlo = 2 * (grp - 1); qhi = qlo + 2; }
                    else if (grp < 7) { s = 62; qlo = grp - 3; qhi = qlo + 1; }
                    else break;
                }
                if (s == 0) wait_counters(ctrl, C_V0, 32, 1, lane);
                else if (vp <= (unsigned)s) vp = wait_counters(ctrl, C_VP, 32, s + 1, lane);
                u32x4 uf[32];
#pragma unroll
                for (int i = 0; i < 32; ++i) {
                    uf[i] = __builtin_amdgcn_raw_buffer_load_b128(rs, (unsigned)(OFF_UF + ((size_t)s * NB + ct) * 32768) + lane * 16, i * 1024, 16);
                    if ((i & 3) == 3) { __builtin_amdgcn_sched_barrier(0); __builtin_amdgcn_s_sleep(24); __builtin_amdgcn_sched_barrier(0); }
                }
                const int et = tid >> 4, c8 = (tid & 15) * 8, row = 32 * s + et;
                const u32x4 av = __builtin_amdgcn_raw_buffer_load_b128(rs, (unsigned)(OFF_A + ((size_t)row * D_ + 128 * c + c8) * 2), 0, 16);
                const float gv = u2f(__builtin_amdgcn_raw_buffer_load_b32(rs, (unsigned)(OFF_G + row * 4), 0, 16));
                if (zp <= (unsigned)s) zp = wait_counters(ctrl, C_ZP + ct * 32, 1, s + 1, lane);
                u32x4 zq[8];
#pragma unroll
                for (int i = 0; i < 8; ++i) zq[i] = __builtin_amdgcn_raw_buffer_load_b128(rs, (unsigned)(OFF_ZF + ((size_t)s * NB + ct) * 8192) + lane * 16, i * 1024, 16);
                if (ap <= (unsigned)s) ap = wait_counters(ctrl, C_AP, 32, s + 1, lane);
                for (int c0 = qlo * 1280; c0 < qhi * 1280; c0 += 2560) {
                    u32x4 t[5];
#pragma unroll
                    for (int i = 0; i < 5; ++i) t[i] = __builtin_amdgcn_raw_buffer_load_b128(rs, (unsigned)(OFF_PXF + (size_t)s * 81920) + min(c0 + i * NT + tid, 5119) * 16, 0, 16);
#pragma unroll
                    for (int i = 0; i < 5; ++i) *(u32x4*)(lds + min(c0 + i * NT + tid, 5119) * 16) = t[i];
                }
                __syncthreads();
                float* tile = (float*)(lds + 81920);
                const unsigned pmo = 2048 + g * 128 + (n & 7) * 16;
#pragma unroll
                for (int qo = 0; qo < 4; ++qo) {
                    if (qo < qlo || qo >= qhi) continue;
                    f32x4 xa = {0.f, 0.f, 0.f, 0.f}, xb = xa, ma = xa;
#pragma unroll
                    for (int wg = 0; wg < 8; ++wg) {
                        const char* pbp = lds + qo * 20480 + wg * 2560;
                        const bf16x8 p0 = *(const bf16x8*)(pbp + lane * 16), p1 = *(const bf16x8*)(pbp + 1024 + lane * 16);
                        const bf16x8 p2 = *(const bf16x8*)(pbp + pmo);
                        xa = MFMA32(BC8(uf[4 * wg]), p0, xa); xb = MFMA32(BC8(uf[4 * wg + 1]), p0, xb);
                        xa = MFMA32(BC8(uf[4 * wg + 2]), p1, xa); xb = MFMA32(BC8(uf[4 * wg + 3]), p1, xb);
                        ma = MFMA32(BC8(zq[wg]), p2, ma);
                    }
                    f32x4 tot;
#pragma unroll
                    for (int r = 0; r < 4; ++r) {
                        const float y = hi ? xb[r] : xa[r] + ma[r];
                        tot[r] = y + u2f(__builtin_amdgcn_update_dpp(0, (int)f2u(y), 0x128, 0xf, 0xf, false));
                    }
                    if (n < 8) *(f32x4*)(tile + (8 * qo + n) * 132 + wv * 16 + 4 * g) = tot;
                }
                __syncthreads();
                if (et >= 8 * qlo && et < 8 * qhi) {
                    const f32x4 b0 = *(const f32x4*)(tile + et * 132 + c8), b1 = *(const f32x4*)(tile + et * 132 + c8 + 4);
                    const float og = rbf(1.f - gv);
                    auto mix = [&](unsigned aw, float x0, float x1) -> unsigned {
                        return pk_bf16(rbf(gv * u2f(aw << 16)) + rbf(og * rbf(x0)), rbf(gv * u2f(aw & 0xFFFF0000u)) + rbf(og * rbf(x1)));
                    };
                    u32x4 ov = {mix(av.x, b0[0], b0[1]), mix(av.y, b0[2], b0[3]), mix(av.z, b1[0], b1[1]), mix(av.w, b1[2], b1[3])};
                    *(u32x4*)(a.out + (size_t)row * D_ + 128 * c + c8) = ov;
                }
                __syncthreads();
            }
        }
    }
}

extern "C" void kernel_launch(void* const* d_in, const int* in_sizes, int n_in, void* d_out, int out_size, void* d_ws, size_t ws_size, hipStream_t stream) {
    char* ws = (char*)(((uintptr_t)d_ws + 2 * MB - 1) & ~(uintptr_t)(2 * MB - 1));
    (void)hipMemsetAsync(ws + OFF_CTRL, 0, CTRL_WORDS * 4, stream);
    MainArgs a;
    a.x = (const u16*)d_in[0]; a.mem0 = (const u16*)d_in[4]; a.Wq = (const u16*)d_in[5]; a.Wk = (const u16*)d_in[6]; a.Wv = (const u16*)d_in[7]; a.Wo = (const u16*)d_in[8];
    a.wr = (const u16*)d_in[1]; a.wi = (const u16*)d_in[2]; a.gw = (const u16*)d_in[10]; a.ws = ws; a.out = (u16*)d_out;
    (void)hipFuncSetAttribute((const void*)k_main, hipFuncAttributeMaxDynamicSharedMemorySize, LDS_BYTES);
    k_main<<<256, NT, LDS_BYTES, stream>>>(a);
}
```

```cpp
#include <hip/hip_runtime.h>
#include <hip/hip_bf16.h>
#include <stdint.h>

#define LAS __attribute__((address_space(3)))
typedef __attribute__((ext_vector_type(8))) __bf16 bf16x8;
typedef __attribute__((ext_vector_type(16))) __bf16 bf16x16;
typedef __attribute__((ext_vector_type(4))) short s16x4;
typedef __attribute__((ext_vector_type(8))) short s16x8;
typedef __attribute__((ext_vector_type(16))) short s16x16;
typedef __attribute__((ext_vector_type(4))) float f32x4;
typedef __attribute__((ext_vector_type(4))) unsigned u32x4;
typedef __attribute__((ext_vector_type(2))) unsigned u32x2;
typedef unsigned short u16;
typedef __amdgpu_buffer_rsrc_t rsrc_t;

namespace gm {
constexpr int S_ = 2048, D_ = 640, H_ = 32, E_ = 128, NSEG = 64;
constexpr int NA = 32, NM = 32, NB = 40, NREC = NA + NM + NB, NP = 32, NV = 32, NO = 64;
constexpr int R_M = NA, R_B = NA + NM, R_P = NREC, R_V = R_P + NP, R_O = R_V + NV, R_S = R_O + NO;
constexpr int NS = 256 - R_S;
constexpr int NT = 512;
constexpr size_t MB = 1024 * 1024;
constexpr size_t OFF_HO = 0;
constexpr size_t OFF_CTRL = 4 * MB;
constexpr size_t OFF_XF = 5 * MB;
constexpr size_t OFF_KXF = 8 * MB;
constexpr size_t OFF_QXF = 24 * MB;
constexpr size_t OFF_UF = 40 * MB;
constexpr size_t OFF_ZF = 120 * MB;
constexpr size_t OFF_PXF = 144 * MB;
constexpr size_t OFF_X1T = 150 * MB;
constexpr size_t OFF_WT = 156 * MB;
constexpr size_t OFF_A = 159 * MB;
constexpr size_t OFF_G = 162 * MB;
constexpr int SLOT_P = 0, SLOT_MEM = 20480, SLOT_SIGA = 30720, SLOT_SIGB = 30848, SLOT_PXL = 52 * 8192;
constexpr int C_POL = 0, C_ABORT = 32, C_CNT = 64, C_CNT0 = C_CNT, C_CNT1 = C_CNT + 128;
constexpr int C_PP = 320, C_VP = C_PP + 32 * 32, C_AP = C_VP + 32 * 32, C_ZP = C_AP + 32 * 32, C_ATT = C_ZP + 40 * 32, C_OB = C_ATT + 64 * 32,
              C_XF = C_OB + 4 * 32, C_FILL = C_XF + 32, C_V0 = C_ATT, C_END = C_FILL + 32;
constexpr int CTRL_WORDS = 6976;
static_assert(C_END <= CTRL_WORDS, "ctrl");
constexpr unsigned SPIN_CAP = 1u << 20;
}
using namespace gm;

__device__ __forceinline__ unsigned xcc_id() { return (unsigned)__builtin_amdgcn_s_getreg(20 | (3 << 11)) & 0xfu; }
__host__ __device__ constexpr unsigned lmap(unsigned L) { return ((L >> 13) << 14) + (L & 8191u); }
__device__ __forceinline__ unsigned pk_bf16(float a, float b) {
    typedef __attribute__((ext_vector_type(2))) __bf16 bf2; typedef __attribute__((ext_vector_type(2))) float f2;
    f2 v = {a, b}; bf2 r = __builtin_convertvector(v, bf2); return __builtin_bit_cast(unsigned, r);
}
__device__ __forceinline__ unsigned f2u(float a) { return __builtin_bit_cast(unsigned, a); }
__device__ __forceinline__ float u2f(unsigned a) { return __builtin_bit_cast(float, a); }
__device__ __forceinline__ float rbf(float a) { return __builtin_bit_cast(float, pk_bf16(a, 0.f) << 16); }
__device__ __forceinline__ unsigned nosent(unsigned w) { return w < 0xFFFFFFFEu ? w : 0xFFFFFFFEu; }
#define BARRIER() do { asm volatile("s_waitcnt lgkmcnt(0)\n\ts_barrier" ::: "memory"); } while (0)
__device__ __forceinline__ bf16x8 lo8(bf16x16 w) { return __builtin_shufflevector(w, w, 0, 1, 2, 3, 4, 5, 6, 7); }
__device__ __forceinline__ bf16x8 hi8(bf16x16 w) { return __builtin_shufflevector(w, w, 8, 9, 10, 11, 12, 13, 14, 15); }
__host__ __device__ __forceinline__ int k2d(int k) { int G = k >> 4, L = k & 15, q = L >> 2, p = L & 3; return 16 * G + (p < 2 ? 2 * q + p : 8 + 2 * q + (p - 2)); }
__host__ __device__ __forceinline__ int frag_k(int gb, int jb) { return jb < 8 ? 8 * gb + jb : 32 + 8 * gb + (jb - 8); }
__device__ __forceinline__ void glds16(const void* gsrc, unsigned lds_dst) {
    unsigned keep;
    asm volatile("s_mov_b32 %0, m0\n\ts_mov_b32 m0, %2\n\ts_nop 0\n\tglobal_load_lds_dwordx4 %1, off sc1\n\ts_mov_b32 m0, %0"
                 : "=&s"(keep) : "v"(gsrc), "s"(lds_dst) : "memory");
}
typedef __attribute__((ext_vector_type(2))) float c32;
__device__ __forceinline__ c32 cmul(c32 a, c32 b) { return c32{a.x * b.x - a.y * b.y, a.x * b.y + a.y * b.x}; }
__device__ __forceinline__ c32 cmulc(c32 a, c32 b) { return c32{a.x * b.x + a.y * b.y, a.y * b.x - a.x * b.y}; }
template <int SG> __device__ __forceinline__ c32 rot90(c32 a) { return SG < 0 ? c32{a.y, -a.x} : c32{-a.y, a.x}; }
template <int SG> __device__ __forceinline__ void dft4(c32& a0, c32& a1, c32& a2, c32& a3) {
    c32 t0 = a0 + a2, t1 = a0 - a2, t2 = a1 + a3, t3 = rot90<SG>(a1 - a3);
    a0 = t0 + t2; a1 = t1 + t3; a2 = t0 - t2; a3 = t1 - t3;
}
template <int SG> __device__ __forceinline__ void dft8(c32 (&v)[8]) {
    const float r = 0.70710678118654752f;
    c32 e0 = v[0], e1 = v[2], e2 = v[4], e3 = v[6], o0 = v[1], o1 = v[3], o2 = v[5], o3 = v[7];
    dft4<SG>(e0, e1, e2, e3); dft4<SG>(o0, o1, o2, o3);
    c32 p1 = (SG < 0) ? c32{(o1.x + o1.y) * r, (o1.y - o1.x) * r} : c32{(o1.x - o1.y) * r, (o1.y + o1.x) * r};
    c32 p2 = rot90<SG>(o2);
    c32 p3 = (SG < 0) ? c32{(o3.y - o3.x) * r, -(o3.x + o3.y) * r} : c32{-(o3.x + o3.y) * r, (o3.x - o3.y) * r};
    v[0] = e0 + o0; v[4] = e0 - o0; v[1] = e1 + p1; v[5] = e1 - p1; v[2] = e2 + p2; v[6] = e2 - p2; v[3] = e3 + p3; v[7] = e3 - p3;
}
__device__ __forceinline__ void dft5f(c32& a0, c32& a1, c32& a2, c32& a3, c32& a4) {
    const float c1 = 0.30901699437494742f, c2 = -0.80901699437494742f, s1 = 0.95105651629515357f, s2 = 0.58778525229247313f;
    c32 t1 = a1 + a4, t2 = a2 + a3, t3 = a1 - a4, t4 = a2 - a3;
    c32 m1 = a0 + c1 * t1 + c2 * t2, m2 = a0 + c2 * t1 + c1 * t2;
    c32 n1 = s1 * t3 + s2 * t4, n2 = s2 * t3 - s1 * t4;
    a0 = a0 + t1 + t2;
    a1 = c32{m1.x + n1.y, m1.y - n1.x}; a4 = c32{m1.x - n1.y, m1.y + n1.x};
    a2 = c32{m2.x + n2.y, m2.y - n2.x}; a3 = c32{m2.x - n2.y, m2.y + n2.x};
}
__device__ __forceinline__ void dft10f(c32 (&v)[10]) {
    c32 a[5], b[5];
#pragma unroll
    for (int n2 = 0; n2 < 5; ++n2) { c32 x0 = v[(2 * n2) % 10], x1 = v[(5 + 2 * n2) % 10]; a[n2] = x0 + x1; b[n2] = x0 - x1; }
    dft5f(a[0], a[1], a[2], a[3], a[4]); dft5f(b[0], b[1], b[2], b[3], b[4]);
#pragma unroll
    for (int k2 = 0; k2 < 5; ++k2) { v[(6 * k2) % 10] = a[k2]; v[(5 + 6 * k2) % 10] = b[k2]; }
}
#define LDSWAIT() asm volatile("s_waitcnt lgkmcnt(0)" ::: "memory")
__device__ __forceinline__ void fft640(c32 (&v)[10], c32* buf, const c32* tw, int lane) {
    dft10f(v);
#pragma unroll
    for (int k1 = 0; k1 < 10; ++k1) { c32 t = k1 ? cmul(v[k1], tw[lane * k1]) : v[k1]; buf[k1 * 72 + (lane >> 3) * 9 + (lane & 7)] = t; }
    LDSWAIT();
    c32 w[2][8];
#pragma unroll
    for (int rd = 0; rd < 2; ++rd) {
        const int id = lane + 64 * rd;
        if (id < 80) {
            const int k1 = id >> 3, b = id & 7;
#pragma unroll
            for (int a = 0; a < 8; ++a) w[rd][a] = buf[k1 * 72 + a * 9 + b];
            dft8<-1>(w[rd]);
#pragma unroll
            for (int c = 1; c < 8; ++c) w[rd][c] = cmul(w[rd][c], tw[10 * b * c]);
        }
    }
    LDSWAIT();
#pragma unroll
    for (int rd = 0; rd < 2; ++rd) {
        const int id = lane + 64 * rd;
        if (id < 80) {
            const int k1 = id >> 3, b = id & 7;
#pragma unroll
            for (int c = 0; c < 8; ++c) buf[k1 * 72 + c * 9 + b] = w[rd][c];
        }
    }
    LDSWAIT();
#pragma unroll
    for (int rd = 0; rd < 2; ++rd) {
        const int id = lane + 64 * rd;
        if (id < 80) {
            const int k1 = id >> 3, c = id & 7;
#pragma unroll
            for (int b = 0; b < 8; ++b) w[rd][b] = buf[k1 * 72 + c * 9 + b];
            dft8<-1>(w[rd]);
        }
    }
    LDSWAIT();
#pragma unroll
    for (int rd = 0; rd < 2; ++rd) {
        const int id = lane + 64 * rd;
        if (id < 80) {
            const int k1 = id >> 3, c = id & 7;
#pragma unroll
            for (int d = 0; d < 8; ++d) buf[k1 + 10 * (c + 8 * d)] = w[rd][d];
        }
    }
    LDSWAIT();
}

struct MainArgs {
    const u16* x; const u16* mem0; const u16* Wq; const u16* Wk; const u16* Wv; const u16* Wo; const u16* wr; const u16* wi; const u16* gw; char* ws; u16* out;
};
#define DBC 16
#define DAC 16
#define ATSLEEP 8
#define ZLEAD 6
constexpr int L_XB = 0;
constexpr int L_MF = 81920;
constexpr int L_QF = 92160;
constexpr int L_KM = 96256;
constexpr int L_KX = 100352;
constexpr int L_T = 122880;
constexpr int L_RED = 0;
constexpr int L_UB = 8192;
constexpr int L_MISC = 139264;
constexpr int LDS_BYTES = 152 * 1024;

__device__ __forceinline__ unsigned rt32() { return (unsigned)__builtin_amdgcn_s_memrealtime(); }
__device__ __forceinline__ void wait_rt(unsigned target) {
    if ((int)(target - rt32()) > 2000) return;
    while ((int)(rt32() - target) < 0) {}
}
__device__ __forceinline__ unsigned umax3(unsigned a, unsigned b, unsigned c) { return max(max(a, b), c); }
__device__ __forceinline__ bool spin_abort(unsigned& spins, unsigned* ctrl) {
    ++spins;
    if (spins > SPIN_CAP) { __hip_atomic_store(ctrl + C_ABORT, 1u, __ATOMIC_RELAXED, __HIP_MEMORY_SCOPE_AGENT); return true; }
    if ((spins & 1023u) == 0u) return __builtin_amdgcn_readfirstlane(__hip_atomic_load(ctrl + C_ABORT, __ATOMIC_RELAXED, __HIP_MEMORY_SCOPE_AGENT)) != 0;
    return false;
}
__device__ __forceinline__ unsigned read_sig(const char* p, unsigned* ctrl) {
    unsigned v, spins = 0;
    for (;;) {
        asm volatile("v_mov_b32 %0, 0\n\tglobal_load_dword %0, %0, %1 sc1\n\ts_waitcnt vmcnt(0)" : "=&v"(v) : "s"(p) : "memory");
        v = __builtin_amdgcn_readfirstlane(v);
        if (v != ~0u) break;
        if (spin_abort(spins, ctrl)) break;
    }
    return v;
}
__device__ __forceinline__ unsigned wait_counters(unsigned* ctrl, int base, int cnt, unsigned need, int lane) {
    unsigned spins = 0, m;
    const unsigned off = (unsigned)(base + (lane < cnt ? lane : 0) * 32) * 4u;
    for (;;) {
        unsigned v;
        asm volatile("global_load_dword %0, %1, %2 sc1\n\ts_waitcnt vmcnt(0)" : "=&v"(v) : "v"(off), "s"(ctrl) : "memory");
        if (cnt > 1) {
#pragma unroll
            for (int o = 16; o >= 1; o >>= 1) v = min(v, (unsigned)__shfl_xor((int)v, o));
        }
        m = __builtin_amdgcn_readfirstlane(v);
        if (m >= need) break;
        __builtin_amdgcn_s_sleep(4);
        if (spin_abort(spins, ctrl)) break;
    }
    return m;
}
__device__ __forceinline__ void set_counter(unsigned* ctrl, int idx, unsigned v) { __hip_atomic_store(ctrl + idx, v, __ATOMIC_RELAXED, __HIP_MEMORY_SCOPE_AGENT); }
__device__ __forceinline__ void st16_sc1(const void* base, unsigned off, u32x4 v) { asm volatile("global_store_dwordx4 %0, %1, %2 sc1\n\ts_nop 1" :: "v"(off), "v"(v), "s"(base) : "memory"); }
__device__ __forceinline__ void st8_sc1(const void* base, unsigned off, u32x2 v) { asm volatile("global_store_dwordx2 %0, %1, %2 sc1\n\ts_nop 1" :: "v"(off), "v"(v), "s"(base) : "memory"); }
__device__ __forceinline__ void st4_sc1(const void* base, unsigned off, unsigned v) { asm volatile("global_store_dword %0, %1, %2 sc1" :: "v"(off), "v"(v), "s"(base) : "memory"); }
__device__ __forceinline__ void load_mem0(const u16* mem0, char* lds, int tid) {
    for (int c = tid; c < 640; c += NT) {
        const int kb = c >> 6, l = c & 63;
        *(u32x4*)(lds + L_MF + c * 16) = *(const u32x4*)(mem0 + (l & 7) * D_ + 64 * kb + 16 * (l >> 4) + 8 * ((l >> 3) & 1));
    }
}
__device__ __forceinline__ void prefetch_x(const char* xf, int seg, int buf, int wv, int lane) {
    const char* src = xf + (size_t)seg * 40960 + wv * 5120 + lane * 16;
    const unsigned dst = L_XB + buf * 40960 + wv * 5120;
#pragma unroll
    for (int i = 0; i < 5; ++i) glds16(src + i * 1024, dst + i * 1024);
}
#define MFMA32(a, b, c) __builtin_amdgcn_mfma_f32_16x16x32_bf16((a), (b), (c), 0, 0, 0)
#define SMFMAC(a, b, c) __builtin_amdgcn_smfmac_f32_16x16x64_bf16((a), (b), (c), sidx, 0, 0)
#define BC8(x) __builtin_bit_cast(bf16x8, (x))
__device__ __forceinline__ bf16x16 cat4(s16x4 a, s16x4 b, s16x4 c, s16x4 d) {
    s16x8 x = __builtin_shufflevector(a, b, 0, 1, 2, 3, 4, 5, 6, 7), y = __builtin_shufflevector(c, d, 0, 1, 2, 3, 4, 5, 6, 7);
    return __builtin_bit_cast(bf16x16, (s16x16)__builtin_shufflevector(x, y, 0, 1, 2, 3, 4, 5, 6, 7, 8, 9, 10, 11, 12, 13, 14, 15));
}
#define TRREAD(p) __builtin_amdgcn_ds_read_tr16_b64_v4i16((LAS s16x4*)(p))
__device__ __forceinline__ void load_w2(const u16* Wa, const u16* Wb, int h, char* lds, int tid, int wv, bf16x16 (&wa)[10], bf16x16 (&wb)[10]) {
    const int lane = tid & 63, g = lane >> 4, n = lane & 15;
    u32x4 st[10][4];
    const unsigned wo_ = (unsigned)(((tid >> 4) * H_ + h) * E_ + (tid & 15) * 8) * 2u;
#pragma unroll
    for (int kb = 0; kb < 10; ++kb)
#pragma unroll
        for (int i = 0; i < 4; ++i) {
            const char* W = (const char*)((i & 2) ? Wb : Wa) + (size_t)(64 * kb + 32 * (i & 1)) * (H_ * E_ * 2);
            st[kb][i] = *(const u32x4*)(W + wo_);
        }
    const int q4 = n >> 2, p4 = n & 3;
#pragma unroll
    for (int kb = 0; kb < 10; ++kb) {
        char* sb = lds + (kb & 1) * 33792;
#pragma unroll
        for (int i = 0; i < 4; ++i)
            *(u32x4*)(sb + ((tid >> 4) + 32 * (i & 1)) * 528 + (i >> 1) * 256 + (tid & 15) * 16) = st[kb][i];
        BARRIER();
        s16x4 r[2][4];
#pragma unroll
        for (int tl = 0; tl < 2; ++tl)
#pragma unroll
            for (int u = 0; u < 4; ++u) r[tl][u] = TRREAD(sb + k2d(frag_k(g, 4 * u + q4)) * 528 + tl * 256 + (16 * wv + 4 * p4) * 2);
        wa[kb] = cat4(r[0][0], r[0][1], r[0][2], r[0][3]);
        wb[kb] = cat4(r[1][0], r[1][1], r[1][2], r[1][3]);
        asm volatile("" : "+v"(wa[kb]), "+v"(wb[kb]));
    }
    BARRIER();
}
__device__ __forceinline__ void load_wvo(const u16* Wv, const u16* Wo, int h, char* lds, int tid, int wv, bf16x16 (&wvv)[10], bf16x16 (&wo)[5][2]) {
    const int lane = tid & 63, g = lane >> 4, n = lane & 15;
    u32x4 st[10][2], so[2][10];
    const unsigned wo_ = (unsigned)(((tid >> 4) * H_ + h) * E_ + (tid & 15) * 8) * 2u;
#pragma unroll
    for (int kb = 0; kb < 10; ++kb)
#pragma unroll
        for (int i = 0; i < 2; ++i) st[kb][i] = *(const u32x4*)((const char*)Wv + (size_t)(64 * kb + 32 * i) * (H_ * E_ * 2) + wo_);
#pragma unroll
    for (int t = 0; t < 2; ++t)
#pragma unroll
        for (int i = 0; i < 10; ++i) so[t][i] = *(const u32x4*)((const char*)Wo + ((size_t)h * E_ + 64 * t) * (D_ * 2) + (size_t)i * (NT * 16) + tid * 16);
    const int q4 = n >> 2, p4 = n & 3;
#pragma unroll
    for (int kb = 0; kb < 10; ++kb) {
        char* sb = lds + (kb & 1) * 17408;
#pragma unroll
        for (int i = 0; i < 2; ++i) *(u32x4*)(sb + ((tid >> 4) + 32 * i) * 272 + (tid & 15) * 16) = st[kb][i];
        BARRIER();
        s16x4 r[4];
#pragma unroll
        for (int u = 0; u < 4; ++u) r[u] = TRREAD(sb + k2d(frag_k(g, 4 * u + q4)) * 272 + (16 * wv + 4 * p4) * 2);
        wvv[kb] = cat4(r[0], r[1], r[2], r[3]);
        asm volatile("" : "+v"(wvv[kb]));
    }
#pragma unroll
    for (int t = 0; t < 2; ++t) {
        BARRIER();
#pragma unroll
        for (int i = 0; i < 10; ++i) { const int c = i * NT + tid, e = c / 80, cc = c - e * 80; *(u32x4*)(lds + e * 1296 + cc * 16) = so[t][i]; }
        BARRIER();
#pragma unroll
        for (int ct = 0; ct < 5; ++ct) {
            s16x4 r[4];
#pragma unroll
            for (int u = 0; u < 4; ++u) r[u] = TRREAD(lds + k2d(frag_k(g, 4 * u + q4)) * 1296 + (16 * (5 * wv + ct) + 4 * p4) * 2);
            wo[ct][t] = cat4(r[0], r[1], r[2], r[3]);
            asm volatile("" : "+v"(wo[ct][t]));
        }
    }
    BARRIER();
}
__device__ __forceinline__ void tw_store(char* dst, f32x4 a0, f32x4 a1) {
    const unsigned p0 = pk_bf16(a0[0], a0[1]), p1 = pk_bf16(a0[2], a0[3]), p2 = pk_bf16(a1[0], a1[1]), p3 = pk_bf16(a1[2], a1[3]);
    *(u16*)(dst + 0) = (u16)p0; *(u16*)(dst + 16) = (u16)(p0 >> 16); *(u16*)(dst + 32) = (u16)p1; *(u16*)(dst + 48) = (u16)(p1 >> 16);
    *(u16*)(dst + 4096) = (u16)p2; *(u16*)(dst + 4096 + 16) = (u16)(p2 >> 16); *(u16*)(dst + 4096 + 32) = (u16)p3; *(u16*)(dst + 4096 + 48) = (u16)(p3 >> 16);
}
__device__ __forceinline__ void arrive(unsigned* ctrl, int idx, int tid) {
    asm volatile("s_waitcnt vmcnt(0)" ::: "memory");
    __syncthreads();
    if (tid == 0) __hip_atomic_fetch_add(ctrl + idx, 1u, __ATOMIC_RELAXED, __HIP_MEMORY_SCOPE_AGENT);
}
__device__ __forceinline__ u32x2 half_add(f32x4 a, f32x4 b) {
    float cv[4];
#pragma unroll
    for (int r = 0; r < 4; ++r) {
        auto sw = __builtin_amdgcn_permlane32_swap(f2u(a[r]), f2u(b[r]), false, false);
        cv[r] = u2f(sw[0]) + u2f(sw[1]);
    }
    return u32x2{pk_bf16(cv[0], cv[1]), pk_bf16(cv[2], cv[3])};
}

template <bool PF>
__device__ __forceinline__ void proj_qk(const char* xb, const bf16x16 (&wq)[10], const bf16x16 (&wk)[10], char* qdst, char* kdst, const char* psrc, unsigned pdst) {
    f32x4 q0 = {0.f, 0.f, 0.f, 0.f}, q1 = q0, k0 = q0, k1 = q0;
    bf16x8 xr[10][4];
#pragma unroll
    for (int i = 0; i < 4; ++i) xr[0][i] = *(const bf16x8*)(xb + i * 1024);
#pragma unroll
    for (int kb = 0; kb < 10; ++kb) {
        if (kb + 1 < 10) {
#pragma unroll
            for (int i = 0; i < 4; ++i) xr[kb + 1][i] = *(const bf16x8*)(xb + ((kb + 1) * 4 + i) * 1024);
        }
        __builtin_amdgcn_sched_barrier(0);
        q0 = MFMA32(xr[kb][0], lo8(wq[kb]), q0); q1 = MFMA32(xr[kb][1], lo8(wq[kb]), q1); k0 = MFMA32(xr[kb][0], lo8(wk[kb]), k0); k1 = MFMA32(xr[kb][1], lo8(wk[kb]), k1);
        q0 = MFMA32(xr[kb][2], hi8(wq[kb]), q0); q1 = MFMA32(xr[kb][3], hi8(wq[kb]), q1); k0 = MFMA32(xr[kb][2], hi8(wk[kb]), k0); k1 = MFMA32(xr[kb][3], hi8(wk[kb]), k1);
        __builtin_amdgcn_sched_barrier(0);
        if (PF && (kb & 1) == 0) glds16(psrc + (kb >> 1) * 1024, pdst + (kb >> 1) * 1024);
    }
    tw_store(qdst, q0, q1);
    tw_store(kdst, k0, k1);
}
template <bool PF>
__device__ __forceinline__ void proj_u(char* lds, const char* xb, const bf16x16 (&wvv)[10], const bf16x16 (&wo)[5][2], int wv, int lane, const char* ub, int h, const char* psrc, unsigned pdst, unsigned* cnt, unsigned cval) {
    const int g = lane >> 4, n = lane & 15;
    const int Lq = (n < 8) ? 4 * (n >> 1) + (n & 1) : 4 * ((n - 8) >> 1) + 2 + (n & 1);
    const unsigned twv_off = (wv >> 1) * 1024 + (2 * (wv & 1) + (Lq >> 3)) * 256 + (Lq & 7) * 2;
    const unsigned uo = (unsigned)(5 * wv * 32768 + h * 1024 + lane * 16);
    f32x4 v0 = {0.f, 0.f, 0.f, 0.f}, v1 = v0;
    bf16x8 xr[10][4];
#pragma unroll
    for (int kb = 0; kb < 2; ++kb)
#pragma unroll
        for (int i = 0; i < 4; ++i) xr[kb][i] = *(const bf16x8*)(xb + (kb * 4 + i) * 1024);
#pragma unroll
    for (int kb = 0; kb < 10; ++kb) {
        if (kb + 2 < 10) {
#pragma unroll
            for (int i = 0; i < 4; ++i) xr[kb + 2][i] = *(const bf16x8*)(xb + ((kb + 2) * 4 + i) * 1024);
        }
        __builtin_amdgcn_sched_barrier(0);
        v0 = MFMA32(xr[kb][0], lo8(wvv[kb]), v0); v1 = MFMA32(xr[kb][1], lo8(wvv[kb]), v1);
        v0 = MFMA32(xr[kb][2], hi8(wvv[kb]), v0); v1 = MFMA32(xr[kb][3], hi8(wvv[kb]), v1);
        __builtin_amdgcn_sched_barrier(0);
        if (PF && (kb & 1) == 0) glds16(psrc + (kb >> 1) * 1024, pdst + (kb >> 1) * 1024);
    }
    tw_store(lds + L_T + twv_off + g * 64, v0, v1);
    if (PF) asm volatile("s_waitcnt vmcnt(5)" ::: "memory");
    BARRIER();
    if (PF && wv == 0 && lane == 0) __hip_atomic_store(cnt, cval, __ATOMIC_RELAXED, __HIP_MEMORY_SCOPE_AGENT);
    bf16x8 vf[2][4];
#pragma unroll
    for (int tt = 0; tt < 2; ++tt)
#pragma unroll
        for (int t = 0; t < 4; ++t) vf[tt][t] = *(const bf16x8*)(lds + L_T + (tt * 4 + t) * 1024 + lane * 16);
#pragma unroll
    for (int ct = 0; ct < 5; ++ct) {
        f32x4 u0 = {0.f, 0.f, 0.f, 0.f}, u1 = u0;
#pragma unroll
        for (int t = 0; t < 2; ++t) {
            u0 = MFMA32(vf[0][2 * t], lo8(wo[ct][t]), u0); u1 = MFMA32(vf[1][2 * t], lo8(wo[ct][t]), u1);
            u0 = MFMA32(vf[0][2 * t + 1], hi8(wo[ct][t]), u0); u1 = MFMA32(vf[1][2 * t + 1], hi8(wo[ct][t]), u1);
        }
        u32x4 o = {pk_bf16(u0[0], u0[1]), pk_bf16(u0[2], u0[3]), pk_bf16(u1[0], u1[1]), pk_bf16(u1[2], u1[3])};
        st16_sc1(ub + ct * 32768, uo, o);
    }
}

__global__ void __launch_bounds__(NT, 2) k_main(MainArgs a) {
    extern __shared__ __attribute__((aligned(16))) char lds[];
    const int tid = threadIdx.x, lane = tid & 63;
    const int wv = __builtin_amdgcn_readfirstlane(tid >> 6);
    unsigned* ctrl = (unsigned*)(a.ws + OFF_CTRL);
    int role;
    {
        unsigned* sm = (unsigned*)(lds + L_MISC);
        if (tid == 0) {
            const unsigned xc = xcc_id();
            int r = 0;
            for (unsigned k = 0; k < 8; ++k) {
                const int x = (int)((xc + k) & 7u);
                const int t = (int)__hip_atomic_fetch_add(ctrl + C_CNT + x * 32, 1u, __ATOMIC_RELAXED, __HIP_MEMORY_SCOPE_AGENT);
                if (t < 32) {
                    if (x < 4) r = t < 8 ? x * 8 + t : t < 16 ? R_M + x * 8 + t - 8 : t < 26 ? R_B + x * 10 + t - 16 : R_S + x * 6 + t - 26;
                    else r = t < 8 ? R_P + (x - 4) * 8 + t : t < 16 ? R_V + (x - 4) * 8 + t - 8 : R_O + (x - 4) * 16 + t - 16;
                    break;
                }
            }
            sm[0] = (unsigned)r;
        }
        __syncthreads();
        role = __builtin_amdgcn_readfirstlane((int)sm[0]);
        __syncthreads();
    }
    const rsrc_t rs = __builtin_amdgcn_make_buffer_rsrc(a.ws, 0, (unsigned)(250 * MB), 0x00020000);
    if (role >= R_S) {
        const int sp = role - R_S;
        if (sp == 0) {
            if (tid == 0) {
                unsigned best[4] = {~0u, ~0u, ~0u, ~0u};
                for (int rep = 0; rep < 4; ++rep)
#pragma unroll
                    for (int t = 0; t < 4; ++t) {
                        unsigned* ad = (unsigned*)(a.ws + OFF_HO + (size_t)(t >> 1) * 2 * MB + 50 * 16384 + (t & 1) * 8192);
                        unsigned z = 0, r;
                        unsigned long long c0 = __builtin_amdgcn_s_memtime();
                        asm volatile("s_waitcnt lgkmcnt(0)\n\tglobal_atomic_add %0, %1, %2, off sc0\n\ts_waitcnt vmcnt(0)" : "=v"(r) : "v"(ad), "v"(z) : "memory");
                        unsigned long long c1 = __builtin_amdgcn_s_memtime();
                        unsigned d = (unsigned)(c1 - c0); best[t] = d < best[t] ? d : best[t];
                    }
                const int d0 = (int)(best[0] - best[1]), d1 = (int)(best[2] - best[3]);
                unsigned p0 = d0 > 0, p1 = d1 > 0;
                if (abs(d0) < 120 && abs(d1) >= 120) p0 = !p1;
                if (abs(d1) < 120 && abs(d0) >= 120) p1 = !p0;
                __hip_atomic_store(ctrl + C_POL, 0x100u | p0 | (p1 << 1), __ATOMIC_RELAXED, __HIP_MEMORY_SCOPE_AGENT);
            }
            __syncthreads();
        }
        for (int task = NS - 1 - sp; task < 32; task += NS) {
            const int p0 = task * 64;
            unsigned* tile = (unsigned*)lds;
            unsigned short xr[8][6], xi[8][6];
#pragma unroll
            for (int rr = 0; rr < 8; ++rr) {
                const int pos = p0 + wv + 8 * rr, m = (pos >> 8) + 8 * ((pos >> 5) & 7) + 64 * ((pos >> 2) & 7) + 512 * (pos & 3);
#pragma unroll
                for (int kk = 0; kk < 6; ++kk) { const int k = min(lane + 64 * kk, 320); xr[rr][kk] = a.wr[(size_t)m * 321 + k]; xi[rr][kk] = a.wi[(size_t)m * 321 + k]; }
            }
#pragma unroll
            for (int rr = 0; rr < 8; ++rr)
#pragma unroll
                for (int kk = 0; kk < 6; ++kk) { const int k = lane + 64 * kk; if (k < 321) tile[(wv + 8 * rr) * 323 + k] = (unsigned)xr[rr][kk] | ((unsigned)xi[rr][kk] << 16); }
            __syncthreads();
            for (int i = tid; i < 321 * 16; i += NT) {
                const int k = i >> 4, r = (i & 15) * 4;
                u32x4 v = {tile[r * 323 + k], tile[(r + 1) * 323 + k], tile[(r + 2) * 323 + k], tile[(r + 3) * 323 + k]};
                __builtin_amdgcn_raw_buffer_store_b128(v, rs, (unsigned)(OFF_WT + ((size_t)k * S_ + p0 + r) * 4), 0, 16);
            }
            __syncthreads();
        }
        arrive(ctrl, C_OB, tid);
        return;
    }
    auto get_pol = [&]() -> unsigned {
        if (wv == 0) {
            unsigned v, spins = 0;
            for (;;) {
                v = __builtin_amdgcn_readfirstlane(__hip_atomic_load(ctrl + C_POL, __ATOMIC_RELAXED, __HIP_MEMORY_SCOPE_AGENT));
                if (v & 0x100u) break;
                __builtin_amdgcn_s_sleep(2);
                if (spin_abort(spins, ctrl)) break;
            }
            wait_counters(ctrl, C_FILL, 1, NB, lane);
            if (lane == 0) *(volatile unsigned*)(lds + L_MISC + 32) = v;
        }
        __syncthreads();
        return __builtin_amdgcn_readfirstlane(*(volatile unsigned*)(lds + L_MISC + 32));
    };
    const char* xf = a.ws + OFF_XF;
    char* ho = a.ws + OFF_HO;
    const int g = lane >> 4, n = lane & 15;
    constexpr unsigned so_a = lmap(SLOT_SIGA), so_b = lmap(SLOT_SIGB);
    const int sidx = (lane & 8) ? 0xEEEE : 0x4444;
    const unsigned tw_off = (wv >> 1) * 1024 + (2 * (wv & 1) + (n >> 3)) * 256 + (n & 7) * 2;

    if (role < R_B) {
        const int h = (role & 31);
        const int gw = role < NA ? (wv < 2 ? wv : wv - 2) : wv;
        const int gn = role < NA ? ((wv == 2 || wv == 3) ? 0 : (gw < 4 ? 2 : 1)) : (wv < 2 ? 2 : 1);
        const int f0 = gw, f1 = role < NA ? gw + 6 : gw + 8;
        const unsigned mo0 = lmap(SLOT_MEM + f0 * 1024 + lane * 16), mo1 = lmap(SLOT_MEM + (f1 < 10 ? f1 : 0) * 1024 + lane * 16);
        unsigned pol = 0, target = 0;
        auto slotp = [&](int s) -> char* { return ho + (size_t)(s & 3) * MB + (((pol >> ((s >> 1) & 1)) & 1) ? 8192 : 0); };
        auto gather = [&](int s) {
            if (s > 0 && gn > 0) {
                const char* pslot = slotp(s - 1);
                if (s > 1) wait_rt(target);
                unsigned spins = 0;
                if (gn == 2) {
                    u32x4 v, v2;
                    for (;;) {
                        asm volatile("global_load_dwordx4 %0, %2, %4 sc1\n\tglobal_load_dwordx4 %1, %3, %4 sc1\n\ts_waitcnt vmcnt(0)"
                                     : "=&v"(v), "=&v"(v2) : "v"(mo0), "v"(mo1), "s"(pslot) : "memory");
                        *(u32x4*)(lds + L_MF + f0 * 1024 + lane * 16) = v;
                        *(u32x4*)(lds + L_MF + f1 * 1024 + lane * 16) = v2;
                        __builtin_amdgcn_sched_barrier(0);
                        if (__all(max(umax3(umax3(v.x, v.y, v.z), v.w, v2.x), umax3(v2.y, v2.z, v2.w)) != ~0u)) break;
                        if (spin_abort(spins, ctrl)) break;
                    }
                } else {
                    u32x4 v;
                    for (;;) {
                        asm volatile("global_load_dwordx4 %0, %1, %2 sc1\n\ts_waitcnt vmcnt(0)" : "=&v"(v) : "v"(mo0), "s"(pslot) : "memory");
                        *(u32x4*)(lds + L_MF + f0 * 1024 + lane * 16) = v;
                        __builtin_amdgcn_sched_barrier(0);
                        if (__all(max(umax3(v.x, v.y, v.z), v.w) != ~0u)) break;
                        if (spin_abort(spins, ctrl)) break;
                    }
                }
            }
            BARRIER();
        };
        auto next_target = [&](int s) {
            if (s > 0 && s < NSEG - 1) {
                unsigned sa, gb, spins = 0;
                for (;;) {
                    asm volatile("v_mov_b32 %0, 0\n\tglobal_load_dword %1, %0, %3 sc1\n\tglobal_load_dword %0, %0, %2 sc1\n\ts_waitcnt vmcnt(0)"
                                 : "=&v"(sa), "=&v"(gb) : "s"(slotp(s) + so_a), "s"(slotp(s - 1) + so_b) : "memory");
                    sa = __builtin_amdgcn_readfirstlane(sa); gb = __builtin_amdgcn_readfirstlane(gb);
                    if (sa != ~0u && gb != ~0u) break;
                    if (spin_abort(spins, ctrl)) break;
                }
                target = sa + DBC + gb + DAC;
            }
        };
        if (role < NA) {
            bf16x16 wq[10], wk[10];
            load_w2(a.Wq, a.Wk, h, lds, tid, wv, wq, wk);
            if (wv == 0) wait_counters(ctrl, C_XF, 1, NO, lane);
            __syncthreads();
            prefetch_x(xf, 0, 0, wv, lane);
            load_mem0(a.mem0, lds, tid);
            pol = get_pol();
            asm volatile("s_waitcnt vmcnt(0)" ::: "memory");
            BARRIER();
            proj_qk<false>(lds + L_XB + lane * 16, wq, wk, lds + L_KX + 8192 + tw_off + g * 64, lds + L_KX + tw_off + g * 64, nullptr, 0);
            auto front = [&](int s, unsigned& t_sig, unsigned ap_est, auto&& hook) {
                gather(s);
                f32x4 aq = {0.f, 0.f, 0.f, 0.f}, ak = aq;
                bf16x8 mf[10];
#pragma unroll
                for (int kb = 0; kb < 3; ++kb) mf[kb] = *(const bf16x8*)(lds + L_MF + kb * 1024 + lane * 16);
                if (role == 0 && wv == 1) {
                    t_sig = rt32();
                    if (lane == 0) st4_sc1(slotp(s) + so_a, 0u, nosent(t_sig + ap_est));
                }
                __builtin_amdgcn_sched_barrier(0);
#pragma unroll
                for (int kb = 0; kb < 10; ++kb) {
                    aq = SMFMAC(mf[kb], wq[kb], aq); ak = SMFMAC(mf[kb], wk[kb], ak);
                    if (kb + 3 < 10) mf[kb + 3] = *(const bf16x8*)(lds + L_MF + (kb + 3) * 1024 + lane * 16);
                    __builtin_amdgcn_sched_barrier(0);
                }
                hook();
                const u32x2 p = half_add(aq, ak);
                char* qd = lds + L_QF + (lane >> 5) * 4096 + tw_off + (g & 1) * 64;
                *(u16*)(qd + 0) = (u16)p.x; *(u16*)(qd + 16) = (u16)(p.x >> 16); *(u16*)(qd + 32) = (u16)p.y; *(u16*)(qd + 48) = (u16)(p.y >> 16);
            };
            if (wv == 0) {
                const int wg = h >> 2;
                const unsigned po_x = lmap(SLOT_P + wg * 2560 + ((h >> 1) & 1) * 1024 + (g * 16 + (h & 1) * 8 + (n & 7)) * 16 + (n >> 3) * 8);
                const unsigned po_m = lmap(SLOT_P + wg * 2560 + 2048 + (h & 3) * 128 + (n & 7) * 16 + (g & 1) * 8 + (n >> 3) * 4);
                const unsigned qoff = L_QF + (g * 16 + (n & 7)) * 16;
                const bool hi = n >= 8;
                unsigned dummy = 0;
                BARRIER();
                for (int s = 0; s < NSEG; ++s) {
                    bf16x8 kx[8];
                    front(s, dummy, 0, [&]() {
#pragma unroll
                        for (int t = 0; t < 8; ++t) kx[t] = *(const bf16x8*)(lds + L_KX + (s & 1) * 16384 + t * 1024 + lane * 16);
                    });
                    BARRIER();
                    if (s < NSEG - 1) {
                        char* slot = slotp(s);
                        f32x4 s0 = {0.f, 0.f, 0.f, 0.f}, s1 = s0, s2 = s0;
                        bf16x8 qf[4], km[4];
#pragma unroll
                        for (int t = 0; t < 4; ++t) { qf[t] = *(const bf16x8*)(lds + qoff + t * 1024); km[t] = *(const bf16x8*)(lds + L_KM + t * 1024 + lane * 16); }
#pragma unroll
                        for (int t = 0; t < 4; ++t) { s1 = MFMA32(kx[t], qf[t], s1); s2 = MFMA32(kx[4 + t], qf[t], s2); s0 = MFMA32(km[t], qf[t], s0); }
                        __builtin_amdgcn_sched_group_barrier(0x100, 8, 0);
                        __builtin_amdgcn_sched_group_barrier(0x8, 12, 0);
                        const float cexp = 0.08838834764831845f * 1.4426950408889634f;
                        float v[6];
#pragma unroll
                        for (int r = 0; r < 4; ++r) v[r] = hi ? s2[r] : s1[r];
                        v[4] = hi ? s0[2] : s0[0]; v[5] = hi ? s0[3] : s0[1];
                        if (g >= 2) { v[4] = -INFINITY; v[5] = -INFINITY; }
                        float m, tmp_;
                        asm("v_max3_f32 %0, %2, %3, %4\n\tv_max3_f32 %1, %5, %6, %7\n\tv_max_f32 %0, %0, %1\n\ts_nop 1\n\t"
                            "v_max_f32_dpp %0, %0, %0 row_ror:8 row_mask:0xf bank_mask:0xf\n\tv_mov_b32 %1, %0\n\ts_nop 1\n\t"
                            "v_permlane16_swap_b32 %0, %1\n\tv_max_f32 %0, %0, %1\n\tv_mov_b32 %1, %0\n\ts_nop 1\n\t"
                            "v_permlane32_swap_b32 %0, %1\n\tv_max_f32 %0, %0, %1"
                            : "=&v"(m), "=&v"(tmp_) : "v"(v[0]), "v"(v[1]), "v"(v[2]), "v"(v[3]), "v"(v[4]), "v"(v[5]));
                        const float mc = -m * cexp;
#pragma unroll
                        for (int r = 0; r < 6; ++r) v[r] = __builtin_amdgcn_exp2f(__builtin_fmaf(v[r], cexp, mc));
                        float sum = ((v[0] + v[1]) + (v[2] + v[3])) + (v[4] + v[5]);
                        sum += u2f(__builtin_amdgcn_update_dpp(0, (int)f2u(sum), 0x128, 0xf, 0xf, false));
                        {
                            auto sw = __builtin_amdgcn_permlane16_swap(f2u(sum), f2u(sum), false, false);
                            sum = u2f(sw[0]) + u2f(sw[1]);
                            auto sw2 = __builtin_amdgcn_permlane32_swap(f2u(sum), f2u(sum), false, false);
                            sum = u2f(sw2[0]) + u2f(sw2[1]);
                        }
                        const float inv = __builtin_amdgcn_rcpf(sum);
                        u32x2 px = {nosent(pk_bf16(v[0] * inv, v[1] * inv)), nosent(pk_bf16(v[2] * inv, v[3] * inv))};
                        const unsigned pm = nosent(pk_bf16(v[4] * inv, v[5] * inv));
                        st8_sc1(slot, po_x, px);
                        if (g < 2) st4_sc1(slot, po_m, pm);
                        if (role == 0) { const unsigned t = rt32(); if (lane == 0) *(volatile unsigned*)(lds + L_MISC + 48) = t; LDSWAIT(); }
                    }
                    asm volatile("s_barrier" ::: "memory");
                    if (s >= 2) {
                        st8_sc1(slotp(s + 2), po_x, u32x2{~0u, ~0u});
                        if (g < 2) st4_sc1(slotp(s + 2), po_m, ~0u);
                    }
                    target = __builtin_amdgcn_readfirstlane(*(volatile unsigned*)(lds + L_MISC + 64 + (s & 1) * 4));
                }
            } else if (wv == 2 || wv == 3) {
                const int qt = wv - 2;
                unsigned dummy = 0;
                BARRIER();
                const unsigned pbx = (2 * qt + (n >> 3)) * 20480 + (h >> 2) * 2560 + ((h >> 1) & 1) * 1024 + (g * 16 + (h & 1) * 8 + (n & 7)) * 16;
                const unsigned pbm = (2 * qt + (n >> 3)) * 20480 + (h >> 2) * 2560 + 2048 + (h & 3) * 128 + (n & 7) * 16 + (g & 1) * 8;
                for (int s = 0; s < NSEG; ++s) {
                    front(s, dummy, 0, []() {});
                    asm volatile("s_waitcnt vmcnt(0)" ::: "memory");
                    BARRIER();
                    __builtin_amdgcn_s_sleep(ATSLEEP);
                    const char* kq = lds + L_KX + (s & 1) * 16384 + lane * 16;
                    f32x4 s0 = {0.f, 0.f, 0.f, 0.f}, s1 = s0, s2 = s0;
#pragma unroll
                    for (int t = 0; t < 4; ++t) {
                        const bf16x8 km = *(const bf16x8*)(lds + L_KM + t * 1024 + lane * 16), qf = *(const bf16x8*)(kq + 8192 + (qt * 4 + t) * 1024);
                        const bf16x8 k1 = *(const bf16x8*)(kq + t * 1024), k2 = *(const bf16x8*)(kq + (4 + t) * 1024);
                        s1 = MFMA32(k1, qf, s1); s2 = MFMA32(k2, qf, s2); s0 = MFMA32(km, qf, s0);
                    }
                    const float cexp = 0.08838834764831845f * 1.4426950408889634f, masked = -999292928.f / 0.08838834764831845f;
                    const int qi = 16 * qt + n;
                    float v[12];
#pragma unroll
                    for (int r = 0; r < 4; ++r) {
                        v[r] = (g < 2) ? s0[r] : -INFINITY;
                        v[4 + r] = (4 * g + r <= qi) ? s1[r] : masked;
                        v[8 + r] = (16 + 4 * g + r <= qi) ? s2[r] : masked;
                    }
                    float m, tmp_;
                    asm("v_max3_f32 %0, %2, %3, %4\n\tv_max3_f32 %1, %5, %6, %7\n\tv_max3_f32 %0, %0, %8, %9\n\tv_max3_f32 %1, %1, %10, %11\n\tv_max3_f32 %0, %0, %12, %13\n\tv_max_f32 %0, %0, %1\n\t"
                        "v_mov_b32 %1, %0\n\ts_nop 1\n\tv_permlane16_swap_b32 %0, %1\n\tv_max_f32 %0, %0, %1\n\tv_mov_b32 %1, %0\n\ts_nop 1\n\t"
                        "v_permlane32_swap_b32 %0, %1\n\tv_max_f32 %0, %0, %1"
                        : "=&v"(m), "=&v"(tmp_) : "v"(v[0]), "v"(v[1]), "v"(v[2]), "v"(v[3]), "v"(v[4]), "v"(v[5]), "v"(v[6]), "v"(v[7]), "v"(v[8]), "v"(v[9]), "v"(v[10]), "v"(v[11]));
                    const float mc = -m * cexp;
#pragma unroll
                    for (int r = 0; r < 12; ++r) v[r] = __builtin_amdgcn_exp2f(__builtin_fmaf(v[r], cexp, mc));
                    float sum = ((v[0] + v[1]) + (v[2] + v[3])) + ((v[4] + v[5]) + (v[6] + v[7])) + ((v[8] + v[9]) + (v[10] + v[11]));
                    {
                        auto sw = __builtin_amdgcn_permlane16_swap(f2u(sum), f2u(sum), false, false);
                        sum = u2f(sw[0]) + u2f(sw[1]);
                        auto sw2 = __builtin_amdgcn_permlane32_swap(f2u(sum), f2u(sum), false, false);
                        sum = u2f(sw2[0]) + u2f(sw2[1]);
                    }
                    const float inv = __builtin_amdgcn_rcpf(sum);
                    u32x4 px = {pk_bf16(v[4] * inv, v[5] * inv), pk_bf16(v[6] * inv, v[7] * inv), pk_bf16(v[8] * inv, v[9] * inv), pk_bf16(v[10] * inv, v[11] * inv)};
                    u32x2 pm = {pk_bf16(v[0] * inv, v[1] * inv), pk_bf16(v[2] * inv, v[3] * inv)};
                    if (s < NSEG - 1) {
                        const char* pxb = a.ws + OFF_PXF + (size_t)s * 81920;
                        st16_sc1(pxb, pbx, px);
                        if (g < 2) st8_sc1(pxb, pbm, pm);
                    } else {
                        st16_sc1(slotp(0), lmap(SLOT_PXL + pbx), u32x4{nosent(px.x), nosent(px.y), nosent(px.z), nosent(px.w)});
                        if (g < 2) st8_sc1(slotp(0), lmap(SLOT_PXL + pbm), u32x2{nosent(pm.x), nosent(pm.y)});
                    }
                    asm volatile("s_barrier" ::: "memory");
                }
                asm volatile("s_waitcnt vmcnt(0)" ::: "memory");
            } else {
                unsigned ap_est = 70, t_sig = 0, pp = 0;
                auto load_qk = [&](int s) {
                    if (wv >= 4) {
                        __builtin_amdgcn_s_sleep(2);
                        if (pp <= (unsigned)s) pp = wait_counters(ctrl, C_PP + h * 32, 1, s + 1, lane);
                        const char* src = a.ws + (wv < 6 ? OFF_KXF : OFF_QXF) + ((size_t)s * H_ + h) * 8192 + (wv & 1) * 4096 + lane * 16;
#pragma unroll
                        for (int i = 0; i < 4; ++i) glds16(src + i * 1024, L_KX + (s & 1) * 16384 + (wv - 4) * 4096 + i * 1024);
                    }
                };
                BARRIER();
                for (int s = 0; s < NSEG; ++s) {
                    front(s, t_sig, ap_est, []() {});
                    BARRIER();
                    if (wv == 1) { next_target(s); if (lane == 0) *(volatile unsigned*)(lds + L_MISC + 64 + (s & 1) * 4) = target; }
                    if (s + 1 < NSEG) load_qk(s + 1);
                    BARRIER();
                    if (role == 0 && wv == 1) {
                        ap_est = __builtin_amdgcn_readfirstlane(*(volatile unsigned*)(lds + L_MISC + 48)) - t_sig;
                        if (s >= 2 && lane == 0) st4_sc1(slotp(s + 2) + so_a, 0u, ~0u);
                    }
                    if (wv == 4 && lane == 0 && s > 0) set_counter(ctrl, C_AP + h * 32, s);
                    target = __builtin_amdgcn_readfirstlane(*(volatile unsigned*)(lds + L_MISC + 64 + (s & 1) * 4));
                }
            }
            __syncthreads();
            if (tid == 0) set_counter(ctrl, C_AP + h * 32, NSEG);
        } else {
            bf16x16 wvv[10], wo[5][2];
            load_wvo(a.Wv, a.Wo, h, lds, tid, wv, wvv, wo);
            if (wv == 0) wait_counters(ctrl, C_XF, 1, NO, lane);
            __syncthreads();
            prefetch_x(xf, 0, 0, wv, lane);
            load_mem0(a.mem0, lds, tid);
            pol = get_pol();
            asm volatile("s_waitcnt vmcnt(0)" ::: "memory");
            BARRIER();
            proj_u<false>(lds, lds + L_XB + lane * 16, wvv, wo, wv, lane, a.ws + OFF_UF, h, nullptr, 0, nullptr, 0);
            asm volatile("s_waitcnt vmcnt(0)" ::: "memory");
            __syncthreads();
            if (tid == 0) set_counter(ctrl, C_V0 + h * 32, 1u);
            const unsigned vm_off = L_QF + (wv >> 2) * 1024 + ((wv & 3) * 16 + (n >> 3) * 8 + 4 * (g & 1)) * 16 + (n & 7) * 2;
            const unsigned zo4 = (4 + 5 * wv + (g >> 1) + 2 * (g & 1)) * 16384 + h * 256 + n * 16;
            const unsigned zo1 = (4 + 5 * wv + 4) * 16384 + h * 256 + n * 16 + (g & 1) * 8;
            for (int s = 0; s < NSEG; ++s) {
                gather(s);
                f32x4 av = {0.f, 0.f, 0.f, 0.f};
                bf16x8 mf[10];
#pragma unroll
                for (int kb = 0; kb < 4; ++kb) mf[kb] = *(const bf16x8*)(lds + L_MF + kb * 1024 + lane * 16);
                __builtin_amdgcn_sched_barrier(0);
#pragma unroll
                for (int kb = 0; kb < 10; ++kb) {
                    av = SMFMAC(mf[kb], wvv[kb], av);
                    if (kb + 4 < 10) mf[kb + 4] = *(const bf16x8*)(lds + L_MF + (kb + 4) * 1024 + lane * 16);
                    __builtin_amdgcn_sched_barrier(0);
                }
                const u32x2 p = half_add(av, av);
                char* vd = lds + vm_off;
                *(u16*)(vd + 0) = (u16)p.x; *(u16*)(vd + 16) = (u16)(p.x >> 16); *(u16*)(vd + 32) = (u16)p.y; *(u16*)(vd + 48) = (u16)(p.y >> 16);
                BARRIER();
                const bf16x8 vf0 = *(const bf16x8*)(lds + L_QF + lane * 16), vf1 = *(const bf16x8*)(lds + L_QF + 1024 + lane * 16);
                f32x4 z[5];
#pragma unroll
                for (int ct = 0; ct < 5; ++ct) { z[ct] = f32x4{0.f, 0.f, 0.f, 0.f}; z[ct] = SMFMAC(vf0, wo[ct][0], z[ct]); }
#pragma unroll
                for (int ct = 0; ct < 5; ++ct) z[ct] = SMFMAC(vf1, wo[ct][1], z[ct]);
                {
                    char* slot = slotp(s);
                    const u32x2 z01 = half_add(z[0], z[1]), z23 = half_add(z[2], z[3]);
                    auto sx = __builtin_amdgcn_permlane16_swap(z01.x, z23.x, false, false);
                    auto sy = __builtin_amdgcn_permlane16_swap(z01.y, z23.y, false, false);
                    st16_sc1(slot, zo4, u32x4{nosent(sx[0]), nosent(sy[0]), nosent(sx[1]), nosent(sy[1])});
                    const u32x2 z4 = half_add(z[4], z[4]);
                    if (lane < 32) st8_sc1(slot, zo1, u32x2{nosent(z4.x), nosent(z4.y)});
                }
                next_target(s);
            }
        }
    } else if (role < NREC) {
        const int ct = role - R_B;
        {
            const u32x4 sv = {~0u, ~0u, ~0u, ~0u};
            for (int q = ct; q < 186; q += NB) {
                const unsigned base = (unsigned)OFF_HO + (q < 176 ? (q / 44) * (unsigned)MB + (q % 44) * 16384 : (52 + q - 176) * 16384) + tid * 16;
                __builtin_amdgcn_raw_buffer_store_b128(sv, rs, base, 0, 16);
                __builtin_amdgcn_raw_buffer_store_b128(sv, rs, base, 8192, 16);
            }
            arrive(ctrl, C_FILL, tid);
        }
        if (tid == 0) *(volatile unsigned*)(lds + L_MISC + 68) = 0u;
        const unsigned pol = get_pol();
        auto slotp = [&](int s) -> char* { return ho + (size_t)(s & 3) * MB + (((pol >> ((s >> 1) & 1)) & 1) ? 8192 : 0); };
        if (wv < 2) {
            unsigned pox[8], pom[2];
#pragma unroll
            for (int i = 0; i < 8; ++i) pox[i] = lmap(SLOT_P + (4 * wv + (i >> 1)) * 2560 + (i & 1) * 1024 + lane * 16);
#pragma unroll
            for (int i = 0; i < 2; ++i) pom[i] = lmap(SLOT_P + (4 * wv + 2 * i + (n >> 3)) * 2560 + 2048 + g * 128 + (n & 7) * 16);
            const unsigned zo = (4 + ct) * 16384 + wv * 4096 + lane * 16;
            const unsigned mo_pub = lmap(SLOT_MEM + (2 * ct + (g >> 1)) * 128 + (n & 7) * 16 + (g & 1) * 8);
            const bool hi = n >= 8;
            unsigned vp = 0;
            u32x4 uf[16];
            auto load_u = [&](int s) {
                if (s == 0) wait_counters(ctrl, C_V0 + 16 * wv * 32, 16, 1, lane);
                else if (vp <= (unsigned)s) vp = wait_counters(ctrl, C_VP + 16 * wv * 32, 16, s + 1, lane);
                const unsigned src = (unsigned)(OFF_UF + ((size_t)s * NB + ct) * 32768) + wv * 16384 + lane * 16;
#pragma unroll
                for (int q = 0; q < 16; ++q) uf[q] = __builtin_amdgcn_raw_buffer_load_b128(rs, src, q * 1024, 16);
#pragma unroll
                for (int q = 0; q < 16; ++q) asm volatile("" : "+v"(uf[q]));
            };
            load_u(0);
            unsigned gbmin = 85;
            for (int s = 0; s < NSEG; ++s) {
                char* slot = slotp(s);
                u32x4 px[8], pm[2], zz[4];
                unsigned spins = 0, target = 0;
#define B_LOADZ() asm volatile("global_load_dwordx4 %0, %4, %5 sc1\n\tglobal_load_dwordx4 %1, %4, %5 offset:1024 sc1\n\tglobal_load_dwordx4 %2, %4, %5 offset:2048 sc1\n\tglobal_load_dwordx4 %3, %4, %5 offset:3072 sc1" \
                             : "=&v"(zz[0]), "=&v"(zz[1]), "=&v"(zz[2]), "=&v"(zz[3]) : "v"(zo), "s"(slot) : "memory")
#define B_LOADP() asm volatile("global_load_dwordx4 %8, %18, %20 sc1\n\tglobal_load_dwordx4 %9, %19, %20 sc1\n\t" \
                             "global_load_dwordx4 %0, %10, %20 sc1\n\tglobal_load_dwordx4 %1, %11, %20 sc1\n\tglobal_load_dwordx4 %2, %12, %20 sc1\n\tglobal_load_dwordx4 %3, %13, %20 sc1\n\t" \
                             "global_load_dwordx4 %4, %14, %20 sc1\n\tglobal_load_dwordx4 %5, %15, %20 sc1\n\tglobal_load_dwordx4 %6, %16, %20 sc1\n\tglobal_load_dwordx4 %7, %17, %20 sc1" \
                             : "=&v"(px[0]), "=&v"(px[1]), "=&v"(px[2]), "=&v"(px[3]), "=&v"(px[4]), "=&v"(px[5]), "=&v"(px[6]), "=&v"(px[7]), "=&v"(pm[0]), "=&v"(pm[1]) \
                             : "v"(pox[0]), "v"(pox[1]), "v"(pox[2]), "v"(pox[3]), "v"(pox[4]), "v"(pox[5]), "v"(pox[6]), "v"(pox[7]), "v"(pom[0]), "v"(pom[1]), "s"(slot) : "memory")
                if (s < NSEG - 1) {
                    {
                        unsigned spt = 0;
                        while (__builtin_amdgcn_readfirstlane(*(volatile unsigned*)(lds + L_MISC + 68)) != (unsigned)(s + 1)) { __builtin_amdgcn_s_sleep(1); if (++spt > (SPIN_CAP << 4)) break; }
                        target = __builtin_amdgcn_readfirstlane(*(volatile unsigned*)(lds + L_MISC + 64));
                    }
                    wait_rt(target - ZLEAD);
                    B_LOADZ();
                    wait_rt(target);
                    B_LOADP();
                    const f32x4 zero = {0.f, 0.f, 0.f, 0.f};
                    f32x4 xa, xb;
                    unsigned mx;
#define B_MXZ() umax3(umax3(umax3(zz[0].x, zz[0].y, zz[0].z), umax3(zz[0].w, zz[1].x, zz[1].y), umax3(zz[1].z, zz[1].w, zz[2].x)), \
                      umax3(umax3(zz[2].y, zz[2].z, zz[2].w), umax3(zz[3].x, zz[3].y, zz[3].z), umax3(zz[3].w, pm[0].x, pm[0].y)), \
                      umax3(umax3(pm[0].z, pm[0].w, pm[1].x), pm[1].y, max(pm[1].z, pm[1].w)))
#define B_MM() xa = MFMA32(BC8(zz[0]), BC8(pm[0]), zero); xb = MFMA32(BC8(zz[1]), BC8(pm[0]), zero); \
               xa = MFMA32(BC8(zz[2]), BC8(pm[1]), xa); xb = MFMA32(BC8(zz[3]), BC8(pm[1]), xb);
                    asm volatile("s_waitcnt vmcnt(8)" : "+v"(zz[0]), "+v"(zz[1]), "+v"(zz[2]), "+v"(zz[3]), "+v"(pm[0]), "+v"(pm[1]) :: "memory");
                    B_MM();
                    mx = B_MXZ();
                    __builtin_amdgcn_sched_barrier(0);
#define B_STEP(i, cnt) asm volatile("s_waitcnt vmcnt(" #cnt ")" : "+v"(px[i]) :: "memory"); \
                    xa = MFMA32(BC8(uf[2 * i]), BC8(px[i]), xa); xb = MFMA32(BC8(uf[2 * i + 1]), BC8(px[i]), xb); \
                    mx = umax3(mx, umax3(px[i].x, px[i].y, px[i].z), px[i].w); __builtin_amdgcn_sched_barrier(0);
                    B_STEP(0, 7) B_STEP(1, 6) B_STEP(2, 5) B_STEP(3, 4) B_STEP(4, 3) B_STEP(5, 2) B_STEP(6, 1) B_STEP(7, 0)
                    if (!__all(mx != ~0u)) {
                        const unsigned sb = (unsigned)(slot - a.ws);
                        do {
                            if (spin_abort(spins, ctrl)) break;
                            asm volatile("" ::: "memory");
#pragma unroll
                            for (int q = 0; q < 4; ++q) zz[q] = __builtin_amdgcn_raw_buffer_load_b128(rs, zo + q * 1024, sb, 16);
#pragma unroll
                            for (int q = 0; q < 2; ++q) pm[q] = __builtin_amdgcn_raw_buffer_load_b128(rs, pom[q], sb, 16);
#pragma unroll
                            for (int q = 0; q < 8; ++q) px[q] = __builtin_amdgcn_raw_buffer_load_b128(rs, pox[q], sb, 16);
                            mx = B_MXZ();
#pragma unroll
                            for (int q = 0; q < 8; ++q) mx = umax3(mx, umax3(px[q].x, px[q].y, px[q].z), px[q].w);
                        } while (!__all(mx != ~0u));
                        B_MM();
#pragma unroll
                        for (int q = 0; q < 8; ++q) { xa = MFMA32(BC8(uf[2 * q]), BC8(px[q]), xa); xb = MFMA32(BC8(uf[2 * q + 1]), BC8(px[q]), xb); }
                    }
                    f32x4 tot;
#pragma unroll
                    for (int r = 0; r < 4; ++r) {
                        const float y = hi ? xb[r] : xa[r];
                        tot[r] = y + u2f(__builtin_amdgcn_update_dpp(0, (int)f2u(y), 0x128, 0xf, 0xf, false));
                    }
                    if (wv == 1) *(f32x4*)(lds + L_RED + lane * 16) = tot;
                    BARRIER();
                    if (wv == 0) {
                        tot += *(const f32x4*)(lds + L_RED + lane * 16);
                        const u32x2 o = {nosent(pk_bf16(tot[0], tot[1])), nosent(pk_bf16(tot[2], tot[3]))};
                        if (!hi) st8_sc1(slot, mo_pub, o);
                        if (ct == 0) {
                            const unsigned gb = rt32() - target;
                            gbmin = gb < gbmin ? gb : gbmin;
                            if (lane == 0) { st4_sc1(slot + so_b, 0u, gbmin); if (s >= 2) st4_sc1(slotp(s + 2) + so_b, 0u, ~0u); }
                        }
                        if (s >= 2 && !hi) st8_sc1(slotp(s + 2), mo_pub, u32x2{~0u, ~0u});
                    }
                    asm volatile("s_barrier" ::: "memory");
                    if (wv == 1 && lane == 0 && s > 0) set_counter(ctrl, C_ZP + ct * 32, s);
                } else {
                    for (;;) {
                        asm volatile("" ::: "memory");
#pragma unroll
                        for (int q = 0; q < 4; ++q) zz[q] = __builtin_amdgcn_raw_buffer_load_b128(rs, zo + q * 1024, (unsigned)(slot - a.ws), 16);
                        const unsigned mx = umax3(umax3(umax3(zz[0].x, zz[0].y, zz[0].z), umax3(zz[0].w, zz[1].x, zz[1].y), umax3(zz[1].z, zz[1].w, zz[2].x)),
                                                  umax3(umax3(zz[2].y, zz[2].z, zz[2].w), umax3(zz[3].x, zz[3].y, zz[3].z), zz[3].w), 0u);
                        if (__all(mx != ~0u)) break;
                        __builtin_amdgcn_s_sleep(2);
                        if (spin_abort(spins, ctrl)) break;
                    }
                }
                if (s < NSEG - 1) {
                    const char* zc = a.ws + OFF_ZF + ((size_t)s * NB + ct) * 8192 + wv * 4096;
#pragma unroll
                    for (int q = 0; q < 4; ++q) st16_sc1(zc + q * 1024, lane * 16, zz[q]);
                } else {
#pragma unroll
                    for (int q = 0; q < 4; ++q) *(u32x4*)(lds + L_RED + (wv * 4 + q) * 1024 + lane * 16) = zz[q];
                }
                if (s < NSEG - 2) {
                    asm volatile("s_barrier" ::: "memory");
#pragma unroll
                    for (int q = 0; q < 16; ++q) uf[q] = *(const u32x4*)(lds + L_UB + (s & 1) * 32768 + (wv * 16 + q) * 1024 + lane * 16);
#pragma unroll
                    for (int q = 0; q < 16; ++q) asm volatile("" : "+v"(uf[q]));
                } else if (s == NSEG - 2) {
                    asm volatile("s_waitcnt vmcnt(0)\n\ts_barrier" ::: "memory");
                    if (wv == 1 && lane == 0) set_counter(ctrl, C_ZP + ct * 32, NSEG - 1);
                }
            }
            asm volatile("s_waitcnt vmcnt(0)" ::: "memory");
        } else {
            unsigned vp = 0;
            for (int s = 0; s < NSEG - 1; ++s) {
                if (wv == 2) {
                    const unsigned t = read_sig(slotp(s) + so_a, ctrl) + DBC;
                    if (lane == 0) { *(volatile unsigned*)(lds + L_MISC + 64) = t; LDSWAIT(); *(volatile unsigned*)(lds + L_MISC + 68) = (unsigned)(s + 1); }
                }
                const int sn = s + 1;
                const char* usrc = a.ws + OFF_UF + ((size_t)sn * NB + ct) * 32768 + (wv - 4) * 8192 + lane * 16;
                bool early = false;
                if (wv >= 4) {
                    if (vp <= (unsigned)sn) vp = wait_counters(ctrl, C_VP + 8 * (wv - 4) * 32, 8, 0, lane);
                    early = vp > (unsigned)sn;
                    if (early) {
                        unsigned spt = 0;
                        while (__builtin_amdgcn_readfirstlane(*(volatile unsigned*)(lds + L_MISC + 68)) != (unsigned)(s + 1)) { __builtin_amdgcn_s_sleep(1); if (++spt > (SPIN_CAP << 4)) break; }
                        wait_rt(__builtin_amdgcn_readfirstlane(*(volatile unsigned*)(lds + L_MISC + 64)) + 8);
#pragma unroll
                        for (int q = 0; q < 8; ++q) glds16(usrc + q * 1024, L_UB + (s & 1) * 32768 + (wv - 4) * 8192 + q * 1024);
                    }
                }
                asm volatile("s_waitcnt lgkmcnt(0)\n\ts_barrier" ::: "memory");
                asm volatile("s_barrier" ::: "memory");
                if (wv >= 4) {
                    const unsigned o = (4 + ct) * 16384 + (wv - 4) * 2048 + lane * 16;
                    st16_sc1(slotp(s), o, u32x4{~0u, ~0u, ~0u, ~0u}); st16_sc1(slotp(s) + 1024, o, u32x4{~0u, ~0u, ~0u, ~0u});
                    if (!early) {
                        vp = wait_counters(ctrl, C_VP + 8 * (wv - 4) * 32, 8, sn + 1, lane);
#pragma unroll
                        for (int q = 0; q < 8; ++q) glds16(usrc + q * 1024, L_UB + (s & 1) * 32768 + (wv - 4) * 8192 + q * 1024);
                    }
                }
                asm volatile("s_waitcnt vmcnt(0)\n\ts_barrier" ::: "memory");
            }
        }
        __syncthreads();
        if (tid == 0) set_counter(ctrl, C_ZP + ct * 32, NSEG);
        if (wv < 4) {
            const int row = 32 * (NSEG - 1) + 8 * wv + (n & 7);
            wait_counters(ctrl, C_OB + 2 * 32, 1, NO, lane);
            const u32x2 av = __builtin_amdgcn_raw_buffer_load_b64(rs, (unsigned)(OFF_A + ((size_t)row * D_ + 16 * ct + 4 * g) * 2), 0, 16);
            const float gv = u2f(__builtin_amdgcn_raw_buffer_load_b32(rs, (unsigned)(OFF_G + row * 4), 0, 16));
            const unsigned sb = (unsigned)(slotp(0) - a.ws);
            u32x4 px[16], pm[4];
            unsigned spins = 0;
            for (;;) {
                asm volatile("" ::: "memory");
#pragma unroll
                for (int q = 0; q < 16; ++q) px[q] = __builtin_amdgcn_raw_buffer_load_b128(rs, lmap(SLOT_PXL + wv * 20480 + (q >> 1) * 2560 + (q & 1) * 1024 + lane * 16), sb, 16);
#pragma unroll
                for (int q = 0; q < 4; ++q) pm[q] = __builtin_amdgcn_raw_buffer_load_b128(rs, lmap(SLOT_PXL + wv * 20480 + (2 * q + (n >> 3)) * 2560 + 2048 + g * 128 + (n & 7) * 16), sb, 16);
                unsigned mx = 0;
#pragma unroll
                for (int q = 0; q < 16; ++q) mx = umax3(mx, umax3(px[q].x, px[q].y, px[q].z), px[q].w);
#pragma unroll
                for (int q = 0; q < 4; ++q) mx = umax3(mx, umax3(pm[q].x, pm[q].y, pm[q].z), pm[q].w);
                if (__all(mx != ~0u)) break;
                __builtin_amdgcn_s_sleep(4);
                if (spin_abort(spins, ctrl)) break;
            }
            const char* ub = lds + L_UB + ((NSEG - 2) & 1) * 32768 + lane * 16;
            f32x4 xa = {0.f, 0.f, 0.f, 0.f}, xb = xa;
#pragma unroll
            for (int q = 0; q < 16; ++q) {
                xa = MFMA32(*(const bf16x8*)(ub + (2 * q) * 1024), BC8(px[q]), xa);
                xb = MFMA32(*(const bf16x8*)(ub + (2 * q + 1) * 1024), BC8(px[q]), xb);
            }
#pragma unroll
            for (int q = 0; q < 4; ++q) {
                xa = MFMA32(*(const bf16x8*)(lds + L_RED + (2 * q) * 1024 + lane * 16), BC8(pm[q]), xa);
                xb = MFMA32(*(const bf16x8*)(lds + L_RED + (2 * q + 1) * 1024 + lane * 16), BC8(pm[q]), xb);
            }
            float tot[4];
#pragma unroll
            for (int r = 0; r < 4; ++r) {
                const float y = n >= 8 ? xb[r] : xa[r];
                tot[r] = y + u2f(__builtin_amdgcn_update_dpp(0, (int)f2u(y), 0x128, 0xf, 0xf, false));
            }
            const float og = rbf(1.f - gv);
            auto mix = [&](unsigned aw, float x0, float x1) -> unsigned {
                return pk_bf16(rbf(gv * u2f(aw << 16)) + rbf(og * rbf(x0)), rbf(gv * u2f(aw & 0xFFFF0000u)) + rbf(og * rbf(x1)));
            };
            if (n < 8) *(u32x2*)(a.out + (size_t)row * D_ + 16 * ct + 4 * g) = u32x2{mix(av.x, tot[0], tot[1]), mix(av.y, tot[2], tot[3])};
        }
    } else if (role < R_V) {
        const int h = role - R_P;
        bf16x16 wq[10], wk[10];
        load_w2(a.Wq, a.Wk, h, lds, tid, wv, wq, wk);
        if (wv == 0) wait_counters(ctrl, C_XF, 1, NO, lane);
        __syncthreads();
        prefetch_x(xf, 1, 0, wv, lane);
        prefetch_x(xf, 2, 1, wv, lane);
        int b3 = 0;
        for (int s = 1; s < NSEG; ++s) {
            if (s < 4) asm volatile("s_waitcnt vmcnt(0)" ::: "memory");
            BARRIER();
            if (s > 1 && s < 4 && tid == 0) set_counter(ctrl, C_PP + h * 32, s);
            proj_qk<true>(lds + L_XB + b3 * 40960 + lane * 16, wq, wk, lds + L_T + tw_off + g * 64, lds + L_T + 8192 + tw_off + g * 64,
                          xf + (size_t)min(s + 2, NSEG - 1) * 40960 + wv * 5120 + lane * 16, L_XB + (b3 == 0 ? 2 : b3 - 1) * 40960 + wv * 5120);
            b3 = b3 == 2 ? 0 : b3 + 1;
            asm volatile("s_waitcnt vmcnt(5)" ::: "memory");
            BARRIER();
            if (tid == 0) set_counter(ctrl, C_PP + h * 32, s);
            const u32x4 vq = *(const u32x4*)(lds + L_T + tid * 16), vk = *(const u32x4*)(lds + L_T + 8192 + tid * 16);
            st16_sc1(a.ws + OFF_KXF + ((size_t)s * H_ + h) * 8192, tid * 16, vk);
            st16_sc1(a.ws + OFF_QXF + ((size_t)s * H_ + h) * 8192, tid * 16, vq);
        }
        asm volatile("s_waitcnt vmcnt(0)" ::: "memory");
        __syncthreads();
        if (tid == 0) set_counter(ctrl, C_PP + h * 32, NSEG);
    } else if (role < R_O) {
        const int h = role - R_V;
        bf16x16 wvv[10], wo[5][2];
        load_wvo(a.Wv, a.Wo, h, lds, tid, wv, wvv, wo);
        if (wv == 0) wait_counters(ctrl, C_XF, 1, NO, lane);
        __syncthreads();
        prefetch_x(xf, 1, 0, wv, lane);
        prefetch_x(xf, 2, 1, wv, lane);
        int b3 = 0;
        for (int s = 1; s < NSEG; ++s) {
            if (s < 4) asm volatile("s_waitcnt vmcnt(0)" ::: "memory");
            BARRIER();
            if (s > 1 && s < 4 && tid == 0) set_counter(ctrl, C_VP + h * 32, s);
            proj_u<true>(lds, lds + L_XB + b3 * 40960 + lane * 16, wvv, wo, wv, lane, a.ws + OFF_UF + (size_t)s * NB * 32768, h,
                         xf + (size_t)min(s + 2, NSEG - 1) * 40960 + wv * 5120 + lane * 16, L_XB + (b3 == 0 ? 2 : b3 - 1) * 40960 + wv * 5120, ctrl + C_VP + h * 32, s);
            b3 = b3 == 2 ? 0 : b3 + 1;
        }
        asm volatile("s_waitcnt vmcnt(0)" ::: "memory");
        __syncthreads();
        if (tid == 0) set_counter(ctrl, C_VP + h * 32, NSEG);
    } else {
        const int o = role - R_O;
        {
#pragma unroll
            for (int i = 0; i < 5; ++i) {
                const int id = i * NT + tid, l = id & 63, tt = (id >> 6) & 1, hf = (id >> 7) & 1, kb = id >> 8;
                const int ga = l >> 4, tok = 32 * o + 16 * tt + (l & 15), dbase = 64 * kb + 16 * (2 * hf + (ga >> 1)) + 4 * (ga & 1);
                const u32x2 c0 = *(const u32x2*)(a.x + (size_t)tok * D_ + dbase), c1 = *(const u32x2*)(a.x + (size_t)tok * D_ + dbase + 8);
                u32x4 ov = {c0.x, c1.x, c0.y, c1.y};
                __builtin_amdgcn_raw_buffer_store_b128(ov, rs, (unsigned)OFF_XF + o * 40960 + id * 16, 0, 16);
            }
            arrive(ctrl, C_XF, tid);
        }
        auto obar = [&](int idx) {
            asm volatile("s_waitcnt vmcnt(0)" ::: "memory");
            __syncthreads();
            if (wv == 0) {
                if (lane == 0) __hip_atomic_fetch_add(ctrl + C_OB + idx * 32, 1u, __ATOMIC_RELAXED, __HIP_MEMORY_SCOPE_AGENT);
                wait_counters(ctrl, C_OB + idx * 32, 1, idx == 0 ? NO + NS : NO, lane);
            }
            __syncthreads();
        };
        auto bf2f = [](unsigned short u) -> float { return u2f((unsigned)u << 16); };
        c32* tw = (c32*)(lds + 96 * 1024);
        c32* wbuf = (c32*)(lds) + wv * 720;
        for (int i = tid; i < 640; i += NT) { float sn, cs_; sincospif(-(float)i / 320.f, &sn, &cs_); tw[i] = c32{cs_, sn}; }
        float gwv[10];
#pragma unroll
        for (int j = 0; j < 10; ++j) gwv[j] = bf2f(a.gw[64 * j + lane]);
        __syncthreads();
        for (int task = o; task < 128; task += NO) {
            {
                const int r0 = task * 16 + wv * 2;
                c32 v[10]; float ga = 0.f, gb_ = 0.f;
#pragma unroll
                for (int j = 0; j < 10; ++j) {
                    v[j] = c32{bf2f(a.x[(size_t)r0 * D_ + 64 * j + lane]), bf2f(a.x[(size_t)(r0 + 1) * D_ + 64 * j + lane])};
                    ga += rbf(v[j].x * gwv[j]); gb_ += rbf(v[j].y * gwv[j]);
                }
#pragma unroll
                for (int of = 32; of >= 1; of >>= 1) { ga += __shfl_xor(ga, of); gb_ += __shfl_xor(gb_, of); }
                if (lane < 2) {
                    const float z = rbf(lane ? gb_ : ga);
                    const float gv = rbf(1.f / (1.f + __expf(-z)));
                    __builtin_amdgcn_raw_buffer_store_b32(f2u(gv), rs, (unsigned)(OFF_G + (r0 + lane) * 4), 0, 16);
                }
                fft640(v, wbuf, tw, lane);
                __syncthreads();
                for (int i = tid; i < 321 * 16; i += NT) {
                    const int k = i >> 4, t = i & 15;
                    const c32* zb = (const c32*)lds + (t >> 1) * 720;
                    const c32 z1 = zb[k], z2 = zb[k ? 640 - k : 0];
                    c32 r = (t & 1) ? c32{0.5f * (z1.y + z2.y), 0.5f * (z2.x - z1.x)} : c32{0.5f * (z1.x + z2.x), 0.5f * (z1.y - z2.y)};
                    __builtin_amdgcn_raw_buffer_store_b64(__builtin_bit_cast(u32x2, r), rs, (unsigned)(OFF_X1T + ((size_t)k * S_ + task * 16 + t) * 8), 0, 16);
                }
                __syncthreads();
            }
        }
        for (int i = tid; i < 2048; i += NT) { float sn, cs_; sincospif(-(float)i / 1024.f, &sn, &cs_); tw[i] = c32{cs_, sn}; }
        obar(0);
        {
            const int j = tid & 255, hf = tid >> 8;
            c32* buf = (c32*)lds + hf * 2304;
            auto pd = [](int p) { return p + ((p >> 5) << 2); };
            for (int b = o; b < 161; b += NO) {
                const int col = min(2 * b + hf, 320);
                const unsigned cb = (unsigned)(OFF_X1T + (size_t)col * S_ * 8);
                c32 v[8];
#pragma unroll
                for (int q = 0; q < 8; ++q) v[q] = __builtin_bit_cast(c32, __builtin_amdgcn_raw_buffer_load_b64(rs, cb + (q * 256 + j) * 8, 0, 16));
                const u32x4 wa = __builtin_amdgcn_raw_buffer_load_b128(rs, (unsigned)(OFF_WT + ((size_t)col * S_ + 8 * j) * 4), 0, 16);
                const u32x4 wb = __builtin_amdgcn_raw_buffer_load_b128(rs, (unsigned)(OFF_WT + ((size_t)col * S_ + 8 * j + 4) * 4), 0, 16);
                dft8<-1>(v);
#pragma unroll
                for (int q = 0; q < 8; ++q) buf[pd(q * 256 + j)] = q ? cmul(v[q], tw[j * q]) : v[q];
                __syncthreads();
                const int b2 = (j >> 5) * 256 + (j & 31), lo = j & 31;
#pragma unroll
                for (int q = 0; q < 8; ++q) v[q] = buf[pd(b2 + q * 32)];
                dft8<-1>(v);
#pragma unroll
                for (int q = 0; q < 8; ++q) buf[pd(b2 + q * 32)] = q ? cmul(v[q], tw[8 * lo * q]) : v[q];
                __syncthreads();
                const int b3 = (j >> 2) * 32 + (j & 3), n0 = j & 3;
#pragma unroll
                for (int q = 0; q < 8; ++q) v[q] = buf[pd(b3 + q * 4)];
                dft8<-1>(v);
#pragma unroll
                for (int q = 0; q < 8; ++q) buf[pd(b3 + q * 4)] = q ? cmul(v[q], tw[64 * n0 * q]) : v[q];
                __syncthreads();
#pragma unroll
                for (int q = 0; q < 8; ++q) v[q] = buf[pd(8 * j + q)];
                dft4<-1>(v[0], v[1], v[2], v[3]); dft4<-1>(v[4], v[5], v[6], v[7]);
                const unsigned wv8[8] = {wa.x, wa.y, wa.z, wa.w, wb.x, wb.y, wb.z, wb.w};
#pragma unroll
                for (int q = 0; q < 8; ++q) v[q] = cmul(v[q], c32{u2f(wv8[q] << 16), u2f(wv8[q] & 0xFFFF0000u)});
                dft4<1>(v[0], v[1], v[2], v[3]); dft4<1>(v[4], v[5], v[6], v[7]);
#pragma unroll
                for (int q = 0; q < 8; ++q) buf[pd(8 * j + q)] = v[q];
                __syncthreads();
#pragma unroll
                for (int q = 0; q < 8; ++q) { c32 t = buf[pd(b3 + q * 4)]; v[q] = q ? cmulc(t, tw[64 * n0 * q]) : t; }
                dft8<1>(v);
#pragma unroll
                for (int q = 0; q < 8; ++q) buf[pd(b3 + q * 4)] = v[q];
                __syncthreads();
#pragma unroll
                for (int q = 0; q < 8; ++q) { c32 t = buf[pd(b2 + q * 32)]; v[q] = q ? cmulc(t, tw[8 * lo * q]) : t; }
                dft8<1>(v);
#pragma unroll
                for (int q = 0; q < 8; ++q) buf[pd(b2 + q * 32)] = v[q];
                __syncthreads();
#pragma unroll
                for (int q = 0; q < 8; ++q) { c32 t = buf[pd(q * 256 + j)]; v[q] = q ? cmulc(t, tw[j * q]) : t; }
                dft8<1>(v);
                if (2 * b + hf <= 320) {
#pragma unroll
                    for (int q = 0; q < 8; ++q) __builtin_amdgcn_raw_buffer_store_b64(__builtin_bit_cast(u32x2, v[q]), rs, cb + (q * 256 + j) * 8, 0, 16);
                }
                __syncthreads();
            }
        }
        for (int i = tid; i < 640; i += NT) { float sn, cs_; sincospif(-(float)i / 320.f, &sn, &cs_); tw[i] = c32{cs_, sn}; }
        obar(1);
        for (int task = o; task < 128; task += NO) {
            for (int i = tid; i < 321 * 8; i += NT) {
                const int k = i >> 3, p = i & 7;
                u32x4 y = __builtin_amdgcn_raw_buffer_load_b128(rs, (unsigned)(OFF_X1T + ((size_t)k * S_ + task * 16 + 2 * p) * 8), 0, 16);
                float ar = u2f(y.x), ai = u2f(y.y), br = u2f(y.z), bi = u2f(y.w);
                if (k == 0 || k == 320) { ai = 0.f; bi = 0.f; }
                c32* zb = (c32*)lds + p * 720;
                zb[k] = c32{ar - bi, -(ai + br)};
                if (k > 0 && k < 320) zb[640 - k] = c32{ar + bi, -(br - ai)};
            }
            __syncthreads();
            c32 v[10];
#pragma unroll
            for (int j = 0; j < 10; ++j) v[j] = wbuf[64 * j + lane];
            LDSWAIT();
            fft640(v, wbuf, tw, lane);
            const float sc = 1.f / (2048.f * 640.f);
            const int r0 = task * 16 + wv * 2;
            for (int i = lane; i < 320; i += 64) {
                const c32 z0 = wbuf[2 * i], z1 = wbuf[2 * i + 1];
                __builtin_amdgcn_raw_buffer_store_b32(pk_bf16(z0.x * sc, z1.x * sc), rs, (unsigned)(OFF_A + ((size_t)r0 * D_ + 2 * i) * 2), 0, 16);
                __builtin_amdgcn_raw_buffer_store_b32(pk_bf16(-z0.y * sc, -z1.y * sc), rs, (unsigned)(OFF_A + ((size_t)(r0 + 1) * D_ + 2 * i) * 2), 0, 16);
            }
            __syncthreads();
        }
        obar(2);
        if (o < 60) {
            const int grp = o / 5, c = o - grp * 5;
            const int ct = 8 * c + wv;
            const bool hi = n >= 8;
            unsigned vp = 0, zp = 0, ap = 0;
            for (int it = 0; it < 6; ++it) {
                int s = grp + 12 * it, qlo = 0, qhi = 4;
                if (it == 5) {
                    if (grp < 1) s = 60;
                    else if (grp < 3) { s = 61; qlo = 2 * (grp - 1); qhi = qlo + 2; }
                    else if (grp < 7) { s = 62; qlo = grp - 3; qhi = qlo + 1; }
                    else break;
                }
                if (s == 0) wait_counters(ctrl, C_V0, 32, 1, lane);
                else if (vp <= (unsigned)s) vp = wait_counters(ctrl, C_VP, 32, s + 1, lane);
                u32x4 uf[32];
#pragma unroll
                for (int i = 0; i < 32; ++i) {
                    uf[i] = __builtin_amdgcn_raw_buffer_load_b128(rs, (unsigned)(OFF_UF + ((size_t)s * NB + ct) * 32768) + lane * 16, i * 1024, 16);
                    if ((i & 3) == 3) { __builtin_amdgcn_sched_barrier(0); __builtin_amdgcn_s_sleep(24); __builtin_amdgcn_sched_barrier(0); }
                }
                const int et = tid >> 4, c8 = (tid & 15) * 8, row = 32 * s + et;
                const u32x4 av = __builtin_amdgcn_raw_buffer_load_b128(rs, (unsigned)(OFF_A + ((size_t)row * D_ + 128 * c + c8) * 2), 0, 16);
                const float gv = u2f(__builtin_amdgcn_raw_buffer_load_b32(rs, (unsigned)(OFF_G + row * 4), 0, 16));
                if (zp <= (unsigned)s) zp = wait_counters(ctrl, C_ZP + ct * 32, 1, s + 1, lane);
                u32x4 zq[8];
#pragma unroll
                for (int i = 0; i < 8; ++i) zq[i] = __builtin_amdgcn_raw_buffer_load_b128(rs, (unsigned)(OFF_ZF + ((size_t)s * NB + ct) * 8192) + lane * 16, i * 1024, 16);
                if (ap <= (unsigned)s) ap = wait_counters(ctrl, C_AP, 32, s + 1, lane);
                for (int c0 = qlo * 1280; c0 < qhi * 1280; c0 += 2560) {
                    u32x4 t[5];
#pragma unroll
                    for (int i = 0; i < 5; ++i) t[i] = __builtin_amdgcn_raw_buffer_load_b128(rs, (unsigned)(OFF_PXF + (size_t)s * 81920) + min(c0 + i * NT + tid, 5119) * 16, 0, 16);
#pragma unroll
                    for (int i = 0; i < 5; ++i) *(u32x4*)(lds + min(c0 + i * NT + tid, 5119) * 16) = t[i];
                }
                __syncthreads();
                float* tile = (float*)(lds + 81920);
                const unsigned pmo = 2048 + g * 128 + (n & 7) * 16;
#pragma unroll
                for (int qo = 0; qo < 4; ++qo) {
                    if (qo < qlo || qo >= qhi) continue;
                    f32x4 xa = {0.f, 0.f, 0.f, 0.f}, xb = xa, ma = xa;
#pragma unroll
                    for (int wg = 0; wg < 8; ++wg) {
                        const char* pbp = lds + qo * 20480 + wg * 2560;
                        const bf16x8 p0 = *(const bf16x8*)(pbp + lane * 16), p1 = *(const bf16x8*)(pbp + 1024 + lane * 16);
                        const bf16x8 p2 = *(const bf16x8*)(pbp + pmo);
                        xa = MFMA32(BC8(uf[4 * wg]), p0, xa); xb = MFMA32(BC8(uf[4 * wg + 1]), p0, xb);
                        xa = MFMA32(BC8(uf[4 * wg + 2]), p1, xa); xb = MFMA32(BC8(uf[4 * wg + 3]), p1, xb);
                        ma = MFMA32(BC8(zq[wg]), p2, ma);
                    }
                    f32x4 tot;
#pragma unroll
                    for (int r = 0; r < 4; ++r) {
                        const float y = hi ? xb[r] : xa[r] + ma[r];
                        tot[r] = y + u2f(__builtin_amdgcn_update_dpp(0, (int)f2u(y), 0x128, 0xf, 0xf, false));
                    }
                    if (n < 8) *(f32x4*)(tile + (8 * qo + n) * 132 + wv * 16 + 4 * g) = tot;
                }
                __syncthreads();
                if (et >= 8 * qlo && et < 8 * qhi) {
                    const f32x4 b0 = *(const f32x4*)(tile + et * 132 + c8), b1 = *(const f32x4*)(tile + et * 132 + c8 + 4);
                    const float og = rbf(1.f - gv);
                    auto mix = [&](unsigned aw, float x0, float x1) -> unsigned {
                        return pk_bf16(rbf(gv * u2f(aw << 16)) + rbf(og * rbf(x0)), rbf(gv * u2f(aw & 0xFFFF0000u)) + rbf(og * rbf(x1)));
                    };
                    u32x4 ov = {mix(av.x, b0[0], b0[1]), mix(av.y, b0[2], b0[3]), mix(av.z, b1[0], b1[1]), mix(av.w, b1[2], b1[3])};
                    *(u32x4*)(a.out + (size_t)row * D_ + 128 * c + c8) = ov;
                }
                __syncthreads();
            }
        }
    }
}

extern "C" void kernel_launch(void* const* d_in, const int* in_sizes, int n_in, void* d_out, int out_size, void* d_ws, size_t ws_size, hipStream_t stream) {
    char* ws = (char*)(((uintptr_t)d_ws + 2 * MB - 1) & ~(uintptr_t)(2 * MB - 1));
    (void)hipMemsetAsync(ws + OFF_CTRL, 0, CTRL_WORDS * 4, stream);
    MainArgs a;
    a.x = (const u16*)d_in[0]; a.mem0 = (const u16*)d_in[4]; a.Wq = (const u16*)d_in[5]; a.Wk = (const u16*)d_in[6]; a.Wv = (const u16*)d_in[7]; a.Wo = (const u16*)d_in[8];
    a.wr = (const u16*)d_in[1]; a.wi = (const u16*)d_in[2]; a.gw = (const u16*)d_in[10]; a.ws = ws; a.out = (u16*)d_out;
    (void)hipFuncSetAttribute((const void*)k_main, hipFuncAttributeMaxDynamicSharedMemorySize, LDS_BYTES);
    k_main<<<256, NT, LDS_BYTES, stream>>>(a);
}
```
